```python
import torch

def _softmax(x, axis=-1):
    return torch.softmax(x, dim=axis)

def _gelu(x):
    return torch.nn.functional.gelu(x, approximate='tanh')

def _silu(x):
    return torch.nn.functional.silu(x)

def _sigmoid(x):
    return torch.sigmoid(x)

def _layernorm(x, axis=-1, eps=1e-05):
    m = x.mean(dim=axis, keepdim=True)
    v = x.var(dim=axis, keepdim=True, unbiased=False)
    return (x - m) / torch.sqrt(v + eps)

def _scatter_add(tgt, idx, src, axis=0):
    return tgt.clone().index_add_(axis, idx, src)

def _segment_sum(src, seg, n):
    out = torch.zeros((n,) + src.shape[1:], dtype=src.dtype)
    return out.index_add_(0, seg, src)

def _pad(x, pw):
    flat = tuple((v for pair in reversed(pw) for v in pair))
    return torch.nn.functional.pad(x, flat)
kernel_name = 'gated_mix__compressed_kv_cla__memory_aug_recurrent_mem_tr__prompt'

def setup_inputs():

    _w = lambda a: (lambda t: t.to(torch.bfloat16) if t.is_floating_point() else t)(torch.as_tensor(a))

    def _A_setup():
        B, H, Sq, Sk, D, M = (1, 32, 2048, 2048, 128, 512)
        Dc = 4 * D
        return {'x': _w((lambda *s: torch.randn(*s, dtype=torch.bfloat16) * 0.05)(B, Sq, M)), 'c_kv': _w((lambda *s: torch.randn(*s, dtype=torch.bfloat16) * 0.8)(B, Sk, Dc)), 'w_q': _w((lambda *s: torch.randn(*s, dtype=torch.bfloat16) * 6.4)(M, H, D) * M ** (-0.5)), 'w_uk': _w((lambda *s: torch.randn(*s, dtype=torch.bfloat16) * 6.4)(Dc, H, D) * Dc ** (-0.5)), 'w_uv': _w((lambda *s: torch.randn(*s, dtype=torch.bfloat16) * 0.8)(Dc, H, D) * Dc ** (-0.5)), 'w_o': _w((lambda *s: torch.randn(*s, dtype=torch.bfloat16) * 0.8)(H, D, M) * (H * D) ** (-0.5))}
    _A_in = _A_setup()

    def _B_setup():
        B, S, Dm = (1, 2048, 512)
        H, Dh = (32, 128)
        M, seg = (8, 32)
        seg = min(seg, S)
        S = S // seg * seg
        return {'x': _w((lambda *s: torch.randn(*s, dtype=torch.bfloat16) * 0.05)(B, S, Dm)), 'mem0': _w((lambda *s: torch.randn(*s, dtype=torch.bfloat16) * 0.8)(B, M, Dm)), 'Wq': _w((lambda *s: torch.randn(*s, dtype=torch.bfloat16) * 3.2)(Dm, H, Dh) * Dm ** (-0.5)), 'Wk': _w((lambda *s: torch.randn(*s, dtype=torch.bfloat16) * 3.2)(Dm, H, Dh) * Dm ** (-0.5)), 'Wv': _w((lambda *s: torch.randn(*s, dtype=torch.bfloat16) * 0.8)(Dm, H, Dh) * Dm ** (-0.5)), 'Wo': _w((lambda *s: torch.randn(*s, dtype=torch.bfloat16) * 0.8)(H, Dh, Dm) * (H * Dh) ** (-0.5)), 'seg_len': seg}
    _B_in = _B_setup()
    return {'A_x': _A_in['x'], 'A_c_kv': _A_in['c_kv'], 'A_w_q': _A_in['w_q'], 'A_w_uk': _A_in['w_uk'], 'A_w_uv': _A_in['w_uv'], 'A_w_o': _A_in['w_o'], 'B_x': _B_in['x'], 'B_mem0': _B_in['mem0'], 'B_Wq': _B_in['Wq'], 'B_Wk': _B_in['Wk'], 'B_Wv': _B_in['Wv'], 'B_Wo': _B_in['Wo'], 'B_seg_len': _B_in['seg_len'], 'gate_w': _w((lambda *s: torch.randn(*s, dtype=torch.bfloat16) * 0.05)(512) * 16.0)}

def reference(A_x, A_c_kv, A_w_q, A_w_uk, A_w_uv, A_w_o, B_x, B_mem0, B_Wq, B_Wk, B_Wv, B_Wo, B_seg_len, gate_w):

    def _A_ref(x, c_kv, w_q, w_uk, w_uv, w_o):
        B, Sq, M = (x.shape[0], x.shape[1], x.shape[2])
        Sk = c_kv.shape[1]
        H, D = (w_q.shape[1], w_q.shape[2])
        q = torch.einsum('bqm,mhd->bhqd', x, w_q)
        k = torch.einsum('bkc,chd->bhkd', c_kv, w_uk)
        v = torch.einsum('bkc,chd->bhkd', c_kv, w_uv)
        scores = torch.einsum('bhqd,bhkd->bhqk', q, k) * D ** (-0.5)
        off = Sk - Sq
        qi = torch.arange(Sq)[:, None]
        kj = torch.arange(Sk)[None, :]
        cmask = kj <= qi + off
        scores = torch.where(cmask[None, None], scores, -1000000000.0)
        p = _softmax(scores, axis=-1)
        ctx = torch.einsum('bhqk,bhkd->bhqd', p, v)
        return torch.einsum('bhqd,hdm->bqm', ctx, w_o)

    def _B_ref(x, mem0, Wq, Wk, Wv, Wo, seg_len):
        B, S, Dm = x.shape
        H, Dh = (Wq.shape[1], Wq.shape[2])
        M = mem0.shape[1]
        scale = Dh ** (-0.5)
        n_seg = S // seg_len
        L = M + seg_len
        idx = torch.arange(L)
        mask = (idx[:, None] < M) | (idx[:, None] >= idx[None, :])
        mem = mem0
        outs = []
        for s in range(n_seg):
            xs = x[:, s * seg_len:(s + 1) * seg_len, :].clone()
            h = torch.concatenate([mem, xs], axis=1)
            q = torch.einsum('btd,dhe->bhte', h, Wq)
            k = torch.einsum('btd,dhe->bhte', h, Wk)
            v = torch.einsum('btd,dhe->bhte', h, Wv)
            scores = torch.einsum('bhqe,bhke->bhqk', q, k) * scale
            scores = torch.where(mask[None, None], scores, -1000000000.0)
            p = _softmax(scores, axis=-1)
            o = torch.einsum('bhqk,bhke->bhqe', p, v)
            o = torch.einsum('bhte,hed->btd', o, Wo)
            mem = o[:, :M, :].clone()
            outs.append(o[:, M:, :])
        return torch.concatenate(outs, axis=1)
    x = A_x
    g = _sigmoid((x * gate_w).sum(-1, keepdims=True))
    a = _A_ref(x, A_c_kv, A_w_q, A_w_uk, A_w_uv, A_w_o)
    b = _B_ref(x, B_mem0, B_Wq, B_Wk, B_Wv, B_Wo, B_seg_len)
    return g * a + (1 - g) * b
```

```cpp
#include <hip/hip_runtime.h>
#include <hip/hip_bf16.h>
#include <cstdint>
#include <type_traits>
#include <utility>

using bf16 = __hip_bfloat16;
typedef short bf16x8 __attribute__((ext_vector_type(8)));
typedef short s16x4 __attribute__((ext_vector_type(4)));
typedef float f32x16 __attribute__((ext_vector_type(16)));
typedef float f32x4 __attribute__((ext_vector_type(4)));
typedef float f32x2 __attribute__((ext_vector_type(2)));
typedef unsigned u32x16 __attribute__((ext_vector_type(16)));
typedef unsigned u32x8 __attribute__((ext_vector_type(8)));
typedef unsigned u32x4 __attribute__((ext_vector_type(4)));
typedef unsigned u32x2 __attribute__((ext_vector_type(2)));
typedef __bf16 bfv8 __attribute__((ext_vector_type(8)));
typedef __bf16 bfv16 __attribute__((ext_vector_type(16)));
#define LAS __attribute__((address_space(3)))

constexpr int SEQ = 2048, DM = 512, NH = 32, DH = 128, HD = NH * DH;
constexpr int NSEG = 64, SEGL = 32;
constexpr float ATT_SCALE = 0.08838834764831845f;

#define ANCH 1
#define MDT 3000
#define VDT 3300
#define PDT 4200
#define XDT 0
#define PDL 300
#define EARLY 1
#define HDT 0
#define KVSC false
#define ADELAY 13000
#define MIX 1
#define ROT 1
#define LAG 2
#define PKM 0
#define PREV 0
#define CALA 0
#define CALN 8
#define JIT 3
#define RDT 0
#define PRIO 0
#define AM 182
#define AV 182
#define AX 96
#define AP 104
#define BDT 1500
#define APEN 40
#define SLACK 0
#define WARM 1
#define KPOIS 1
#define AA 66u
#define ADT 300
#define PCONT 1
#define GMAP 0x76543210u
#define WFLIP 1
#define PBAR 8
#define PDS 2
#define AH 160u
#define AFM 20u
#define PF1 1
#define PUBW 4
#define SANC 0
#define EF 1
#define PF 3
#define TCH 1
#define XT 6
#define ADS 2u
#define ADW 4u
#define ADM 12u
#define LOOK 1
#define ADS0 3u
#define FDT 200
#define VAD 900
#define AS0 6
#define VPD 2800
constexpr size_t SZ_ACT = (size_t)SEQ * HD * 2;
constexpr int NR = 4, SL_MEM = 0, SL_P = 1, SL_VM = 5, SLOTS = 37;
constexpr size_t SZ_RING = (size_t)NR * SLOTS * 16384;
constexpr size_t SZ_PREG = SZ_RING;
constexpr size_t SZ_POISON = SZ_PREG + 16384;
constexpr int NPAGE = (int)(SZ_RING >> 21) + 2;
static_assert(NPAGE <= 4, "parity bits");
constexpr size_t OFF_TMO = (SZ_POISON + 255) & ~(size_t)255;
constexpr size_t OFF_MAIL = OFF_TMO + 65536;
constexpr size_t OFF_QA = OFF_MAIL + 65536;
constexpr size_t OFF_KA = OFF_QA + SZ_ACT;
constexpr size_t OFF_VA = OFF_KA + SZ_ACT;
constexpr size_t OFF_OA = OFF_VA + SZ_ACT;
constexpr size_t OFF_QB = OFF_OA + SZ_ACT;
constexpr size_t OFF_KB = OFF_QB + SZ_ACT;
constexpr size_t OFF_VOX = OFF_KB + SZ_ACT;
constexpr size_t OFF_PR = OFF_VOX + 4 * SZ_ACT;
constexpr size_t OFF_PART = OFF_PR + (size_t)NSEG * NH * 2048;
constexpr size_t SZ_PART = (size_t)5 * SEQ * DM * 4;
constexpr size_t OFF_BB = OFF_PART + SZ_PART;
constexpr size_t OFF_G = OFF_BB + (size_t)SEQ * DM * 2;
constexpr size_t OFF_WT = OFF_G + 8192;
constexpr size_t WS_END = OFF_WT + (size_t)HD * DM * 2;
constexpr int W_CTRA = 288, W_AQ = 128, W_CTRP = 320, W_PROG = 1024, W_QCNT = 2048, W_OQ = 3584, W_LIST = 4352, W_PG = 4736, W_CAL = 6656, W_VO = 8192, W_RDY = 10240, W_FA = 12288;
constexpr int MASTER = 159;
constexpr int FSP = 56;
constexpr int SPLIT = 60;

typedef __bf16 bf16x2_t __attribute__((ext_vector_type(2)));
__device__ __forceinline__ unsigned cvtpk(float lo, float hi) {
    const f32x2 f = {lo, hi}; return __builtin_bit_cast(unsigned, __builtin_convertvector(f, bf16x2_t));
}
__device__ __forceinline__ float bf2f(unsigned short b) { return __uint_as_float((unsigned)b << 16); }
__device__ __forceinline__ float rbf(float x) { return bf2f((unsigned short)(cvtpk(x, x) & 0xFFFF)); }

__device__ __forceinline__ unsigned off_b(unsigned row, unsigned ch) { return 256u * row + 16u * (ch ^ (((row & 3) << 2) | ((row >> 2) & 3))); }
__device__ __forceinline__ unsigned tr_addr16(unsigned lane, unsigned c, unsigned t) {
    const unsigned g = lane >> 4, q = (lane & 15) >> 2, p = lane & 3;
    return off_b(8 * g + 4 * t + q, 2 * c + (p >> 1)) + 8 * (p & 1);
}
__device__ __forceinline__ s16x4 tr_rd(const char* lds) { return __builtin_amdgcn_ds_read_tr16_b64_v4i16((LAS s16x4*)lds); }
__device__ __forceinline__ u32x2 tr_rdu(const char* lds) { return __builtin_bit_cast(u32x2, tr_rd(lds)); }
__device__ __forceinline__ bf16x8 cat8(s16x4 a, s16x4 b) {
    const u32x2 x = __builtin_bit_cast(u32x2, a), y = __builtin_bit_cast(u32x2, b);
    const u32x4 w = {x[0], x[1], y[0], y[1]}; return __builtin_bit_cast(bf16x8, w); }
__device__ __forceinline__ f32x4 smf(u32x4 a, u32x8 b, f32x4 c, int idx) {
    return __builtin_amdgcn_smfmac_f32_16x16x64_bf16(__builtin_bit_cast(bfv8, a), __builtin_bit_cast(bfv16, b), c, idx, 0, 0); }
__device__ __forceinline__ u32x8 ilv(u32x2 a0, u32x2 a1, u32x2 b0, u32x2 b1) {
    return u32x8{a0[0], a1[0], a0[1], a1[1], b0[0], b1[0], b0[1], b1[1]}; }
__device__ __forceinline__ float swadd(float a, float b) {
    auto r = __builtin_amdgcn_permlane32_swap(__float_as_uint(a), __float_as_uint(b), false, false);
    return __uint_as_float(r[0]) + __uint_as_float(r[1]); }
__device__ __forceinline__ u32x4 mk_rs(const void* p, unsigned bytes) {
    return u32x4{(unsigned)(uintptr_t)p, (unsigned)((uintptr_t)p >> 32) & 0xFFFFu, bytes, 0x00020000u}; }
__device__ __forceinline__ u32x4 ldw(const bf16* p, unsigned v, unsigned so) {
    return __builtin_amdgcn_raw_buffer_load_b128(__builtin_amdgcn_make_buffer_rsrc((void*)p, 0, DM * HD * 2, 0x00020000), v, so, 0); }
template <bool SC1>
__device__ __forceinline__ void dma16(u32x4 rs, unsigned voff, unsigned soff, unsigned ldsaddr) {
    unsigned keep;
    soff = __builtin_amdgcn_readfirstlane(soff); ldsaddr = __builtin_amdgcn_readfirstlane(ldsaddr);
    if (SC1) asm volatile("s_mov_b32 %0, m0\n\ts_mov_b32 m0, %4\n\ts_nop 0\n\tbuffer_load_dwordx4 %1, %2, %3 offen sc1 lds\n\ts_mov_b32 m0, %0"
                          : "=&s"(keep) : "v"(voff), "s"(rs), "s"(soff), "s"(ldsaddr) : "memory");
    else asm volatile("s_mov_b32 %0, m0\n\ts_mov_b32 m0, %4\n\ts_nop 0\n\tbuffer_load_dwordx4 %1, %2, %3 offen lds\n\ts_mov_b32 m0, %0"
                      : "=&s"(keep) : "v"(voff), "s"(rs), "s"(soff), "s"(ldsaddr) : "memory");
}
#define DMA4K "buffer_load_dwordx4 %2, %3, %1 offen sc1 lds\n\tbuffer_load_dwordx4 %2, %3, %1 offen offset:1024 sc1 lds\n\t" \
              "buffer_load_dwordx4 %2, %3, %1 offen offset:2048 sc1 lds\n\tbuffer_load_dwordx4 %2, %3, %1 offen offset:3072 sc1 lds\n\t"
#define DMANX "s_add_u32 m0, m0, 0x1000\n\ts_add_u32 %1, %1, 0x1000\n\t"
__device__ __forceinline__ void dma16k(u32x4 rs, unsigned voff, unsigned soff, unsigned ldsaddr) {
    unsigned keep;
    soff = __builtin_amdgcn_readfirstlane(soff); ldsaddr = __builtin_amdgcn_readfirstlane(ldsaddr);
    asm volatile("s_mov_b32 %0, m0\n\ts_mov_b32 m0, %4\n\ts_nop 0\n\t" DMA4K DMANX DMA4K DMANX DMA4K DMANX DMA4K "s_mov_b32 m0, %0"
                 : "=&s"(keep), "+s"(soff) : "v"(voff), "s"(rs), "s"(ldsaddr) : "memory", "scc");
}
__device__ __forceinline__ void dma4(u32x4 rs, unsigned voff, unsigned ldsaddr) {
    unsigned keep;
    ldsaddr = __builtin_amdgcn_readfirstlane(ldsaddr);
    asm volatile("s_mov_b32 %0, m0\n\ts_mov_b32 m0, %3\n\ts_nop 0\n\tbuffer_load_dword %1, %2, 0 offen sc1 lds\n\ts_mov_b32 m0, %0"
                 : "=&s"(keep) : "v"(voff), "s"(rs), "s"(ldsaddr) : "memory");
}
#define LDS_BARRIER() do { asm volatile("s_waitcnt lgkmcnt(0)" ::: "memory"); __builtin_amdgcn_s_barrier(); asm volatile("" ::: "memory"); } while (0)
#define LRD(d, a, o) asm volatile("ds_read_b128 %0, %1 offset:%2" : "=v"(d) : "v"(a), "i"(o))
#define LWAIT1(n, a) asm volatile("s_waitcnt lgkmcnt(%1)" : "+v"(a) : "i"(n))
#define LWAIT2(n, a, b) asm volatile("s_waitcnt lgkmcnt(" #n ")" : "+v"(a), "+v"(b))
#define VMWAIT(n) asm volatile("s_waitcnt vmcnt(%0)" :: "i"(n) : "memory")

constexpr unsigned SPIN_CAP = 1u << 17;
__device__ __forceinline__ bool bail(unsigned spins, unsigned cap, unsigned* tmo, unsigned code) {
    if ((spins & 63u) == 63u && __hip_atomic_load(tmo, __ATOMIC_RELAXED, __HIP_MEMORY_SCOPE_AGENT) != 0u) return true;
    if (spins > cap) { __hip_atomic_store(tmo, code, __ATOMIC_RELAXED, __HIP_MEMORY_SCOPE_AGENT); return true; }
    return false; }
template <int SLP>
__device__ __forceinline__ unsigned wave_wait(unsigned* w, unsigned target, unsigned* tmo, unsigned code, unsigned mask = ~0u) {
    unsigned v;
    for (unsigned spins = 0;; ++spins) {
        v = __builtin_amdgcn_readfirstlane(__hip_atomic_load(w, __ATOMIC_RELAXED, __HIP_MEMORY_SCOPE_AGENT));
        if ((v & mask) >= target) break;
        if (bail(spins, SPIN_CAP << 1, tmo, code)) break;
        __builtin_amdgcn_s_sleep(SLP); asm volatile("" ::: "memory");
    }
    return v;
}
__device__ __forceinline__ unsigned pin_s(unsigned x) { x = __builtin_amdgcn_readfirstlane(x); asm volatile("" : "+s"(x)); return x; }
constexpr int NPA = 96;
struct Hello {
    unsigned long long* rep; unsigned long long num;
#define PRB(k) do {} while (0)
    __device__ __forceinline__ unsigned long long peek() const { return __hip_atomic_load(rep, __ATOMIC_RELAXED, __HIP_MEMORY_SCOPE_AGENT); }
    __device__ __forceinline__ void wait(unsigned long long v) const {
        for (unsigned i = 0; v != num; ++i) {
            if (i > SPIN_CAP) { __hip_atomic_store((unsigned*)((char*)(rep - blockIdx.x) - 98304), 0xC0000u, __ATOMIC_RELAXED, __HIP_MEMORY_SCOPE_AGENT); break; }
            __builtin_amdgcn_s_sleep(4); v = peek(); } }
};

#define ASM_PROJ_PUB \
        "s_mov_b32 %[keep], m0\n\ts_mov_b32 %[n], 0\n\tv_add_u32 v180, 0x18000, %[xb]\n\tds_read_b128 v[128:131], v180 offset:0\n\tds_read_b128 v[132:135], v180 offset:512\n\t" \
        "ds_read_b128 v[136:139], v180 offset:1024\n\tds_read_b128 v[140:143], v180 offset:1536\n\t1:\ns_waitcnt lgkmcnt(3)\n\tv_mfma_f32_16x16x32_bf16 v[160:163], v[0:3], v[128:131], 0\n\t" \
        "ds_read_b128 v[144:147], v180 offset:2048\n\tv_mfma_f32_16x16x32_bf16 v[164:167], v[64:67], v[128:131], 0\n\ts_lshl_b32 %[im], %[n], 15\n\ts_and_b32 %[im], %[im], 0x18000\n\t" \
        "s_waitcnt lgkmcnt(3)\n\tv_mfma_f32_16x16x32_bf16 v[160:163], v[4:7], v[132:135], v[160:163]\n\tds_read_b128 v[148:151], v180 offset:2560\n\t" \
        "v_mfma_f32_16x16x32_bf16 v[164:167], v[68:71], v[132:135], v[164:167]\n\ts_xor_b32 %[t0], %[im], 0x10000\n\ts_add_u32 %[t0], %[t0], %[lb]\n\ts_cmp_eq_u32 %[n], 0\n\ts_waitcnt lgkmcnt(3)\n\t" \
        "v_mfma_f32_16x16x32_bf16 v[160:163], v[8:11], v[136:139], v[160:163]\n\tds_read_b128 v[152:155], v180 offset:3072\n\tv_mfma_f32_16x16x32_bf16 v[164:167], v[72:75], v[136:139], v[164:167]\n\t" \
        "v_mul_f32 v168, %[sc], v168\n\tv_mul_f32 v169, %[sc], v169\n\tv_mul_f32 v170, %[sc], v170\n\tv_mul_f32 v171, %[sc], v171\n\ts_waitcnt lgkmcnt(3)\n\t" \
        "v_mfma_f32_16x16x32_bf16 v[160:163], v[12:15], v[140:143], v[160:163]\n\tds_read_b128 v[156:159], v180 offset:3584\n\t" \
        "v_mfma_f32_16x16x32_bf16 v[164:167], v[76:79], v[140:143], v[164:167]\n\tv_mul_f32 v172, %[sc], v172\n\tv_mul_f32 v173, %[sc], v173\n\tv_mul_f32 v174, %[sc], v174\n\t" \
        "v_mul_f32 v175, %[sc], v175\n\ts_waitcnt lgkmcnt(3)\n\tv_mfma_f32_16x16x32_bf16 v[160:163], v[16:19], v[144:147], v[160:163]\n\tds_read_b128 v[128:131], v180 offset:4096\n\t" \
        "v_mfma_f32_16x16x32_bf16 v[164:167], v[80:83], v[144:147], v[164:167]\n\ts_cbranch_scc1 2f\n\tv_cvt_pk_bf16_f32 v176, v168, v169\n\tv_cvt_pk_bf16_f32 v177, v170, v171\n\t" \
        "v_cvt_pk_bf16_f32 v178, v172, v173\n\tv_cvt_pk_bf16_f32 v179, v174, v175\n\tbuffer_store_dwordx4 v[176:179], %[co], %[rc], %[sp] offen offset:1024 sc1\n\t2:\ns_waitcnt lgkmcnt(3)\n\t" \
        "v_mfma_f32_16x16x32_bf16 v[160:163], v[20:23], v[148:151], v[160:163]\n\tds_read_b128 v[132:135], v180 offset:4608\n\t" \
        "v_mfma_f32_16x16x32_bf16 v[164:167], v[84:87], v[148:151], v[164:167]\n\ts_add_u32 %[sp], %[sp], 262144\n\ts_waitcnt lgkmcnt(3)\n\t" \
        "v_mfma_f32_16x16x32_bf16 v[160:163], v[24:27], v[152:155], v[160:163]\n\tds_read_b128 v[136:139], v180 offset:5120\n\t" \
        "v_mfma_f32_16x16x32_bf16 v[164:167], v[88:91], v[152:155], v[164:167]\n\ts_waitcnt lgkmcnt(3)\n\tv_mfma_f32_16x16x32_bf16 v[160:163], v[28:31], v[156:159], v[160:163]\n\t" \
        "ds_read_b128 v[140:143], v180 offset:5632\n\tv_mfma_f32_16x16x32_bf16 v[164:167], v[92:95], v[156:159], v[164:167]\n\ts_sub_u32 %[t1], %[n], 1\n\ts_cmp_lt_u32 %[n], 3\n\t" \
        "s_waitcnt lgkmcnt(3)\n\tv_mfma_f32_16x16x32_bf16 v[160:163], v[32:35], v[128:131], v[160:163]\n\tds_read_b128 v[144:147], v180 offset:6144\n\t" \
        "v_mfma_f32_16x16x32_bf16 v[164:167], v[96:99], v[128:131], v[164:167]\n\ts_cbranch_scc1 4f\n\ts_waitcnt vmcnt(6)\n\t3:\ns_barrier\n\ts_mov_b32 m0, %[t0]\n\tv_mov_b32 v181, %[t1]\n\t" \
        "s_mov_b64 exec, %[em]\n\tglobal_store_dword %[co], v181, %[pg] sc1\n\ts_mov_b64 exec, -1\n\ts_waitcnt lgkmcnt(3)\n\tv_mfma_f32_16x16x32_bf16 v[160:163], v[36:39], v[132:135], v[160:163]\n\t" \
        "ds_read_b128 v[148:151], v180 offset:6656\n\tv_mfma_f32_16x16x32_bf16 v[164:167], v[100:103], v[132:135], v[164:167]\n\tbuffer_load_dwordx4 %[dv], %[ra], %[so] offen lds\n\t" \
        "s_add_u32 m0, m0, 896\n\ts_waitcnt lgkmcnt(3)\n\tv_mfma_f32_16x16x32_bf16 v[160:163], v[40:43], v[136:139], v[160:163]\n\tds_read_b128 v[152:155], v180 offset:7168\n\t" \
        "v_mfma_f32_16x16x32_bf16 v[164:167], v[104:107], v[136:139], v[164:167]\n\ts_waitcnt lgkmcnt(3)\n\tv_mfma_f32_16x16x32_bf16 v[160:163], v[44:47], v[140:143], v[160:163]\n\t" \
        "ds_read_b128 v[156:159], v180 offset:7680\n\tv_mfma_f32_16x16x32_bf16 v[164:167], v[108:111], v[140:143], v[164:167]\n\tbuffer_load_dwordx4 %[dv], %[ra], %[so] offen offset:128 lds\n\t" \
        "s_add_u32 m0, m0, 896\n\ts_waitcnt lgkmcnt(3)\n\tv_mfma_f32_16x16x32_bf16 v[160:163], v[48:51], v[144:147], v[160:163]\n\tds_read_b128 v[128:131], v180 offset:16384\n\t" \
        "v_mfma_f32_16x16x32_bf16 v[164:167], v[112:115], v[144:147], v[164:167]\n\ts_waitcnt lgkmcnt(3)\n\tv_mfma_f32_16x16x32_bf16 v[160:163], v[52:55], v[148:151], v[160:163]\n\t" \
        "ds_read_b128 v[132:135], v180 offset:16896\n\tv_mfma_f32_16x16x32_bf16 v[164:167], v[116:119], v[148:151], v[164:167]\n\tbuffer_load_dwordx4 %[dv], %[ra], %[so] offen offset:256 lds\n\t" \
        "s_add_u32 m0, m0, 896\n\ts_waitcnt lgkmcnt(3)\n\tv_mfma_f32_16x16x32_bf16 v[160:163], v[56:59], v[152:155], v[160:163]\n\tds_read_b128 v[136:139], v180 offset:17408\n\t" \
        "v_mfma_f32_16x16x32_bf16 v[164:167], v[120:123], v[152:155], v[164:167]\n\ts_waitcnt lgkmcnt(3)\n\tv_mfma_f32_16x16x32_bf16 v[160:163], v[60:63], v[156:159], v[160:163]\n\t" \
        "ds_read_b128 v[140:143], v180 offset:17920\n\tv_mfma_f32_16x16x32_bf16 v[164:167], v[124:127], v[156:159], v[164:167]\n\tbuffer_load_dwordx4 %[dv], %[ra], %[so] offen offset:384 lds\n\t" \
        "s_add_u32 %[so], %[so], 32768\n\ts_min_u32 %[so], %[so], 0x1f8000\n\ts_waitcnt lgkmcnt(3)\n\tv_mfma_f32_16x16x32_bf16 v[168:171], v[0:3], v[128:131], 0\n\t" \
        "ds_read_b128 v[144:147], v180 offset:18432\n\tv_mfma_f32_16x16x32_bf16 v[172:175], v[64:67], v[128:131], 0\n\ts_waitcnt lgkmcnt(3)\n\t" \
        "v_mfma_f32_16x16x32_bf16 v[168:171], v[4:7], v[132:135], v[168:171]\n\tds_read_b128 v[148:151], v180 offset:18944\n\tv_mfma_f32_16x16x32_bf16 v[172:175], v[68:71], v[132:135], v[172:175]\n\t" \
        "s_waitcnt lgkmcnt(3)\n\tv_mfma_f32_16x16x32_bf16 v[168:171], v[8:11], v[136:139], v[168:171]\n\tds_read_b128 v[152:155], v180 offset:19456\n\t" \
        "v_mfma_f32_16x16x32_bf16 v[172:175], v[72:75], v[136:139], v[172:175]\n\tv_mul_f32 v160, %[sc], v160\n\tv_mul_f32 v161, %[sc], v161\n\tv_mul_f32 v162, %[sc], v162\n\t" \
        "v_mul_f32 v163, %[sc], v163\n\ts_waitcnt lgkmcnt(3)\n\tv_mfma_f32_16x16x32_bf16 v[168:171], v[12:15], v[140:143], v[168:171]\n\tds_read_b128 v[156:159], v180 offset:19968\n\t" \
        "v_mfma_f32_16x16x32_bf16 v[172:175], v[76:79], v[140:143], v[172:175]\n\tv_mul_f32 v164, %[sc], v164\n\tv_mul_f32 v165, %[sc], v165\n\tv_mul_f32 v166, %[sc], v166\n\t" \
        "v_mul_f32 v167, %[sc], v167\n\ts_waitcnt lgkmcnt(3)\n\tv_mfma_f32_16x16x32_bf16 v[168:171], v[16:19], v[144:147], v[168:171]\n\tds_read_b128 v[128:131], v180 offset:20480\n\t" \
        "v_mfma_f32_16x16x32_bf16 v[172:175], v[80:83], v[144:147], v[172:175]\n\tv_cvt_pk_bf16_f32 v176, v160, v161\n\tv_cvt_pk_bf16_f32 v177, v162, v163\n\tv_cvt_pk_bf16_f32 v178, v164, v165\n\t" \
        "v_cvt_pk_bf16_f32 v179, v166, v167\n\tbuffer_store_dwordx4 v[176:179], %[co], %[rc], %[sp] offen sc1\n\ts_waitcnt lgkmcnt(3)\n\t" \
        "v_mfma_f32_16x16x32_bf16 v[168:171], v[20:23], v[148:151], v[168:171]\n\tds_read_b128 v[132:135], v180 offset:20992\n\t" \
        "v_mfma_f32_16x16x32_bf16 v[172:175], v[84:87], v[148:151], v[172:175]\n\ts_waitcnt lgkmcnt(3)\n\tv_mfma_f32_16x16x32_bf16 v[168:171], v[24:27], v[152:155], v[168:171]\n\t" \
        "ds_read_b128 v[136:139], v180 offset:21504\n\tv_mfma_f32_16x16x32_bf16 v[172:175], v[88:91], v[152:155], v[172:175]\n\ts_waitcnt lgkmcnt(3)\n\t" \
        "v_mfma_f32_16x16x32_bf16 v[168:171], v[28:31], v[156:159], v[168:171]\n\tds_read_b128 v[140:143], v180 offset:22016\n\t" \
        "v_mfma_f32_16x16x32_bf16 v[172:175], v[92:95], v[156:159], v[172:175]\n\ts_waitcnt lgkmcnt(3)\n\tv_mfma_f32_16x16x32_bf16 v[168:171], v[32:35], v[128:131], v[168:171]\n\t" \
        "ds_read_b128 v[144:147], v180 offset:22528\n\tv_mfma_f32_16x16x32_bf16 v[172:175], v[96:99], v[128:131], v[172:175]\n\ts_waitcnt lgkmcnt(3)\n\t" \
        "v_mfma_f32_16x16x32_bf16 v[168:171], v[36:39], v[132:135], v[168:171]\n\tds_read_b128 v[148:151], v180 offset:23040\n\t" \
        "v_mfma_f32_16x16x32_bf16 v[172:175], v[100:103], v[132:135], v[172:175]\n\ts_waitcnt lgkmcnt(3)\n\tv_mfma_f32_16x16x32_bf16 v[168:171], v[40:43], v[136:139], v[168:171]\n\t" \
        "ds_read_b128 v[152:155], v180 offset:23552\n\tv_mfma_f32_16x16x32_bf16 v[172:175], v[104:107], v[136:139], v[172:175]\n\ts_waitcnt lgkmcnt(3)\n\t" \
        "v_mfma_f32_16x16x32_bf16 v[168:171], v[44:47], v[140:143], v[168:171]\n\tds_read_b128 v[156:159], v180 offset:24064\n\t" \
        "v_mfma_f32_16x16x32_bf16 v[172:175], v[108:111], v[140:143], v[172:175]\n\tv_add_u32 v180, %[im], %[xb]\n\ts_waitcnt lgkmcnt(3)\n\t" \
        "v_mfma_f32_16x16x32_bf16 v[168:171], v[48:51], v[144:147], v[168:171]\n\tds_read_b128 v[128:131], v180 offset:0\n\tv_mfma_f32_16x16x32_bf16 v[172:175], v[112:115], v[144:147], v[172:175]\n\t" \
        "s_waitcnt lgkmcnt(3)\n\tv_mfma_f32_16x16x32_bf16 v[168:171], v[52:55], v[148:151], v[168:171]\n\tds_read_b128 v[132:135], v180 offset:512\n\t" \
        "v_mfma_f32_16x16x32_bf16 v[172:175], v[116:119], v[148:151], v[172:175]\n\ts_waitcnt lgkmcnt(3)\n\tv_mfma_f32_16x16x32_bf16 v[168:171], v[56:59], v[152:155], v[168:171]\n\t" \
        "ds_read_b128 v[136:139], v180 offset:1024\n\tv_mfma_f32_16x16x32_bf16 v[172:175], v[120:123], v[152:155], v[172:175]\n\ts_add_u32 %[n], %[n], 1\n\ts_cmp_lt_u32 %[n], 64\n\t" \
        "s_waitcnt lgkmcnt(3)\n\tv_mfma_f32_16x16x32_bf16 v[168:171], v[60:63], v[156:159], v[168:171]\n\tds_read_b128 v[140:143], v180 offset:1536\n\t" \
        "v_mfma_f32_16x16x32_bf16 v[172:175], v[124:127], v[156:159], v[172:175]\n\ts_cbranch_scc1 1b\n\ts_mov_b32 m0, %[keep]\n\ts_waitcnt lgkmcnt(0)\n\ts_nop 7\n\tv_mul_f32 v168, %[sc], v168\n\t" \
        "v_mul_f32 v169, %[sc], v169\n\tv_mul_f32 v170, %[sc], v170\n\tv_mul_f32 v171, %[sc], v171\n\tv_mul_f32 v172, %[sc], v172\n\tv_mul_f32 v173, %[sc], v173\n\tv_mul_f32 v174, %[sc], v174\n\t" \
        "v_mul_f32 v175, %[sc], v175\n\tv_cvt_pk_bf16_f32 v176, v168, v169\n\tv_cvt_pk_bf16_f32 v177, v170, v171\n\tv_cvt_pk_bf16_f32 v178, v172, v173\n\tv_cvt_pk_bf16_f32 v179, v174, v175\n\t" \
        "buffer_store_dwordx4 v[176:179], %[co], %[rc], %[sp] offen offset:1024 sc1\n\ts_branch 5f\n\t4:\ns_waitcnt vmcnt(0)\n\ts_mov_b32 %[t1], %[n]\n\ts_branch 3b\n\t5:\n"
#define ASM_PROJ_A \
        "s_mov_b32 %[keep], m0\n\ts_mov_b32 %[n], 0\n\tv_add_u32 v180, 0x18000, %[xb]\n\tds_read_b128 v[128:131], v180 offset:0\n\tds_read_b128 v[132:135], v180 offset:512\n\t" \
        "ds_read_b128 v[136:139], v180 offset:1024\n\tds_read_b128 v[140:143], v180 offset:1536\n\t1:\ns_waitcnt lgkmcnt(3)\n\tv_mfma_f32_16x16x32_bf16 v[160:163], v[0:3], v[128:131], 0\n\t" \
        "ds_read_b128 v[144:147], v180 offset:2048\n\tv_mfma_f32_16x16x32_bf16 v[164:167], v[64:67], v[128:131], 0\n\ts_lshl_b32 %[im], %[n], 15\n\ts_and_b32 %[im], %[im], 0x18000\n\t" \
        "s_waitcnt lgkmcnt(3)\n\tv_mfma_f32_16x16x32_bf16 v[160:163], v[4:7], v[132:135], v[160:163]\n\tds_read_b128 v[148:151], v180 offset:2560\n\t" \
        "v_mfma_f32_16x16x32_bf16 v[164:167], v[68:71], v[132:135], v[164:167]\n\ts_xor_b32 %[t0], %[im], 0x10000\n\ts_add_u32 %[t0], %[t0], %[lb]\n\ts_cmp_eq_u32 %[n], 0\n\ts_waitcnt lgkmcnt(3)\n\t" \
        "v_mfma_f32_16x16x32_bf16 v[160:163], v[8:11], v[136:139], v[160:163]\n\tds_read_b128 v[152:155], v180 offset:3072\n\tv_mfma_f32_16x16x32_bf16 v[164:167], v[72:75], v[136:139], v[164:167]\n\t" \
        "v_mul_f32 v168, %[sc], v168\n\tv_mul_f32 v169, %[sc], v169\n\tv_mul_f32 v170, %[sc], v170\n\tv_mul_f32 v171, %[sc], v171\n\ts_waitcnt lgkmcnt(3)\n\t" \
        "v_mfma_f32_16x16x32_bf16 v[160:163], v[12:15], v[140:143], v[160:163]\n\tds_read_b128 v[156:159], v180 offset:3584\n\t" \
        "v_mfma_f32_16x16x32_bf16 v[164:167], v[76:79], v[140:143], v[164:167]\n\tv_mul_f32 v172, %[sc], v172\n\tv_mul_f32 v173, %[sc], v173\n\tv_mul_f32 v174, %[sc], v174\n\t" \
        "v_mul_f32 v175, %[sc], v175\n\ts_waitcnt lgkmcnt(3)\n\tv_mfma_f32_16x16x32_bf16 v[160:163], v[16:19], v[144:147], v[160:163]\n\tds_read_b128 v[128:131], v180 offset:4096\n\t" \
        "v_mfma_f32_16x16x32_bf16 v[164:167], v[80:83], v[144:147], v[164:167]\n\ts_cbranch_scc1 2f\n\tv_cvt_pk_bf16_f32 v176, v168, v169\n\tv_cvt_pk_bf16_f32 v177, v170, v171\n\t" \
        "v_cvt_pk_bf16_f32 v178, v172, v173\n\tv_cvt_pk_bf16_f32 v179, v174, v175\n\tbuffer_store_dwordx4 v[176:179], %[co], %[rc], %[sp] offen offset:1024 sc1\n\t2:\ns_waitcnt lgkmcnt(3)\n\t" \
        "v_mfma_f32_16x16x32_bf16 v[160:163], v[20:23], v[148:151], v[160:163]\n\tds_read_b128 v[132:135], v180 offset:4608\n\t" \
        "v_mfma_f32_16x16x32_bf16 v[164:167], v[84:87], v[148:151], v[164:167]\n\ts_add_u32 %[sp], %[sp], 524288\n\ts_waitcnt lgkmcnt(3)\n\t" \
        "v_mfma_f32_16x16x32_bf16 v[160:163], v[24:27], v[152:155], v[160:163]\n\tds_read_b128 v[136:139], v180 offset:5120\n\t" \
        "v_mfma_f32_16x16x32_bf16 v[164:167], v[88:91], v[152:155], v[164:167]\n\ts_waitcnt lgkmcnt(3)\n\tv_mfma_f32_16x16x32_bf16 v[160:163], v[28:31], v[156:159], v[160:163]\n\t" \
        "ds_read_b128 v[140:143], v180 offset:5632\n\tv_mfma_f32_16x16x32_bf16 v[164:167], v[92:95], v[156:159], v[164:167]\n\ts_cmp_lt_u32 %[n], 2\n\ts_waitcnt lgkmcnt(3)\n\t" \
        "v_mfma_f32_16x16x32_bf16 v[160:163], v[32:35], v[128:131], v[160:163]\n\tds_read_b128 v[144:147], v180 offset:6144\n\t" \
        "v_mfma_f32_16x16x32_bf16 v[164:167], v[96:99], v[128:131], v[164:167]\n\ts_cbranch_scc1 4f\n\ts_waitcnt vmcnt(8)\n\t3:\ns_barrier\n\ts_mov_b32 m0, %[t0]\n\ts_waitcnt lgkmcnt(3)\n\t" \
        "v_mfma_f32_16x16x32_bf16 v[160:163], v[36:39], v[132:135], v[160:163]\n\tds_read_b128 v[148:151], v180 offset:6656\n\t" \
        "v_mfma_f32_16x16x32_bf16 v[164:167], v[100:103], v[132:135], v[164:167]\n\tbuffer_load_dwordx4 %[dv], %[ra], %[so] offen lds\n\ts_add_u32 m0, m0, 896\n\ts_waitcnt lgkmcnt(3)\n\t" \
        "v_mfma_f32_16x16x32_bf16 v[160:163], v[40:43], v[136:139], v[160:163]\n\tds_read_b128 v[152:155], v180 offset:7168\n\t" \
        "v_mfma_f32_16x16x32_bf16 v[164:167], v[104:107], v[136:139], v[164:167]\n\ts_waitcnt lgkmcnt(3)\n\tv_mfma_f32_16x16x32_bf16 v[160:163], v[44:47], v[140:143], v[160:163]\n\t" \
        "ds_read_b128 v[156:159], v180 offset:7680\n\tv_mfma_f32_16x16x32_bf16 v[164:167], v[108:111], v[140:143], v[164:167]\n\tbuffer_load_dwordx4 %[dv], %[ra], %[so] offen offset:128 lds\n\t" \
        "s_add_u32 m0, m0, 896\n\ts_waitcnt lgkmcnt(3)\n\tv_mfma_f32_16x16x32_bf16 v[160:163], v[48:51], v[144:147], v[160:163]\n\tds_read_b128 v[128:131], v180 offset:16384\n\t" \
        "v_mfma_f32_16x16x32_bf16 v[164:167], v[112:115], v[144:147], v[164:167]\n\ts_waitcnt lgkmcnt(3)\n\tv_mfma_f32_16x16x32_bf16 v[160:163], v[52:55], v[148:151], v[160:163]\n\t" \
        "ds_read_b128 v[132:135], v180 offset:16896\n\tv_mfma_f32_16x16x32_bf16 v[164:167], v[116:119], v[148:151], v[164:167]\n\tbuffer_load_dwordx4 %[dv], %[ra], %[so] offen offset:256 lds\n\t" \
        "s_add_u32 m0, m0, 896\n\ts_waitcnt lgkmcnt(3)\n\tv_mfma_f32_16x16x32_bf16 v[160:163], v[56:59], v[152:155], v[160:163]\n\tds_read_b128 v[136:139], v180 offset:17408\n\t" \
        "v_mfma_f32_16x16x32_bf16 v[164:167], v[120:123], v[152:155], v[164:167]\n\ts_waitcnt lgkmcnt(3)\n\tv_mfma_f32_16x16x32_bf16 v[160:163], v[60:63], v[156:159], v[160:163]\n\t" \
        "ds_read_b128 v[140:143], v180 offset:17920\n\tv_mfma_f32_16x16x32_bf16 v[164:167], v[124:127], v[156:159], v[164:167]\n\tbuffer_load_dwordx4 %[dv], %[ra], %[so] offen offset:384 lds\n\t" \
        "s_add_u32 %[so], %[so], 65536\n\ts_min_u32 %[so], %[so], 0x1f8000\n\ts_waitcnt lgkmcnt(3)\n\tv_mfma_f32_16x16x32_bf16 v[168:171], v[0:3], v[128:131], 0\n\t" \
        "ds_read_b128 v[144:147], v180 offset:18432\n\tv_mfma_f32_16x16x32_bf16 v[172:175], v[64:67], v[128:131], 0\n\ts_waitcnt lgkmcnt(3)\n\t" \
        "v_mfma_f32_16x16x32_bf16 v[168:171], v[4:7], v[132:135], v[168:171]\n\tds_read_b128 v[148:151], v180 offset:18944\n\tv_mfma_f32_16x16x32_bf16 v[172:175], v[68:71], v[132:135], v[172:175]\n\t" \
        "s_waitcnt lgkmcnt(3)\n\tv_mfma_f32_16x16x32_bf16 v[168:171], v[8:11], v[136:139], v[168:171]\n\tds_read_b128 v[152:155], v180 offset:19456\n\t" \
        "v_mfma_f32_16x16x32_bf16 v[172:175], v[72:75], v[136:139], v[172:175]\n\tv_mul_f32 v160, %[sc], v160\n\tv_mul_f32 v161, %[sc], v161\n\tv_mul_f32 v162, %[sc], v162\n\t" \
        "v_mul_f32 v163, %[sc], v163\n\ts_waitcnt lgkmcnt(3)\n\tv_mfma_f32_16x16x32_bf16 v[168:171], v[12:15], v[140:143], v[168:171]\n\tds_read_b128 v[156:159], v180 offset:19968\n\t" \
        "v_mfma_f32_16x16x32_bf16 v[172:175], v[76:79], v[140:143], v[172:175]\n\tv_mul_f32 v164, %[sc], v164\n\tv_mul_f32 v165, %[sc], v165\n\tv_mul_f32 v166, %[sc], v166\n\t" \
        "v_mul_f32 v167, %[sc], v167\n\ts_waitcnt lgkmcnt(3)\n\tv_mfma_f32_16x16x32_bf16 v[168:171], v[16:19], v[144:147], v[168:171]\n\tds_read_b128 v[128:131], v180 offset:20480\n\t" \
        "v_mfma_f32_16x16x32_bf16 v[172:175], v[80:83], v[144:147], v[172:175]\n\tv_cvt_pk_bf16_f32 v176, v160, v161\n\tv_cvt_pk_bf16_f32 v177, v162, v163\n\tv_cvt_pk_bf16_f32 v178, v164, v165\n\t" \
        "v_cvt_pk_bf16_f32 v179, v166, v167\n\tbuffer_store_dwordx4 v[176:179], %[co], %[rc], %[sp] offen sc1\n\ts_waitcnt lgkmcnt(3)\n\t" \
        "v_mfma_f32_16x16x32_bf16 v[168:171], v[20:23], v[148:151], v[168:171]\n\tds_read_b128 v[132:135], v180 offset:20992\n\t" \
        "v_mfma_f32_16x16x32_bf16 v[172:175], v[84:87], v[148:151], v[172:175]\n\ts_waitcnt lgkmcnt(3)\n\tv_mfma_f32_16x16x32_bf16 v[168:171], v[24:27], v[152:155], v[168:171]\n\t" \
        "ds_read_b128 v[136:139], v180 offset:21504\n\tv_mfma_f32_16x16x32_bf16 v[172:175], v[88:91], v[152:155], v[172:175]\n\ts_waitcnt lgkmcnt(3)\n\t" \
        "v_mfma_f32_16x16x32_bf16 v[168:171], v[28:31], v[156:159], v[168:171]\n\tds_read_b128 v[140:143], v180 offset:22016\n\t" \
        "v_mfma_f32_16x16x32_bf16 v[172:175], v[92:95], v[156:159], v[172:175]\n\ts_waitcnt lgkmcnt(3)\n\tv_mfma_f32_16x16x32_bf16 v[168:171], v[32:35], v[128:131], v[168:171]\n\t" \
        "ds_read_b128 v[144:147], v180 offset:22528\n\tv_mfma_f32_16x16x32_bf16 v[172:175], v[96:99], v[128:131], v[172:175]\n\ts_waitcnt lgkmcnt(3)\n\t" \
        "v_mfma_f32_16x16x32_bf16 v[168:171], v[36:39], v[132:135], v[168:171]\n\tds_read_b128 v[148:151], v180 offset:23040\n\t" \
        "v_mfma_f32_16x16x32_bf16 v[172:175], v[100:103], v[132:135], v[172:175]\n\ts_waitcnt lgkmcnt(3)\n\tv_mfma_f32_16x16x32_bf16 v[168:171], v[40:43], v[136:139], v[168:171]\n\t" \
        "ds_read_b128 v[152:155], v180 offset:23552\n\tv_mfma_f32_16x16x32_bf16 v[172:175], v[104:107], v[136:139], v[172:175]\n\ts_waitcnt lgkmcnt(3)\n\t" \
        "v_mfma_f32_16x16x32_bf16 v[168:171], v[44:47], v[140:143], v[168:171]\n\tds_read_b128 v[156:159], v180 offset:24064\n\t" \
        "v_mfma_f32_16x16x32_bf16 v[172:175], v[108:111], v[140:143], v[172:175]\n\tv_add_u32 v180, %[im], %[xb]\n\ts_waitcnt lgkmcnt(3)\n\t" \
        "v_mfma_f32_16x16x32_bf16 v[168:171], v[48:51], v[144:147], v[168:171]\n\tds_read_b128 v[128:131], v180 offset:0\n\tv_mfma_f32_16x16x32_bf16 v[172:175], v[112:115], v[144:147], v[172:175]\n\t" \
        "s_waitcnt lgkmcnt(3)\n\tv_mfma_f32_16x16x32_bf16 v[168:171], v[52:55], v[148:151], v[168:171]\n\tds_read_b128 v[132:135], v180 offset:512\n\t" \
        "v_mfma_f32_16x16x32_bf16 v[172:175], v[116:119], v[148:151], v[172:175]\n\ts_waitcnt lgkmcnt(3)\n\tv_mfma_f32_16x16x32_bf16 v[168:171], v[56:59], v[152:155], v[168:171]\n\t" \
        "ds_read_b128 v[136:139], v180 offset:1024\n\tv_mfma_f32_16x16x32_bf16 v[172:175], v[120:123], v[152:155], v[172:175]\n\ts_add_u32 %[n], %[n], 1\n\ts_cmp_lt_u32 %[n], 32\n\t" \
        "s_waitcnt lgkmcnt(3)\n\tv_mfma_f32_16x16x32_bf16 v[168:171], v[60:63], v[156:159], v[168:171]\n\tds_read_b128 v[140:143], v180 offset:1536\n\t" \
        "v_mfma_f32_16x16x32_bf16 v[172:175], v[124:127], v[156:159], v[172:175]\n\ts_cbranch_scc1 1b\n\ts_mov_b32 m0, %[keep]\n\ts_waitcnt lgkmcnt(0)\n\ts_nop 7\n\tv_mul_f32 v168, %[sc], v168\n\t" \
        "v_mul_f32 v169, %[sc], v169\n\tv_mul_f32 v170, %[sc], v170\n\tv_mul_f32 v171, %[sc], v171\n\tv_mul_f32 v172, %[sc], v172\n\tv_mul_f32 v173, %[sc], v173\n\tv_mul_f32 v174, %[sc], v174\n\t" \
        "v_mul_f32 v175, %[sc], v175\n\tv_cvt_pk_bf16_f32 v176, v168, v169\n\tv_cvt_pk_bf16_f32 v177, v170, v171\n\tv_cvt_pk_bf16_f32 v178, v172, v173\n\tv_cvt_pk_bf16_f32 v179, v174, v175\n\t" \
        "buffer_store_dwordx4 v[176:179], %[co], %[rc], %[sp] offen offset:1024 sc1\n\ts_branch 5f\n\t4:\ns_waitcnt vmcnt(4)\n\ts_branch 3b\n\t5:\n"
#define ASM_VOX_N0 4
#define ASM_VOX \
        "s_mov_b32 %[keep], m0\n\ts_mov_b32 %[n], 4\n\tv_add_u32 v224, 0x18000, %[xb]\n\tv_mov_b32 v226, 1\n\tds_read_b128 v[128:131], v224 offset:0\n\tds_read_b128 v[132:135], v224 offset:8192\n\t" \
        "ds_read_b128 v[136:139], v224 offset:512\n\tds_read_b128 v[140:143], v224 offset:8704\n\tds_read_b128 v[144:147], v224 offset:1024\n\tds_read_b128 v[148:151], v224 offset:9216\n\t" \
        "s_waitcnt lgkmcnt(0)\n\t1:\ns_waitcnt lgkmcnt(7)\n\tv_mfma_f32_16x16x32_bf16 v[192:195], v[0:3], v[128:131], 0\n\tds_read_b128 v[152:155], v224 offset:1536\n\t" \
        "ds_read_b128 v[156:159], v224 offset:9728\n\ts_waitcnt lgkmcnt(8)\n\tv_mfma_f32_16x16x32_bf16 v[196:199], v[0:3], v[132:135], 0\n\ts_lshl_b32 %[im], %[n], 15\n\t" \
        "s_and_b32 %[im], %[im], 0x18000\n\ts_xor_b32 %[t0], %[im], 0x10000\n\ts_add_u32 %[t0], %[t0], %[lb]\n\ts_waitcnt lgkmcnt(7)\n\t" \
        "v_mfma_f32_16x16x32_bf16 v[192:195], v[4:7], v[136:139], v[192:195]\n\tds_read_b128 v[128:131], v224 offset:2048\n\tds_read_b128 v[132:135], v224 offset:10240\n\ts_waitcnt lgkmcnt(8)\n\t" \
        "v_mfma_f32_16x16x32_bf16 v[196:199], v[4:7], v[140:143], v[196:199]\n\ts_cmp_eq_u32 %[n], 4\n\ts_cbranch_scc1 2f\n\tv_cvt_pk_bf16_f32 v216, v208, v209\n\t" \
        "v_cvt_pk_bf16_f32 v217, v210, v211\n\tv_cvt_pk_bf16_f32 v218, v212, v213\n\tv_cvt_pk_bf16_f32 v219, v214, v215\n\tbuffer_store_dwordx4 v[216:219], %[st], %[rw], %[sp] offen sc1\n\t" \
        "s_add_u32 %[sp], %[sp], 950272\n\t2:\ns_waitcnt lgkmcnt(7)\n\tv_mfma_f32_16x16x32_bf16 v[192:195], v[8:11], v[144:147], v[192:195]\n\tds_read_b128 v[136:139], v224 offset:2560\n\t" \
        "ds_read_b128 v[140:143], v224 offset:10752\n\ts_waitcnt lgkmcnt(8)\n\tv_mfma_f32_16x16x32_bf16 v[196:199], v[8:11], v[148:151], v[196:199]\n\ts_sub_u32 %[t1], %[n], 2\n\t" \
        "s_lshl_b32 %[t2], %[cnt], 7\n\tv_mov_b32 v225, %[t2]\n\ts_cmp_lt_u32 %[n], 5\n\ts_waitcnt lgkmcnt(5)\n\tv_mfma_f32_16x16x32_bf16 v[192:195], v[12:15], v[152:155], v[192:195]\n\t" \
        "ds_read_b128 v[144:147], v224 offset:3072\n\tds_read_b128 v[148:151], v224 offset:11264\n\ts_waitcnt lgkmcnt(6)\n\tv_mfma_f32_16x16x32_bf16 v[196:199], v[12:15], v[156:159], v[196:199]\n\t" \
        "s_cbranch_scc1 4f\n\ts_waitcnt vmcnt(8)\n\t3:\ns_barrier\n\ts_mov_b32 m0, %[t0]\n\tds_read_b128 v[160:163], %[vi] offset:0\n\tds_read_b128 v[164:167], %[vi] offset:1024\n\t" \
        "s_waitcnt lgkmcnt(7)\n\tv_mfma_f32_16x16x32_bf16 v[192:195], v[16:19], v[128:131], v[192:195]\n\tds_read_b128 v[168:171], %[vi] offset:2048\n\tds_read_b128 v[172:175], %[vi] offset:3072\n\t" \
        "s_waitcnt lgkmcnt(8)\n\tv_mfma_f32_16x16x32_bf16 v[196:199], v[16:19], v[132:135], v[196:199]\n\tds_read_b128 v[176:179], %[vi] offset:4096\n\tds_read_b128 v[180:183], %[vi] offset:5120\n\t" \
        "ds_read_b128 v[152:155], v224 offset:3584\n\tds_read_b128 v[156:159], v224 offset:11776\n\ts_cmp_lt_u32 %[cnt], %[t1]\n\ts_cselect_b64 exec, %[em], 0\n\t" \
        "global_atomic_add v225, v226, %[dn]\n\ts_mov_b64 exec, -1\n\ts_addc_u32 %[cnt], %[cnt], 0\n\ts_waitcnt lgkmcnt(11)\n\t" \
        "v_mfma_f32_16x16x32_bf16 v[192:195], v[20:23], v[136:139], v[192:195]\n\tds_read_b128 v[184:187], %[vi] offset:6144\n\tds_read_b128 v[188:191], %[vi] offset:7168\n\ts_waitcnt lgkmcnt(12)\n\t" \
        "v_mfma_f32_16x16x32_bf16 v[196:199], v[20:23], v[140:143], v[196:199]\n\tds_read_b128 v[128:131], v224 offset:4096\n\tds_read_b128 v[132:135], v224 offset:12288\n\ts_waitcnt lgkmcnt(13)\n\t" \
        "v_mfma_f32_16x16x32_bf16 v[192:195], v[24:27], v[144:147], v[192:195]\n\tds_read_b128 v[136:139], v224 offset:4608\n\tds_read_b128 v[140:143], v224 offset:12800\n\ts_waitcnt lgkmcnt(14)\n\t" \
        "v_mfma_f32_16x16x32_bf16 v[196:199], v[24:27], v[148:151], v[196:199]\n\tbuffer_load_dwordx4 %[dv], %[ra], %[so] offen lds\n\ts_add_u32 m0, m0, 896\n\tv_xor_b32 %[vi], 0x2000, %[vi]\n\t" \
        "s_waitcnt lgkmcnt(13)\n\tv_mfma_f32_16x16x32_bf16 v[200:203], v[160:163], v[64:67], 0\n\ts_waitcnt lgkmcnt(12)\n\tv_mfma_f32_16x16x32_bf16 v[204:207], v[164:167], v[64:67], 0\n\t" \
        "s_waitcnt lgkmcnt(7)\n\tv_mfma_f32_16x16x32_bf16 v[192:195], v[28:31], v[152:155], v[192:195]\n\tds_read_b128 v[144:147], v224 offset:5120\n\tds_read_b128 v[148:151], v224 offset:13312\n\t" \
        "s_waitcnt lgkmcnt(8)\n\tv_mfma_f32_16x16x32_bf16 v[196:199], v[28:31], v[156:159], v[196:199]\n\tbuffer_load_dwordx4 %[dv], %[ra], %[so] offen offset:128 lds\n\ts_add_u32 m0, m0, 896\n\t" \
        "v_mfma_f32_16x16x32_bf16 v[200:203], v[168:171], v[68:71], v[200:203]\n\tv_mfma_f32_16x16x32_bf16 v[204:207], v[172:175], v[68:71], v[204:207]\n\ts_waitcnt lgkmcnt(5)\n\t" \
        "v_mfma_f32_16x16x32_bf16 v[192:195], v[32:35], v[128:131], v[192:195]\n\tds_read_b128 v[152:155], v224 offset:5632\n\tds_read_b128 v[156:159], v224 offset:13824\n\ts_waitcnt lgkmcnt(6)\n\t" \
        "v_mfma_f32_16x16x32_bf16 v[196:199], v[32:35], v[132:135], v[196:199]\n\tbuffer_load_dwordx4 %[dv], %[ra], %[so] offen offset:256 lds\n\ts_add_u32 m0, m0, 896\n\t" \
        "v_mfma_f32_16x16x32_bf16 v[200:203], v[176:179], v[72:75], v[200:203]\n\tv_mfma_f32_16x16x32_bf16 v[204:207], v[180:183], v[72:75], v[204:207]\n\ts_waitcnt lgkmcnt(5)\n\t" \
        "v_mfma_f32_16x16x32_bf16 v[192:195], v[36:39], v[136:139], v[192:195]\n\tds_read_b128 v[128:131], v224 offset:6144\n\tds_read_b128 v[132:135], v224 offset:14336\n\ts_waitcnt lgkmcnt(6)\n\t" \
        "v_mfma_f32_16x16x32_bf16 v[196:199], v[36:39], v[140:143], v[196:199]\n\tbuffer_load_dwordx4 %[dv], %[ra], %[so] offen offset:384 lds\n\ts_add_u32 %[so], %[so], 0x8000\n\t" \
        "s_min_u32 %[so], %[so], 0x1f8000\n\tv_mfma_f32_16x16x32_bf16 v[200:203], v[184:187], v[76:79], v[200:203]\n\tv_mfma_f32_16x16x32_bf16 v[204:207], v[188:191], v[76:79], v[204:207]\n\t" \
        "s_waitcnt lgkmcnt(5)\n\tv_mfma_f32_16x16x32_bf16 v[192:195], v[40:43], v[144:147], v[192:195]\n\tds_read_b128 v[136:139], v224 offset:6656\n\tds_read_b128 v[140:143], v224 offset:14848\n\t" \
        "s_waitcnt lgkmcnt(6)\n\tv_mfma_f32_16x16x32_bf16 v[196:199], v[40:43], v[148:151], v[196:199]\n\tv_mfma_f32_16x16x32_bf16 v[208:211], v[160:163], v[80:83], 0\n\t" \
        "v_mfma_f32_16x16x32_bf16 v[212:215], v[164:167], v[80:83], 0\n\ts_waitcnt lgkmcnt(5)\n\tv_mfma_f32_16x16x32_bf16 v[192:195], v[44:47], v[152:155], v[192:195]\n\t" \
        "ds_read_b128 v[144:147], v224 offset:7168\n\tds_read_b128 v[148:151], v224 offset:15360\n\ts_waitcnt lgkmcnt(6)\n\tv_mfma_f32_16x16x32_bf16 v[196:199], v[44:47], v[156:159], v[196:199]\n\t" \
        "v_cvt_pk_bf16_f32 v216, v200, v201\n\tv_cvt_pk_bf16_f32 v217, v202, v203\n\tv_cvt_pk_bf16_f32 v218, v204, v205\n\tv_cvt_pk_bf16_f32 v219, v206, v207\n\t" \
        "buffer_store_dwordx4 v[216:219], %[st], %[rw], %[sp] offen sc1\n\ts_add_u32 %[sp], %[sp], 32768\n\tv_mfma_f32_16x16x32_bf16 v[208:211], v[168:171], v[84:87], v[208:211]\n\t" \
        "v_mfma_f32_16x16x32_bf16 v[212:215], v[172:175], v[84:87], v[212:215]\n\ts_waitcnt lgkmcnt(5)\n\tv_mfma_f32_16x16x32_bf16 v[192:195], v[48:51], v[128:131], v[192:195]\n\t" \
        "ds_read_b128 v[152:155], v224 offset:7680\n\tds_read_b128 v[156:159], v224 offset:15872\n\ts_waitcnt lgkmcnt(6)\n\tv_mfma_f32_16x16x32_bf16 v[196:199], v[48:51], v[132:135], v[196:199]\n\t" \
        "v_add_u32 v224, %[im], %[xb]\n\tv_mfma_f32_16x16x32_bf16 v[208:211], v[176:179], v[88:91], v[208:211]\n\tv_mfma_f32_16x16x32_bf16 v[212:215], v[180:183], v[88:91], v[212:215]\n\t" \
        "s_waitcnt lgkmcnt(5)\n\tv_mfma_f32_16x16x32_bf16 v[192:195], v[52:55], v[136:139], v[192:195]\n\tds_read_b128 v[128:131], v224 offset:0\n\tds_read_b128 v[132:135], v224 offset:8192\n\t" \
        "s_waitcnt lgkmcnt(6)\n\tv_mfma_f32_16x16x32_bf16 v[196:199], v[52:55], v[140:143], v[196:199]\n\ts_cmp_lt_u32 %[n], 9\n\ts_cbranch_scc0 6f\n\ts_sub_u32 %[t1], %[n], 1\n\t" \
        "s_lshl_b32 %[t2], %[cnt], 7\n\tv_mov_b32 v225, %[t2]\n\ts_waitcnt vmcnt(5)\n\ts_barrier\n\ts_cmp_lt_u32 %[cnt], %[t1]\n\ts_cselect_b64 exec, %[em], 0\n\t" \
        "global_atomic_add v225, v226, %[dn]\n\ts_mov_b64 exec, -1\n\ts_addc_u32 %[cnt], %[cnt], 0\n\t6:\nv_mfma_f32_16x16x32_bf16 v[208:211], v[184:187], v[92:95], v[208:211]\n\t" \
        "v_mfma_f32_16x16x32_bf16 v[212:215], v[188:191], v[92:95], v[212:215]\n\ts_waitcnt lgkmcnt(5)\n\tv_mfma_f32_16x16x32_bf16 v[192:195], v[56:59], v[144:147], v[192:195]\n\t" \
        "ds_read_b128 v[136:139], v224 offset:512\n\tds_read_b128 v[140:143], v224 offset:8704\n\ts_waitcnt lgkmcnt(6)\n\tv_mfma_f32_16x16x32_bf16 v[196:199], v[56:59], v[148:151], v[196:199]\n\t" \
        "v_mfma_f32_16x16x32_bf16 v[200:203], v[160:163], v[96:99], 0\n\tv_mfma_f32_16x16x32_bf16 v[204:207], v[164:167], v[96:99], 0\n\ts_waitcnt lgkmcnt(5)\n\t" \
        "v_mfma_f32_16x16x32_bf16 v[192:195], v[60:63], v[152:155], v[192:195]\n\tds_read_b128 v[144:147], v224 offset:1024\n\tds_read_b128 v[148:151], v224 offset:9216\n\ts_waitcnt lgkmcnt(6)\n\t" \
        "v_mfma_f32_16x16x32_bf16 v[196:199], v[60:63], v[156:159], v[196:199]\n\tv_cvt_pk_bf16_f32 v216, v208, v209\n\tv_cvt_pk_bf16_f32 v217, v210, v211\n\tv_cvt_pk_bf16_f32 v218, v212, v213\n\t" \
        "v_cvt_pk_bf16_f32 v219, v214, v215\n\tbuffer_store_dwordx4 v[216:219], %[st], %[rw], %[sp] offen sc1\n\ts_add_u32 %[sp], %[sp], 32768\n\t" \
        "v_mfma_f32_16x16x32_bf16 v[200:203], v[168:171], v[100:103], v[200:203]\n\tv_mfma_f32_16x16x32_bf16 v[204:207], v[172:175], v[100:103], v[204:207]\n\t" \
        "v_mfma_f32_16x16x32_bf16 v[200:203], v[176:179], v[104:107], v[200:203]\n\tv_mfma_f32_16x16x32_bf16 v[204:207], v[180:183], v[104:107], v[204:207]\n\t" \
        "v_mfma_f32_16x16x32_bf16 v[200:203], v[184:187], v[108:111], v[200:203]\n\tv_mfma_f32_16x16x32_bf16 v[204:207], v[188:191], v[108:111], v[204:207]\n\t" \
        "v_mfma_f32_16x16x32_bf16 v[208:211], v[160:163], v[112:115], 0\n\tv_mfma_f32_16x16x32_bf16 v[212:215], v[164:167], v[112:115], 0\n\tv_cvt_pk_bf16_f32 v220, v192, v193\n\t" \
        "v_cvt_pk_bf16_f32 v221, v194, v195\n\tv_cvt_pk_bf16_f32 v222, v196, v197\n\tv_cvt_pk_bf16_f32 v223, v198, v199\n\tds_write_b64 %[vw], v[220:221]\n\t" \
        "ds_write_b64 %[vw], v[222:223] offset:1024\n\tv_xor_b32 %[vw], 0x2000, %[vw]\n\tv_mfma_f32_16x16x32_bf16 v[208:211], v[168:171], v[116:119], v[208:211]\n\t" \
        "v_mfma_f32_16x16x32_bf16 v[212:215], v[172:175], v[116:119], v[212:215]\n\tv_mfma_f32_16x16x32_bf16 v[208:211], v[176:179], v[120:123], v[208:211]\n\t" \
        "v_mfma_f32_16x16x32_bf16 v[212:215], v[180:183], v[120:123], v[212:215]\n\tv_cvt_pk_bf16_f32 v216, v200, v201\n\tv_cvt_pk_bf16_f32 v217, v202, v203\n\tv_cvt_pk_bf16_f32 v218, v204, v205\n\t" \
        "v_cvt_pk_bf16_f32 v219, v206, v207\n\tbuffer_store_dwordx4 v[216:219], %[st], %[rw], %[sp] offen sc1\n\ts_add_u32 %[sp], %[sp], 32768\n\ts_add_u32 %[n], %[n], 1\n\ts_cmp_lt_u32 %[n], 64\n\t" \
        "v_mfma_f32_16x16x32_bf16 v[208:211], v[184:187], v[124:127], v[208:211]\n\tv_mfma_f32_16x16x32_bf16 v[212:215], v[188:191], v[124:127], v[212:215]\n\ts_cbranch_scc1 1b\n\t" \
        "s_mov_b32 m0, %[keep]\n\ts_waitcnt lgkmcnt(0)\n\ts_nop 7\n\tv_cvt_pk_bf16_f32 v216, v208, v209\n\tv_cvt_pk_bf16_f32 v217, v210, v211\n\tv_cvt_pk_bf16_f32 v218, v212, v213\n\t" \
        "v_cvt_pk_bf16_f32 v219, v214, v215\n\tbuffer_store_dwordx4 v[216:219], %[st], %[rw], %[sp] offen sc1\n\ts_add_u32 %[sp], %[sp], 950272\n\ts_branch 5f\n\t4:\ns_waitcnt vmcnt(0)\n\t" \
        "s_sub_u32 %[t1], %[n], 1\n\ts_branch 3b\n\t5:\n"
#define ASM_OPROJ \
        "ds_read_b128 v[128:131], %[xa] offset:0\n\tds_read_b128 v[132:135], %[xa] offset:1024\n\tds_read_b64 v[168:169], %[fr]\n\tds_read_b128 v[136:139], %[xa] offset:2048\n\t" \
        "ds_read_b128 v[140:143], %[xa] offset:3072\n\tds_read_b128 v[144:147], %[xa] offset:4096\n\tds_read_b128 v[148:151], %[xa] offset:5120\n\ts_mov_b32 %[keep], m0\n\ts_mov_b64 vcc, %[em]\n\t" \
        "s_cbranch_vccz 1f\n\tv_mov_b32 v172, 1\n\tv_mov_b32 v173, 0\n\tv_mov_b32 v174, %[dr]\n\ts_cmp_lt_i32 %[dr], 0\n\ts_cselect_b64 exec, 0, 1\n\tglobal_atomic_add v174, v172, %[fin]\n\t" \
        "s_mov_b64 exec, 1\n\tglobal_atomic_add v170, v173, v172, %[qu] sc0\n\tglobal_load_dword v171, v173, %[li] sc1\n\ts_mov_b64 exec, -1\n\t1:\ns_waitcnt lgkmcnt(6)\n\t" \
        "v_mfma_f32_16x16x32_bf16 v[160:163], v[0:3], v[128:131], 0\n\tds_read_b128 v[152:155], %[xa] offset:6144\n\tds_read_b128 v[156:159], %[xa] offset:7168\n\ts_waitcnt lgkmcnt(7)\n\t" \
        "v_mfma_f32_16x16x32_bf16 v[164:167], v[0:3], v[132:135], 0\n\ts_waitcnt lgkmcnt(5)\n\tv_mfma_f32_16x16x32_bf16 v[160:163], v[4:7], v[136:139], v[160:163]\n\t" \
        "ds_read_b128 v[128:131], %[xa] offset:8192\n\tds_read_b128 v[132:135], %[xa] offset:9216\n\ts_waitcnt lgkmcnt(6)\n\tv_mfma_f32_16x16x32_bf16 v[164:167], v[4:7], v[140:143], v[164:167]\n\t" \
        "v_readfirstlane_b32 %[pos], v168\n\tv_readfirstlane_b32 %[e], v169\n\ts_mov_b32 m0, %[lb]\n\ts_waitcnt lgkmcnt(5)\n\tv_mfma_f32_16x16x32_bf16 v[160:163], v[8:11], v[144:147], v[160:163]\n\t" \
        "ds_read_b128 v[136:139], %[xa] offset:10240\n\tds_read_b128 v[140:143], %[xa] offset:11264\n\ts_waitcnt lgkmcnt(6)\n\tv_mfma_f32_16x16x32_bf16 v[164:167], v[8:11], v[148:151], v[164:167]\n\t" \
        "s_lshr_b32 %[nx], %[pos], 3\n\ts_lshl_b32 %[nx], %[nx], 2\n\ts_lshr_b32 %[e], %[e], %[nx]\n\ts_and_b32 %[e], %[e], 15\n\ts_cmp_lt_u32 %[pos], 64\n\ts_cselect_b32 %[e], %[e], 0\n\t" \
        "s_lshl_b32 %[nx], %[e], 3\n\ts_and_b32 %[so], %[pos], 7\n\ts_add_u32 %[nx], %[nx], %[so]\n\ts_sub_u32 %[nx], %[nx], 8\n\ts_cselect_b32 %[nx], %[ch], %[nx]\n\ts_lshl_b32 %[so], %[nx], 18\n\t" \
        "s_add_u32 %[so], %[so], %[sb]\n\ts_waitcnt lgkmcnt(5)\n\tv_mfma_f32_16x16x32_bf16 v[160:163], v[12:15], v[152:155], v[160:163]\n\tds_read_b128 v[144:147], %[xa] offset:12288\n\t" \
        "ds_read_b128 v[148:151], %[xa] offset:13312\n\ts_waitcnt lgkmcnt(6)\n\tv_mfma_f32_16x16x32_bf16 v[164:167], v[12:15], v[156:159], v[164:167]\n\t" \
        "buffer_load_dwordx4 %[dv], %[ra], %[so] offen lds\n\ts_add_u32 m0, m0, 0x2000\n\ts_add_u32 %[so], %[so], 0x2000\n\ts_waitcnt lgkmcnt(5)\n\t" \
        "v_mfma_f32_16x16x32_bf16 v[160:163], v[16:19], v[128:131], v[160:163]\n\tds_read_b128 v[152:155], %[xa] offset:14336\n\tds_read_b128 v[156:159], %[xa] offset:15360\n\t" \
        "s_waitcnt lgkmcnt(6)\n\tv_mfma_f32_16x16x32_bf16 v[164:167], v[16:19], v[132:135], v[164:167]\n\ts_waitcnt lgkmcnt(5)\n\t" \
        "v_mfma_f32_16x16x32_bf16 v[160:163], v[20:23], v[136:139], v[160:163]\n\tds_read_b128 v[128:131], %[xa] offset:16384\n\tds_read_b128 v[132:135], %[xa] offset:17408\n\t" \
        "s_waitcnt lgkmcnt(6)\n\tv_mfma_f32_16x16x32_bf16 v[164:167], v[20:23], v[140:143], v[164:167]\n\ts_waitcnt lgkmcnt(5)\n\t" \
        "v_mfma_f32_16x16x32_bf16 v[160:163], v[24:27], v[144:147], v[160:163]\n\tds_read_b128 v[136:139], %[xa] offset:18432\n\tds_read_b128 v[140:143], %[xa] offset:19456\n\t" \
        "s_waitcnt lgkmcnt(6)\n\tv_mfma_f32_16x16x32_bf16 v[164:167], v[24:27], v[148:151], v[164:167]\n\tbuffer_load_dwordx4 %[dv], %[ra], %[so] offen lds\n\ts_add_u32 m0, m0, 0x2000\n\t" \
        "s_add_u32 %[so], %[so], 0x2000\n\ts_waitcnt lgkmcnt(5)\n\tv_mfma_f32_16x16x32_bf16 v[160:163], v[28:31], v[152:155], v[160:163]\n\tds_read_b128 v[144:147], %[xa] offset:20480\n\t" \
        "ds_read_b128 v[148:151], %[xa] offset:21504\n\ts_waitcnt lgkmcnt(6)\n\tv_mfma_f32_16x16x32_bf16 v[164:167], v[28:31], v[156:159], v[164:167]\n\ts_waitcnt lgkmcnt(5)\n\t" \
        "v_mfma_f32_16x16x32_bf16 v[160:163], v[32:35], v[128:131], v[160:163]\n\tds_read_b128 v[152:155], %[xa] offset:22528\n\tds_read_b128 v[156:159], %[xa] offset:23552\n\t" \
        "s_waitcnt lgkmcnt(6)\n\tv_mfma_f32_16x16x32_bf16 v[164:167], v[32:35], v[132:135], v[164:167]\n\ts_waitcnt lgkmcnt(5)\n\t" \
        "v_mfma_f32_16x16x32_bf16 v[160:163], v[36:39], v[136:139], v[160:163]\n\tds_read_b128 v[128:131], %[xa] offset:24576\n\tds_read_b128 v[132:135], %[xa] offset:25600\n\t" \
        "s_waitcnt lgkmcnt(6)\n\tv_mfma_f32_16x16x32_bf16 v[164:167], v[36:39], v[140:143], v[164:167]\n\tbuffer_load_dwordx4 %[dv], %[ra], %[so] offen lds\n\ts_add_u32 m0, m0, 0x2000\n\t" \
        "s_add_u32 %[so], %[so], 0x2000\n\ts_waitcnt lgkmcnt(5)\n\tv_mfma_f32_16x16x32_bf16 v[160:163], v[40:43], v[144:147], v[160:163]\n\tds_read_b128 v[136:139], %[xa] offset:26624\n\t" \
        "ds_read_b128 v[140:143], %[xa] offset:27648\n\ts_waitcnt lgkmcnt(6)\n\tv_mfma_f32_16x16x32_bf16 v[164:167], v[40:43], v[148:151], v[164:167]\n\ts_waitcnt lgkmcnt(5)\n\t" \
        "v_mfma_f32_16x16x32_bf16 v[160:163], v[44:47], v[152:155], v[160:163]\n\tds_read_b128 v[144:147], %[xa] offset:28672\n\tds_read_b128 v[148:151], %[xa] offset:29696\n\t" \
        "s_waitcnt lgkmcnt(6)\n\tv_mfma_f32_16x16x32_bf16 v[164:167], v[44:47], v[156:159], v[164:167]\n\ts_waitcnt lgkmcnt(5)\n\t" \
        "v_mfma_f32_16x16x32_bf16 v[160:163], v[48:51], v[128:131], v[160:163]\n\tds_read_b128 v[152:155], %[xa] offset:30720\n\tds_read_b128 v[156:159], %[xa] offset:31744\n\t" \
        "s_waitcnt lgkmcnt(6)\n\tv_mfma_f32_16x16x32_bf16 v[164:167], v[48:51], v[132:135], v[164:167]\n\tbuffer_load_dwordx4 %[dv], %[ra], %[so] offen lds\n\ts_add_u32 m0, m0, 0x2000\n\t" \
        "s_add_u32 %[so], %[so], 0x2000\n\ts_waitcnt lgkmcnt(5)\n\tv_mfma_f32_16x16x32_bf16 v[160:163], v[52:55], v[136:139], v[160:163]\n\tds_read_b128 v[128:131], %[xa] offset:32768\n\t" \
        "ds_read_b128 v[132:135], %[xa] offset:33792\n\ts_waitcnt lgkmcnt(6)\n\tv_mfma_f32_16x16x32_bf16 v[164:167], v[52:55], v[140:143], v[164:167]\n\ts_waitcnt lgkmcnt(5)\n\t" \
        "v_mfma_f32_16x16x32_bf16 v[160:163], v[56:59], v[144:147], v[160:163]\n\tds_read_b128 v[136:139], %[xa] offset:34816\n\tds_read_b128 v[140:143], %[xa] offset:35840\n\t" \
        "s_waitcnt lgkmcnt(6)\n\tv_mfma_f32_16x16x32_bf16 v[164:167], v[56:59], v[148:151], v[164:167]\n\ts_waitcnt lgkmcnt(5)\n\t" \
        "v_mfma_f32_16x16x32_bf16 v[160:163], v[60:63], v[152:155], v[160:163]\n\tds_read_b128 v[144:147], %[xa] offset:36864\n\tds_read_b128 v[148:151], %[xa] offset:37888\n\t" \
        "s_waitcnt lgkmcnt(6)\n\tv_mfma_f32_16x16x32_bf16 v[164:167], v[60:63], v[156:159], v[164:167]\n\tbuffer_load_dwordx4 %[dv], %[ra], %[so] offen lds\n\ts_add_u32 m0, m0, 0x2000\n\t" \
        "s_add_u32 %[so], %[so], 0x2000\n\ts_waitcnt lgkmcnt(5)\n\tv_mfma_f32_16x16x32_bf16 v[160:163], v[64:67], v[128:131], v[160:163]\n\tds_read_b128 v[152:155], %[xa] offset:38912\n\t" \
        "ds_read_b128 v[156:159], %[xa] offset:39936\n\ts_waitcnt lgkmcnt(6)\n\tv_mfma_f32_16x16x32_bf16 v[164:167], v[64:67], v[132:135], v[164:167]\n\ts_waitcnt lgkmcnt(5)\n\t" \
        "v_mfma_f32_16x16x32_bf16 v[160:163], v[68:71], v[136:139], v[160:163]\n\tds_read_b128 v[128:131], %[xa] offset:40960\n\tds_read_b128 v[132:135], %[xa] offset:41984\n\t" \
        "s_waitcnt lgkmcnt(6)\n\tv_mfma_f32_16x16x32_bf16 v[164:167], v[68:71], v[140:143], v[164:167]\n\ts_waitcnt lgkmcnt(5)\n\t" \
        "v_mfma_f32_16x16x32_bf16 v[160:163], v[72:75], v[144:147], v[160:163]\n\tds_read_b128 v[136:139], %[xa] offset:43008\n\tds_read_b128 v[140:143], %[xa] offset:44032\n\t" \
        "s_waitcnt lgkmcnt(6)\n\tv_mfma_f32_16x16x32_bf16 v[164:167], v[72:75], v[148:151], v[164:167]\n\tbuffer_load_dwordx4 %[dv], %[ra], %[so] offen lds\n\ts_add_u32 m0, m0, 0x2000\n\t" \
        "s_add_u32 %[so], %[so], 0x2000\n\ts_waitcnt lgkmcnt(5)\n\tv_mfma_f32_16x16x32_bf16 v[160:163], v[76:79], v[152:155], v[160:163]\n\tds_read_b128 v[144:147], %[xa] offset:45056\n\t" \
        "ds_read_b128 v[148:151], %[xa] offset:46080\n\ts_waitcnt lgkmcnt(6)\n\tv_mfma_f32_16x16x32_bf16 v[164:167], v[76:79], v[156:159], v[164:167]\n\ts_waitcnt lgkmcnt(5)\n\t" \
        "v_mfma_f32_16x16x32_bf16 v[160:163], v[80:83], v[128:131], v[160:163]\n\tds_read_b128 v[152:155], %[xa] offset:47104\n\tds_read_b128 v[156:159], %[xa] offset:48128\n\t" \
        "s_waitcnt lgkmcnt(6)\n\tv_mfma_f32_16x16x32_bf16 v[164:167], v[80:83], v[132:135], v[164:167]\n\ts_waitcnt lgkmcnt(5)\n\t" \
        "v_mfma_f32_16x16x32_bf16 v[160:163], v[84:87], v[136:139], v[160:163]\n\tds_read_b128 v[128:131], %[xa] offset:49152\n\tds_read_b128 v[132:135], %[xa] offset:50176\n\t" \
        "s_waitcnt lgkmcnt(6)\n\tv_mfma_f32_16x16x32_bf16 v[164:167], v[84:87], v[140:143], v[164:167]\n\ts_cmp_lt_u32 %[kq], 3\n\ts_cbranch_scc1 2f\n\t" \
        "buffer_load_dwordx4 %[dv], %[ra], %[so] offen lds\n\t2:\ns_cbranch_vccz 4f\n\ts_waitcnt vmcnt(6)\n\ts_mov_b64 exec, 1\n\tds_write_b64 %[fw], v[170:171]\n\ts_mov_b64 exec, -1\n\t" \
        "s_waitcnt lgkmcnt(0)\n\t4:\ns_waitcnt lgkmcnt(5)\n\tv_mfma_f32_16x16x32_bf16 v[160:163], v[88:91], v[144:147], v[160:163]\n\tds_read_b128 v[136:139], %[xa] offset:51200\n\t" \
        "ds_read_b128 v[140:143], %[xa] offset:52224\n\ts_waitcnt lgkmcnt(6)\n\tv_mfma_f32_16x16x32_bf16 v[164:167], v[88:91], v[148:151], v[164:167]\n\ts_waitcnt lgkmcnt(5)\n\t" \
        "v_mfma_f32_16x16x32_bf16 v[160:163], v[92:95], v[152:155], v[160:163]\n\tds_read_b128 v[144:147], %[xa] offset:53248\n\tds_read_b128 v[148:151], %[xa] offset:54272\n\t" \
        "s_waitcnt lgkmcnt(6)\n\tv_mfma_f32_16x16x32_bf16 v[164:167], v[92:95], v[156:159], v[164:167]\n\ts_lshl_b32 %[so], %[ch], 16\n\ts_cmp_lt_u32 %[kq], 3\n\ts_cbranch_scc1 3f\n\t" \
        "s_waitcnt lgkmcnt(5)\n\tv_mfma_f32_16x16x32_bf16 v[160:163], v[96:99], v[128:131], v[160:163]\n\tds_read_b128 v[152:155], %[xa] offset:55296\n\t" \
        "ds_read_b128 v[156:159], %[xa] offset:56320\n\ts_waitcnt lgkmcnt(6)\n\tv_mfma_f32_16x16x32_bf16 v[164:167], v[96:99], v[132:135], v[164:167]\n\ts_waitcnt lgkmcnt(5)\n\t" \
        "v_mfma_f32_16x16x32_bf16 v[160:163], v[100:103], v[136:139], v[160:163]\n\ts_waitcnt lgkmcnt(4)\n\tv_mfma_f32_16x16x32_bf16 v[164:167], v[100:103], v[140:143], v[164:167]\n\t" \
        "s_waitcnt lgkmcnt(3)\n\tv_mfma_f32_16x16x32_bf16 v[160:163], v[104:107], v[144:147], v[160:163]\n\ts_waitcnt lgkmcnt(2)\n\t" \
        "v_mfma_f32_16x16x32_bf16 v[164:167], v[104:107], v[148:151], v[164:167]\n\ts_waitcnt lgkmcnt(1)\n\tv_mfma_f32_16x16x32_bf16 v[160:163], v[108:111], v[152:155], v[160:163]\n\t" \
        "s_waitcnt lgkmcnt(0)\n\tv_mfma_f32_16x16x32_bf16 v[164:167], v[108:111], v[156:159], v[164:167]\n\t3:\ns_mov_b32 m0, %[keep]\n\ts_nop 6\n\t" \
        "buffer_store_dwordx4 v[160:163], %[co], %[rc], %[so] offen sc1\n\tbuffer_store_dwordx4 v[164:167], %[co], %[rc], %[so] offen offset:1024 sc1\n\ts_waitcnt vmcnt(2) lgkmcnt(0)\n\t"
#define ASM_OPRE \
        "s_waitcnt vmcnt(8)\n\ts_mov_b32 %[keep], m0\n\tv_mov_b32 v172, 2\n\tv_mov_b32 v173, 0\n\ts_mov_b64 exec, %[em]\n\tglobal_atomic_add v168, v173, v172, %[qu] sc0\n\t" \
        "global_load_dword v169, v173, %[li] sc1\n\ts_mov_b64 exec, -1\n\ts_mov_b32 %[so], %[wb]\n\tbuffer_load_dwordx4 v[0:3], %[dv], %[rw], %[so] offen\n\t" \
        "buffer_load_dwordx4 v[4:7], %[dv], %[rw], %[so] offen offset:1024\n\tbuffer_load_dwordx4 v[8:11], %[dv], %[rw], %[so] offen offset:2048\n\t" \
        "buffer_load_dwordx4 v[12:15], %[dv], %[rw], %[so] offen offset:3072\n\ts_add_u32 %[so], %[so], 0x1000\n\tbuffer_load_dwordx4 v[16:19], %[dv], %[rw], %[so] offen\n\t" \
        "buffer_load_dwordx4 v[20:23], %[dv], %[rw], %[so] offen offset:1024\n\tbuffer_load_dwordx4 v[24:27], %[dv], %[rw], %[so] offen offset:2048\n\t" \
        "buffer_load_dwordx4 v[28:31], %[dv], %[rw], %[so] offen offset:3072\n\ts_add_u32 %[so], %[so], 0x1000\n\tbuffer_load_dwordx4 v[32:35], %[dv], %[rw], %[so] offen\n\t" \
        "buffer_load_dwordx4 v[36:39], %[dv], %[rw], %[so] offen offset:1024\n\tbuffer_load_dwordx4 v[40:43], %[dv], %[rw], %[so] offen offset:2048\n\t" \
        "buffer_load_dwordx4 v[44:47], %[dv], %[rw], %[so] offen offset:3072\n\ts_add_u32 %[so], %[so], 0x1000\n\tbuffer_load_dwordx4 v[48:51], %[dv], %[rw], %[so] offen\n\t" \
        "buffer_load_dwordx4 v[52:55], %[dv], %[rw], %[so] offen offset:1024\n\tbuffer_load_dwordx4 v[56:59], %[dv], %[rw], %[so] offen offset:2048\n\t" \
        "buffer_load_dwordx4 v[60:63], %[dv], %[rw], %[so] offen offset:3072\n\ts_add_u32 %[so], %[so], 0x1000\n\tbuffer_load_dwordx4 v[64:67], %[dv], %[rw], %[so] offen\n\t" \
        "buffer_load_dwordx4 v[68:71], %[dv], %[rw], %[so] offen offset:1024\n\tbuffer_load_dwordx4 v[72:75], %[dv], %[rw], %[so] offen offset:2048\n\t" \
        "buffer_load_dwordx4 v[76:79], %[dv], %[rw], %[so] offen offset:3072\n\ts_add_u32 %[so], %[so], 0x1000\n\tbuffer_load_dwordx4 v[80:83], %[dv], %[rw], %[so] offen\n\t" \
        "buffer_load_dwordx4 v[84:87], %[dv], %[rw], %[so] offen offset:1024\n\tbuffer_load_dwordx4 v[88:91], %[dv], %[rw], %[so] offen offset:2048\n\t" \
        "buffer_load_dwordx4 v[92:95], %[dv], %[rw], %[so] offen offset:3072\n\ts_add_u32 %[so], %[so], 0x1000\n\tbuffer_load_dwordx4 v[96:99], %[dv], %[rw], %[so] offen\n\t" \
        "buffer_load_dwordx4 v[100:103], %[dv], %[rw], %[so] offen offset:1024\n\tbuffer_load_dwordx4 v[104:107], %[dv], %[rw], %[so] offen offset:2048\n\t" \
        "buffer_load_dwordx4 v[108:111], %[dv], %[rw], %[so] offen offset:3072\n\ts_cmp_eq_u32 %[rem], 0\n\ts_cbranch_scc1 1f\n\ts_barrier\n\t1:\ns_waitcnt vmcnt(28)\n\tv_add_u32 v168, 1, v168\n\t" \
        "v_mov_b32 v172, 1\n\ts_mov_b64 exec, %[em]\n\tds_write_b64 %[fr], v[168:169]\n\ts_mov_b64 exec, -1\n\ts_waitcnt lgkmcnt(0)\n\ts_barrier\n\tds_read_b64 v[168:169], %[fr]\n\t" \
        "s_mov_b64 exec, %[ep]\n\tglobal_atomic_add %[c], v173, v172, %[qc] sc0\n\ts_mov_b64 exec, -1\n\ts_mov_b32 m0, %[lb]\n\ts_waitcnt lgkmcnt(0)\n\tv_readfirstlane_b32 %[pos], v168\n\t" \
        "v_readfirstlane_b32 %[e], v169\n\ts_nop 0\n\ts_sub_u32 %[pos], %[pos], 1\n\ts_lshr_b32 %[nx], %[pos], 3\n\ts_lshl_b32 %[nx], %[nx], 2\n\ts_lshr_b32 %[e], %[e], %[nx]\n\t" \
        "s_and_b32 %[e], %[e], 15\n\ts_cmp_lt_u32 %[pos], 64\n\ts_cselect_b32 %[e], %[e], 0\n\ts_lshl_b32 %[nx], %[e], 3\n\ts_and_b32 %[so], %[pos], 7\n\ts_add_u32 %[nx], %[nx], %[so]\n\t" \
        "s_sub_u32 %[nx], %[nx], 8\n\ts_cbranch_scc1 2f\n\ts_lshl_b32 %[so], %[nx], 18\n\ts_add_u32 %[so], %[so], %[sb]\n\tbuffer_load_dwordx4 %[dv], %[ra], %[so] offen lds\n\t" \
        "s_add_u32 m0, m0, 0x2000\n\ts_add_u32 %[so], %[so], 0x2000\n\tbuffer_load_dwordx4 %[dv], %[ra], %[so] offen lds\n\ts_add_u32 m0, m0, 0x2000\n\ts_add_u32 %[so], %[so], 0x2000\n\t" \
        "buffer_load_dwordx4 %[dv], %[ra], %[so] offen lds\n\ts_add_u32 m0, m0, 0x2000\n\ts_add_u32 %[so], %[so], 0x2000\n\tbuffer_load_dwordx4 %[dv], %[ra], %[so] offen lds\n\t" \
        "s_add_u32 m0, m0, 0x2000\n\ts_add_u32 %[so], %[so], 0x2000\n\tbuffer_load_dwordx4 %[dv], %[ra], %[so] offen lds\n\ts_add_u32 m0, m0, 0x2000\n\ts_add_u32 %[so], %[so], 0x2000\n\t" \
        "buffer_load_dwordx4 %[dv], %[ra], %[so] offen lds\n\ts_add_u32 m0, m0, 0x2000\n\ts_add_u32 %[so], %[so], 0x2000\n\ts_cmp_lt_u32 %[kq], 3\n\ts_cbranch_scc1 2f\n\t" \
        "buffer_load_dwordx4 %[dv], %[ra], %[so] offen lds\n\t2:\ns_mov_b32 m0, %[keep]\n\ts_waitcnt vmcnt(0)\n\t"
#define ASM_HK \
        "s_mov_b32 %[cnt], 0\n\t1:\nv_mov_b64 v[168:169], 0\n\tv_mov_b64 v[170:171], 0\n\tv_mov_b64 v[172:173], 0\n\tv_mov_b64 v[174:175], 0\n\ts_waitcnt lgkmcnt(0)\n\ts_barrier\n\ts_barrier\n\t" \
        "s_nop 5\n\tds_read_b128 v[136:139], %[ma]\n\tds_read_b128 v[140:143], %[ma] offset:1024\n\tds_read_b128 v[144:147], %[ma] offset:2048\n\tds_read_b128 v[148:151], %[ma] offset:3072\n\t" \
        "ds_read_b128 v[152:155], %[ma] offset:4096\n\tds_read_b128 v[156:159], %[ma] offset:5120\n\tds_read_b128 v[160:163], %[ma] offset:6144\n\tds_read_b128 v[164:167], %[ma] offset:7168\n\t" \
        "s_waitcnt lgkmcnt(0)\n\ts_barrier\n\tv_smfmac_f32_16x16x64_bf16 v[168:171], v[136:139], %[w0], %[ix]\n\tv_smfmac_f32_16x16x64_bf16 v[168:171], v[140:143], %[w1], %[ix]\n\t" \
        "v_smfmac_f32_16x16x64_bf16 v[168:171], v[144:147], %[w2], %[ix]\n\tv_smfmac_f32_16x16x64_bf16 v[168:171], v[148:151], %[w3], %[ix]\n\t" \
        "v_smfmac_f32_16x16x64_bf16 v[168:171], v[152:155], %[w4], %[ix]\n\tv_smfmac_f32_16x16x64_bf16 v[168:171], v[156:159], %[w5], %[ix]\n\t" \
        "v_smfmac_f32_16x16x64_bf16 v[168:171], v[160:163], %[w6], %[ix]\n\tv_smfmac_f32_16x16x64_bf16 v[168:171], v[164:167], %[w7], %[ix]\n\ts_nop 2\n\t" \
        "v_smfmac_f32_16x16x64_bf16 v[172:175], v[136:139], %[w8], %[ix]\n\ts_nop 2\n\tv_smfmac_f32_16x16x64_bf16 v[172:175], v[140:143], %[w9], %[ix]\n\tv_permlane32_swap_b32 v168, v170\n\t" \
        "v_smfmac_f32_16x16x64_bf16 v[172:175], v[144:147], %[w10], %[ix]\n\tv_permlane32_swap_b32 v169, v171\n\tv_smfmac_f32_16x16x64_bf16 v[172:175], v[148:151], %[w11], %[ix]\n\t" \
        "v_add_f32 v168, v168, v170\n\tv_smfmac_f32_16x16x64_bf16 v[172:175], v[152:155], %[w12], %[ix]\n\tv_add_f32 v169, v169, v171\n\t" \
        "v_smfmac_f32_16x16x64_bf16 v[172:175], v[156:159], %[w13], %[ix]\n\tv_mul_f32 v168, %[c2], v168\n\tv_smfmac_f32_16x16x64_bf16 v[172:175], v[160:163], %[w14], %[ix]\n\t" \
        "v_mul_f32 v169, %[c2], v169\n\tv_smfmac_f32_16x16x64_bf16 v[172:175], v[164:167], %[w15], %[ix]\n\tv_cvt_pk_bf16_f32 v170, v168, v169\n\tv_cmp_u_f32 vcc, v168, v169\n\ts_nop 0\n\t" \
        "ds_write_b32 %[qw], v170\n\ts_nop 3\n\tv_permlane32_swap_b32 v172, v174\n\tv_permlane32_swap_b32 v173, v175\n\tv_add_f32 v172, v172, v174\n\tv_add_f32 v173, v173, v175\n\t" \
        "v_mul_f32 v172, %[c2], v172\n\tv_mul_f32 v173, %[c2], v173\n\tv_cvt_pk_bf16_f32 v174, v172, v173\n\tds_write_b32 %[qw], v174 offset:1024\n\ts_barrier\n\ts_barrier\n\ts_cbranch_vccnz 5f\n\t" \
        "4:\ns_sleep 1\n\tds_write_b32 %[qw], %[tg] offset:4096\n\tv_mov_b32 v168, -1\n\tds_write_b32 %[qo], v168\n\tds_write_b32 %[qo], v168 offset:1024\n\ts_branch 8f\n\t5:\n" \
        "s_cmp_eq_u32 %[s], 0\n\ts_cbranch_scc1 4b\n\tv_max3_u32 v170, v136, v137, v138\n\tv_max3_u32 v170, v170, v139, v139\n\tv_max3_u32 v170, v140, v141, v170\n\t" \
        "v_max3_u32 v170, v170, v143, v142\n\tv_max3_u32 v170, v144, v145, v170\n\tv_max3_u32 v170, v170, v147, v146\n\tv_max3_u32 v170, v148, v149, v170\n\tv_max3_u32 v170, v170, v151, v150\n\t" \
        "v_max3_u32 v170, v152, v153, v170\n\tv_max3_u32 v170, v170, v155, v154\n\tv_max3_u32 v170, v156, v157, v170\n\tv_max3_u32 v170, v170, v159, v158\n\tv_max3_u32 v170, v160, v161, v170\n\t" \
        "v_max3_u32 v170, v170, v163, v162\n\tv_max3_u32 v170, v164, v165, v170\n\tv_max3_u32 v170, v170, v167, v166\n\tv_cmp_eq_u32 vcc, -1, v170\n\ts_cbranch_vccz 4b\n\t" \
        "s_add_u32 %[cnt], %[cnt], 1\n\ts_cmp_gt_u32 %[cnt], %[cap]\n\ts_cselect_b32 %[cap], 0, %[cap]\n\ts_cbranch_scc1 4b\n\ts_branch 1b\n\t8:\n"
#define ASM_HQ \
        "s_mov_b32 %[cnt], 0\n\t1:\nv_mov_b64 v[168:169], 0\n\tv_mov_b64 v[170:171], 0\n\tv_mov_b64 v[172:173], 0\n\tv_mov_b64 v[174:175], 0\n\ts_waitcnt lgkmcnt(0)\n\ts_barrier\n\t" \
        "ds_read_b128 v[136:139], %[ma]\n\tds_read_b128 v[140:143], %[ma] offset:1024\n\tds_read_b128 v[144:147], %[ma] offset:2048\n\tds_read_b128 v[148:151], %[ma] offset:3072\n\ts_barrier\n\t" \
        "s_waitcnt lgkmcnt(3)\n\tv_smfmac_f32_16x16x64_bf16 v[168:171], v[136:139], %[w0], %[ix]\n\tds_read_b128 v[152:155], %[ma] offset:4096\n\t" \
        "v_smfmac_f32_16x16x64_bf16 v[172:175], v[136:139], %[w8], %[ix]\n\tds_read_b128 v[156:159], %[ma] offset:5120\n\ts_waitcnt lgkmcnt(4)\n\t" \
        "v_smfmac_f32_16x16x64_bf16 v[168:171], v[140:143], %[w1], %[ix]\n\tds_read_b128 v[160:163], %[ma] offset:6144\n\tv_smfmac_f32_16x16x64_bf16 v[172:175], v[140:143], %[w9], %[ix]\n\t" \
        "ds_read_b128 v[164:167], %[ma] offset:7168\n\ts_waitcnt lgkmcnt(5)\n\tv_smfmac_f32_16x16x64_bf16 v[168:171], v[144:147], %[w2], %[ix]\n\ts_waitcnt lgkmcnt(4)\n\t" \
        "v_smfmac_f32_16x16x64_bf16 v[168:171], v[148:151], %[w3], %[ix]\n\ts_waitcnt lgkmcnt(3)\n\tv_smfmac_f32_16x16x64_bf16 v[168:171], v[152:155], %[w4], %[ix]\n\ts_waitcnt lgkmcnt(2)\n\t" \
        "v_smfmac_f32_16x16x64_bf16 v[168:171], v[156:159], %[w5], %[ix]\n\ts_waitcnt lgkmcnt(1)\n\tv_smfmac_f32_16x16x64_bf16 v[168:171], v[160:163], %[w6], %[ix]\n\ts_waitcnt lgkmcnt(0)\n\t" \
        "v_smfmac_f32_16x16x64_bf16 v[168:171], v[164:167], %[w7], %[ix]\n\ts_nop 2\n\tv_smfmac_f32_16x16x64_bf16 v[172:175], v[144:147], %[w10], %[ix]\n\ts_nop 2\n\t" \
        "v_smfmac_f32_16x16x64_bf16 v[172:175], v[148:151], %[w11], %[ix]\n\tv_permlane32_swap_b32 v168, v170\n\tv_smfmac_f32_16x16x64_bf16 v[172:175], v[152:155], %[w12], %[ix]\n\t" \
        "v_permlane32_swap_b32 v169, v171\n\tv_smfmac_f32_16x16x64_bf16 v[172:175], v[156:159], %[w13], %[ix]\n\tv_add_f32 v168, v168, v170\n\t" \
        "v_smfmac_f32_16x16x64_bf16 v[172:175], v[160:163], %[w14], %[ix]\n\tv_add_f32 v169, v169, v171\n\tv_smfmac_f32_16x16x64_bf16 v[172:175], v[164:167], %[w15], %[ix]\n\ts_barrier\n\t" \
        "v_cvt_pk_bf16_f32 v170, v168, v169\n\tv_cmp_u_f32 vcc, v168, v169\n\ts_nop 0\n\tds_write_b32 %[qw], v170\n\ts_nop 2\n\tv_permlane32_swap_b32 v172, v174\n\t" \
        "v_permlane32_swap_b32 v173, v175\n\tv_add_f32 v172, v172, v174\n\tv_add_f32 v173, v173, v175\n\tv_cvt_pk_bf16_f32 v174, v172, v173\n\tds_write_b32 %[qw], v174 offset:1024\n\t" \
        "s_cbranch_vccnz 5f\n\t4:\ns_barrier\n\ts_barrier\n\ts_sleep 1\n\tds_write_b32 %[qw], %[tg] offset:4096\n\tv_mov_b32 v168, -1\n\tds_write_b32 %[qo], v168\n\t" \
        "ds_write_b32 %[qo], v168 offset:1024\n\ts_branch 8f\n\t5:\ns_cmp_eq_u32 %[s], 0\n\ts_cbranch_scc1 4b\n\tv_max3_u32 v170, v136, v137, v138\n\tv_max3_u32 v170, v170, v139, v139\n\t" \
        "v_max3_u32 v170, v140, v141, v170\n\tv_max3_u32 v170, v170, v143, v142\n\tv_max3_u32 v170, v144, v145, v170\n\tv_max3_u32 v170, v170, v147, v146\n\tv_max3_u32 v170, v148, v149, v170\n\t" \
        "v_max3_u32 v170, v170, v151, v150\n\tv_max3_u32 v170, v152, v153, v170\n\tv_max3_u32 v170, v170, v155, v154\n\tv_max3_u32 v170, v156, v157, v170\n\tv_max3_u32 v170, v170, v159, v158\n\t" \
        "v_max3_u32 v170, v160, v161, v170\n\tv_max3_u32 v170, v170, v163, v162\n\tv_max3_u32 v170, v164, v165, v170\n\tv_max3_u32 v170, v170, v167, v166\n\tv_cmp_eq_u32 vcc, -1, v170\n\t" \
        "s_cbranch_vccz 4b\n\ts_add_u32 %[cnt], %[cnt], 1\n\ts_cmp_gt_u32 %[cnt], %[cap]\n\ts_cselect_b32 %[cap], 0, %[cap]\n\ts_cbranch_scc1 4b\n\ts_barrier\n\ts_barrier\n\ts_branch 1b\n\t8:\n"
#define ASM_HP \
        "s_mov_b32 %[cnt], 0\n\ts_mov_b32 %[keep], m0\n\ts_cmp_eq_u32 %[s], 0\n\ts_cbranch_scc1 2f\n\t1:\ns_mov_b32 m0, %[lm]\n\ts_add_u32 %[f2], %[fb], 0x1000\n\t" \
        "buffer_load_dwordx4 %[o], %[rs], %[fb] offen sc1 lds\n\tbuffer_load_dwordx4 %[o], %[rs], %[fb] offen offset:1024 sc1 lds\n\t" \
        "buffer_load_dwordx4 %[o], %[rs], %[fb] offen offset:2048 sc1 lds\n\tbuffer_load_dwordx4 %[o], %[rs], %[fb] offen offset:3072 sc1 lds\n\ts_add_u32 m0, m0, 0x1000\n\ts_nop 0\n\t" \
        "buffer_load_dwordx4 %[o], %[rs], %[f2] offen sc1 lds\n\tbuffer_load_dwordx4 %[o], %[rs], %[f2] offen offset:1024 sc1 lds\n\t" \
        "buffer_load_dwordx4 %[o], %[rs], %[f2] offen offset:2048 sc1 lds\n\tbuffer_load_dwordx4 %[o], %[rs], %[f2] offen offset:3072 sc1 lds\n\t2:\nv_mov_b64 v[168:169], 0\n\t" \
        "v_mov_b64 v[170:171], 0\n\tv_mov_b64 v[172:173], 0\n\tv_mov_b64 v[174:175], 0\n\ts_waitcnt vmcnt(4) lgkmcnt(0)\n\ts_barrier\n\tds_read_b128 v[136:139], %[ma]\n\t" \
        "ds_read_b128 v[140:143], %[ma] offset:1024\n\tds_read_b128 v[144:147], %[ma] offset:2048\n\tds_read_b128 v[148:151], %[ma] offset:3072\n\ts_waitcnt vmcnt(0)\n\ts_memrealtime s[94:95]\n\t" \
        "s_barrier\n\ts_waitcnt lgkmcnt(3)\n\tv_smfmac_f32_16x16x64_bf16 v[168:171], v[136:139], %[w0], %[ix]\n\tds_read_b128 v[152:155], %[ma] offset:4096\n\t" \
        "v_smfmac_f32_16x16x64_bf16 v[172:175], v[136:139], %[w8], %[ix]\n\tds_read_b128 v[156:159], %[ma] offset:5120\n\ts_waitcnt lgkmcnt(4)\n\t" \
        "v_smfmac_f32_16x16x64_bf16 v[168:171], v[140:143], %[w1], %[ix]\n\tds_read_b128 v[160:163], %[ma] offset:6144\n\tv_smfmac_f32_16x16x64_bf16 v[172:175], v[140:143], %[w9], %[ix]\n\t" \
        "ds_read_b128 v[164:167], %[ma] offset:7168\n\ts_waitcnt lgkmcnt(5)\n\tv_smfmac_f32_16x16x64_bf16 v[168:171], v[144:147], %[w2], %[ix]\n\ts_waitcnt lgkmcnt(4)\n\t" \
        "v_smfmac_f32_16x16x64_bf16 v[168:171], v[148:151], %[w3], %[ix]\n\ts_waitcnt lgkmcnt(3)\n\tv_smfmac_f32_16x16x64_bf16 v[168:171], v[152:155], %[w4], %[ix]\n\ts_waitcnt lgkmcnt(2)\n\t" \
        "v_smfmac_f32_16x16x64_bf16 v[168:171], v[156:159], %[w5], %[ix]\n\ts_waitcnt lgkmcnt(1)\n\tv_smfmac_f32_16x16x64_bf16 v[168:171], v[160:163], %[w6], %[ix]\n\ts_waitcnt lgkmcnt(0)\n\t" \
        "v_smfmac_f32_16x16x64_bf16 v[168:171], v[164:167], %[w7], %[ix]\n\ts_nop 2\n\tv_smfmac_f32_16x16x64_bf16 v[172:175], v[144:147], %[w10], %[ix]\n\ts_nop 2\n\t" \
        "v_smfmac_f32_16x16x64_bf16 v[172:175], v[148:151], %[w11], %[ix]\n\tv_permlane32_swap_b32 v168, v170\n\tv_smfmac_f32_16x16x64_bf16 v[172:175], v[152:155], %[w12], %[ix]\n\t" \
        "v_permlane32_swap_b32 v169, v171\n\tv_smfmac_f32_16x16x64_bf16 v[172:175], v[156:159], %[w13], %[ix]\n\tv_add_f32 v168, v168, v170\n\t" \
        "v_smfmac_f32_16x16x64_bf16 v[172:175], v[160:163], %[w14], %[ix]\n\tv_add_f32 v169, v169, v171\n\tv_smfmac_f32_16x16x64_bf16 v[172:175], v[164:167], %[w15], %[ix]\n\ts_barrier\n\t" \
        "v_cmp_u_f32 vcc, v168, v169\n\ts_lshl_b32 %[f2], s94, 6\n\ts_sub_u32 %[tm], %[f2], %[due]\n\ts_cmp_eq_u32 %[cnt], 0\n\ts_cselect_b32 s95, %[k0], %[k1]\n\ts_sub_u32 %[f2], %[f2], s95\n\t" \
        "s_cmp_le_u32 %[tm], %[l1]\n\ts_cselect_b32 %[f2], %[dp], %[f2]\n\tv_mov_b32 v171, %[f2]\n\ts_cbranch_vccnz 6f\n\tbuffer_store_dword v171, %[av], %[rs], %[ao] offen sc1\n\t7:\n" \
        "v_cvt_pk_bf16_f32 v170, v168, v169\n\tv_permlane32_swap_b32 v172, v174\n\tv_permlane32_swap_b32 v173, v175\n\tds_write_b32 %[qw], v170\n\tv_add_f32 v172, v172, v174\n\t" \
        "v_add_f32 v173, v173, v175\n\tv_cvt_pk_bf16_f32 v174, v172, v173\n\tds_write_b32 %[qw], v174 offset:1024\n\ts_cbranch_vccnz 5f\n\t4:\ns_barrier\n\ts_barrier\n\ts_mov_b32 m0, %[keep]\n\t" \
        "s_sleep 1\n\tds_write_b32 %[qw], %[tg] offset:4096\n\tv_mov_b32 v168, -1\n\tds_write_b32 %[qo], v168\n\tds_write_b32 %[qo], v168 offset:1024\n\ts_branch 8f\n\t5:\ns_cmp_eq_u32 %[s], 0\n\t" \
        "s_cbranch_scc1 4b\n\tv_max3_u32 v170, v136, v137, v138\n\tv_max3_u32 v170, v170, v139, v139\n\tv_max3_u32 v170, v140, v141, v170\n\tv_max3_u32 v170, v170, v143, v142\n\t" \
        "v_max3_u32 v170, v144, v145, v170\n\tv_max3_u32 v170, v170, v147, v146\n\tv_max3_u32 v170, v148, v149, v170\n\tv_max3_u32 v170, v170, v151, v150\n\tv_max3_u32 v170, v152, v153, v170\n\t" \
        "v_max3_u32 v170, v170, v155, v154\n\tv_max3_u32 v170, v156, v157, v170\n\tv_max3_u32 v170, v170, v159, v158\n\tv_max3_u32 v170, v160, v161, v170\n\tv_max3_u32 v170, v170, v163, v162\n\t" \
        "v_max3_u32 v170, v164, v165, v170\n\tv_max3_u32 v170, v170, v167, v166\n\tv_cmp_eq_u32 vcc, -1, v170\n\ts_cbranch_vccz 4b\n\ts_add_u32 %[cnt], %[cnt], 1\n\ts_cmp_gt_u32 %[cnt], %[cap]\n\t" \
        "s_cselect_b32 %[cap], 0, %[cap]\n\ts_cbranch_scc1 4b\n\ts_barrier\n\ts_barrier\n\ts_branch 2b\n\t6:\ns_cmp_eq_u32 %[s], 0\n\ts_cbranch_scc1 7b\n\ts_mov_b32 m0, %[lm]\n\t" \
        "s_add_u32 %[f2], %[fb], 0x1000\n\tbuffer_load_dwordx4 %[o], %[rs], %[fb] offen sc1 lds\n\tbuffer_load_dwordx4 %[o], %[rs], %[fb] offen offset:1024 sc1 lds\n\t" \
        "buffer_load_dwordx4 %[o], %[rs], %[fb] offen offset:2048 sc1 lds\n\tbuffer_load_dwordx4 %[o], %[rs], %[fb] offen offset:3072 sc1 lds\n\ts_add_u32 m0, m0, 0x1000\n\ts_nop 0\n\t" \
        "buffer_load_dwordx4 %[o], %[rs], %[f2] offen sc1 lds\n\tbuffer_load_dwordx4 %[o], %[rs], %[f2] offen offset:1024 sc1 lds\n\t" \
        "buffer_load_dwordx4 %[o], %[rs], %[f2] offen offset:2048 sc1 lds\n\tbuffer_load_dwordx4 %[o], %[rs], %[f2] offen offset:3072 sc1 lds\n\ts_branch 7b\n\t8:\n"
#define ASM_H4 \
        "s_mov_b32 %[cnt], 0\n\t1:\nv_mov_b64 v[168:169], 0\n\tv_mov_b64 v[170:171], 0\n\tv_mov_b64 v[172:173], 0\n\tv_mov_b64 v[174:175], 0\n\ts_waitcnt lgkmcnt(0)\n\ts_barrier\n\t" \
        "ds_read_b128 v[136:139], %[ma]\n\tds_read_b128 v[140:143], %[ma] offset:1024\n\tds_read_b128 v[144:147], %[ma] offset:2048\n\tds_read_b128 v[148:151], %[ma] offset:3072\n\ts_barrier\n\t" \
        "s_waitcnt lgkmcnt(3)\n\tv_smfmac_f32_16x16x64_bf16 v[168:171], v[136:139], %[w0], %[ix]\n\tds_read_b128 v[152:155], %[ma] offset:4096\n\t" \
        "v_smfmac_f32_16x16x64_bf16 v[172:175], v[136:139], %[w8], %[ix]\n\tds_read_b128 v[156:159], %[ma] offset:5120\n\ts_waitcnt lgkmcnt(4)\n\t" \
        "v_smfmac_f32_16x16x64_bf16 v[168:171], v[140:143], %[w1], %[ix]\n\tds_read_b128 v[160:163], %[ma] offset:6144\n\tv_smfmac_f32_16x16x64_bf16 v[172:175], v[140:143], %[w9], %[ix]\n\t" \
        "ds_read_b128 v[164:167], %[ma] offset:7168\n\ts_waitcnt lgkmcnt(5)\n\tv_smfmac_f32_16x16x64_bf16 v[168:171], v[144:147], %[w2], %[ix]\n\ts_waitcnt lgkmcnt(4)\n\t" \
        "v_smfmac_f32_16x16x64_bf16 v[168:171], v[148:151], %[w3], %[ix]\n\ts_waitcnt lgkmcnt(3)\n\tv_smfmac_f32_16x16x64_bf16 v[168:171], v[152:155], %[w4], %[ix]\n\ts_waitcnt lgkmcnt(2)\n\t" \
        "v_smfmac_f32_16x16x64_bf16 v[168:171], v[156:159], %[w5], %[ix]\n\ts_waitcnt lgkmcnt(1)\n\tv_smfmac_f32_16x16x64_bf16 v[168:171], v[160:163], %[w6], %[ix]\n\ts_waitcnt lgkmcnt(0)\n\t" \
        "v_smfmac_f32_16x16x64_bf16 v[168:171], v[164:167], %[w7], %[ix]\n\ts_nop 2\n\tv_smfmac_f32_16x16x64_bf16 v[172:175], v[144:147], %[w10], %[ix]\n\ts_nop 2\n\t" \
        "v_smfmac_f32_16x16x64_bf16 v[172:175], v[148:151], %[w11], %[ix]\n\tv_permlane32_swap_b32 v168, v170\n\tv_smfmac_f32_16x16x64_bf16 v[172:175], v[152:155], %[w12], %[ix]\n\t" \
        "v_permlane32_swap_b32 v169, v171\n\tv_smfmac_f32_16x16x64_bf16 v[172:175], v[156:159], %[w13], %[ix]\n\tv_add_f32 v168, v168, v170\n\t" \
        "v_smfmac_f32_16x16x64_bf16 v[172:175], v[160:163], %[w14], %[ix]\n\tv_add_f32 v169, v169, v171\n\tv_smfmac_f32_16x16x64_bf16 v[172:175], v[164:167], %[w15], %[ix]\n\ts_barrier\n\t" \
        "v_cvt_pk_bf16_f32 v170, v168, v169\n\tv_cmp_u_f32 vcc, v168, v169\n\ts_nop 0\n\tds_write_b32 %[qw], v170\n\ts_nop 2\n\tv_permlane32_swap_b32 v172, v174\n\t" \
        "v_permlane32_swap_b32 v173, v175\n\tv_add_f32 v172, v172, v174\n\tv_add_f32 v173, v173, v175\n\tv_cvt_pk_bf16_f32 v174, v172, v173\n\tds_write_b32 %[qw], v174 offset:1024\n\t" \
        "s_cbranch_vccnz 5f\n\t4:\ns_mov_b32 s95, 0\n\ts_nop 12\n\tds_read_b64_tr_b16 v[136:137], %[fr] offset:0\n\tds_read_b64_tr_b16 v[138:139], %[fr] offset:256\n\t" \
        "ds_read_b64_tr_b16 v[140:141], %[fr] offset:512\n\tds_read_b64_tr_b16 v[142:143], %[fr] offset:768\n\tds_read_b64_tr_b16 v[144:145], %[fr] offset:1024\n\t" \
        "ds_read_b64_tr_b16 v[146:147], %[fr] offset:1280\n\tds_read_b64_tr_b16 v[148:149], %[fr] offset:1536\n\tds_read_b64_tr_b16 v[150:151], %[fr] offset:1792\n\ts_nop 15\n\t" \
        "ds_read_b64_tr_b16 v[152:153], %[fr] offset:2048\n\tds_read_b64_tr_b16 v[154:155], %[fr] offset:2304\n\tds_read_b64_tr_b16 v[156:157], %[fr] offset:2560\n\t" \
        "ds_read_b64_tr_b16 v[158:159], %[fr] offset:2816\n\ts_barrier\n\tds_read_b64_tr_b16 v[160:161], %[fr] offset:3072\n\tds_read_b64_tr_b16 v[162:163], %[fr] offset:3328\n\t" \
        "ds_read_b64_tr_b16 v[164:165], %[fr] offset:3584\n\tds_read_b64_tr_b16 v[166:167], %[fr] offset:3840\n\ts_waitcnt lgkmcnt(4)\n\t" \
        "v_mfma_f32_16x16x32_bf16 v[216:219], v[152:155], v[136:139], 0\n\tv_mfma_f32_16x16x32_bf16 v[216:219], v[156:159], v[140:143], v[216:219]\n\ts_waitcnt lgkmcnt(2)\n\t" \
        "v_mfma_f32_16x16x32_bf16 v[216:219], v[160:163], v[144:147], v[216:219]\n\ts_waitcnt lgkmcnt(0)\n\tv_mfma_f32_16x16x32_bf16 v[216:219], v[164:167], v[148:151], v[216:219]\n\ts_nop 4\n\t" \
        "s_barrier\n\tds_read_b128 v[212:215], %[sr]\n\tds_read_b128 v[208:211], %[er]\n\tv_exp_f32 v216, v216\n\tv_exp_f32 v217, v217\n\tv_exp_f32 v218, v218\n\tv_exp_f32 v219, v219\n\t" \
        "s_waitcnt lgkmcnt(1)\n\tv_pk_add_f32 v[212:213], v[212:213], v[214:215]\n\tv_pk_add_f32 v[226:227], v[216:217], v[218:219]\n\tv_pk_add_f32 v[226:227], v[226:227], v[212:213]\n\t" \
        "v_add_f32 v224, v226, v227\n\tv_add_f32 v225, v226, v227\n\ts_nop 1\n\tv_permlane16_swap_b32 v224, v225\n\tv_add_f32 v225, v224, v225\n\tv_rcp_f32 v226, v225\n\t" \
        "v_subrev_u32 v225, %[lo], v225\n\t3:\nv_cmp_lt_u32 vcc, %[sp], v225\n\tv_pk_mul_f32 v[216:217], v[226:227], v[216:217] op_sel_hi:[0,1]\n\t" \
        "v_pk_mul_f32 v[218:219], v[226:227], v[218:219] op_sel_hi:[0,1]\n\ts_waitcnt lgkmcnt(0)\n\tv_pk_mul_f32 v[208:209], v[226:227], v[208:209] op_sel_hi:[0,1]\n\t" \
        "v_pk_mul_f32 v[210:211], v[226:227], v[210:211] op_sel_hi:[0,1]\n\tv_cvt_pk_bf16_f32 v170, v216, |v217|\n\tv_cvt_pk_bf16_f32 v171, v218, |v219|\n\tv_cvt_pk_bf16_f32 v168, v208, |v209|\n\t" \
        "v_cvt_pk_bf16_f32 v169, v210, |v211|\n\ts_cbranch_vccnz 9f\n\tbuffer_store_dwordx2 v[170:171], %[pm], %[rs], %[po] offen sc1\n\t" \
        "buffer_store_dwordx2 v[168:169], %[px], %[rs], %[po] offen sc1\n\tv_mov_b64 v[144:145], -1\n\ts_nop 0\n\tbuffer_store_dwordx2 v[144:145], %[px], %[rs], %[pq] offen sc1\n\t" \
        "buffer_store_dwordx2 v[144:145], %[pm], %[rs], %[pq] offen sc1\n\ts_branch 8f\n\t9:\nds_write_b32 %[qw], %[tg] offset:4096\n\tds_read_b128 v[176:179], %[kx] offset:0\n\t" \
        "ds_read_b128 v[180:183], %[kx] offset:64\n\tds_read_b128 v[184:187], %[kx] offset:2048\n\tds_read_b128 v[188:191], %[kx] offset:2112\n\tds_read_b128 v[192:195], %[kx] offset:4096\n\t" \
        "ds_read_b128 v[196:199], %[kx] offset:4160\n\tds_read_b128 v[200:203], %[kx] offset:6144\n\tds_read_b128 v[204:207], %[kx] offset:6208\n\t6:\nds_read_b32 v220, %[ta]\n\t" \
        "s_sub_u32 %[cn], %[cn], 1\n\ts_waitcnt lgkmcnt(0)\n\tv_cmp_ne_u32 vcc, v220, %[tg]\n\ts_cmp_lt_i32 %[cn], 0\n\ts_cbranch_scc1 7f\n\ts_cbranch_vccnz 6b\n\t7:\n" \
        "ds_read_b64_tr_b16 v[136:137], %[fr] offset:0\n\tds_read_b64_tr_b16 v[138:139], %[fr] offset:256\n\tds_read_b64_tr_b16 v[140:141], %[fr] offset:512\n\t" \
        "ds_read_b64_tr_b16 v[142:143], %[fr] offset:768\n\tds_read_b64_tr_b16 v[144:145], %[fr] offset:1024\n\tds_read_b64_tr_b16 v[146:147], %[fr] offset:1280\n\t" \
        "ds_read_b64_tr_b16 v[148:149], %[fr] offset:1536\n\tds_read_b64_tr_b16 v[150:151], %[fr] offset:1792\n\ts_waitcnt lgkmcnt(0)\n\tds_read_b64_tr_b16 v[152:153], %[fr] offset:2048\n\t" \
        "ds_read_b64_tr_b16 v[154:155], %[fr] offset:2304\n\tds_read_b64_tr_b16 v[156:157], %[fr] offset:2560\n\tds_read_b64_tr_b16 v[158:159], %[fr] offset:2816\n\t" \
        "ds_read_b64_tr_b16 v[160:161], %[fr] offset:3072\n\tds_read_b64_tr_b16 v[162:163], %[fr] offset:3328\n\tds_read_b64_tr_b16 v[164:165], %[fr] offset:3584\n\t" \
        "ds_read_b64_tr_b16 v[166:167], %[fr] offset:3840\n\ts_waitcnt lgkmcnt(0)\n\tv_mfma_f32_16x16x32_bf16 v[208:211], v[176:179], v[136:139], 0\n\t" \
        "v_mfma_f32_16x16x32_bf16 v[212:215], v[180:183], v[136:139], 0\n\tv_mfma_f32_16x16x32_bf16 v[208:211], v[184:187], v[140:143], v[208:211]\n\t" \
        "v_mfma_f32_16x16x32_bf16 v[212:215], v[188:191], v[140:143], v[212:215]\n\tv_mfma_f32_16x16x32_bf16 v[208:211], v[192:195], v[144:147], v[208:211]\n\t" \
        "v_mfma_f32_16x16x32_bf16 v[212:215], v[196:199], v[144:147], v[212:215]\n\tv_mfma_f32_16x16x32_bf16 v[208:211], v[200:203], v[148:151], v[208:211]\n\t" \
        "v_mfma_f32_16x16x32_bf16 v[212:215], v[204:207], v[148:151], v[212:215]\n\tv_mfma_f32_16x16x32_bf16 v[216:219], v[152:155], v[136:139], 0\n\t" \
        "v_mfma_f32_16x16x32_bf16 v[216:219], v[156:159], v[140:143], v[216:219]\n\tv_mfma_f32_16x16x32_bf16 v[216:219], v[160:163], v[144:147], v[216:219]\n\t" \
        "v_mfma_f32_16x16x32_bf16 v[216:219], v[164:167], v[148:151], v[216:219]\n\ts_nop 3\n\tv_mov_b32_dpp v208, v212 quad_perm:[0,1,2,3] row_mask:0xf bank_mask:0xc\n\t" \
        "v_mov_b32_dpp v209, v213 quad_perm:[0,1,2,3] row_mask:0xf bank_mask:0xc\n\tv_mov_b32_dpp v210, v214 quad_perm:[0,1,2,3] row_mask:0xf bank_mask:0xc\n\t" \
        "v_mov_b32_dpp v211, v215 quad_perm:[0,1,2,3] row_mask:0xf bank_mask:0xc\n\tv_mov_b32 v223, 0\n\ts_add_u32 s95, s95, 1\n\ts_cmp_eq_u32 s95, 1\n\ts_cbranch_scc1 10f\n\t" \
        "v_max3_f32 v223, v208, v209, v210\n\tv_max3_f32 v223, v223, v211, v216\n\tv_max3_f32 v223, v223, v217, v218\n\tv_max_f32 v223, v223, v219\n\ts_nop 1\n\t" \
        "v_max_f32_dpp v223, v223, v223 row_ror:8 row_mask:0xf bank_mask:0xf\n\tv_mov_b32 v224, v223\n\ts_nop 1\n\tv_permlane16_swap_b32 v223, v224\n\ts_nop 1\n\tv_max_f32 v223, v223, v224\n\t" \
        "v_mov_b32 v224, v223\n\ts_nop 1\n\tv_permlane32_swap_b32 v223, v224\n\ts_nop 1\n\tv_max_f32 v223, v223, v224\n\t10:\nv_sub_f32 v208, v208, v223\n\tv_sub_f32 v209, v209, v223\n\t" \
        "v_sub_f32 v210, v210, v223\n\tv_sub_f32 v211, v211, v223\n\tv_sub_f32 v216, v216, v223\n\tv_sub_f32 v217, v217, v223\n\tv_sub_f32 v218, v218, v223\n\tv_sub_f32 v219, v219, v223\n\t" \
        "v_exp_f32 v208, v208\n\tv_exp_f32 v209, v209\n\tv_exp_f32 v210, v210\n\tv_exp_f32 v211, v211\n\tv_exp_f32 v216, v216\n\tv_exp_f32 v217, v217\n\tv_exp_f32 v218, v218\n\t" \
        "v_exp_f32 v219, v219\n\tv_add_f32 v224, v208, v209\n\tv_add_f32 v225, v210, v211\n\tv_add_f32 v224, v224, v225\n\ts_nop 1\n\t" \
        "v_add_f32_dpp v225, v224, v224 row_ror:8 row_mask:0xf bank_mask:0xf\n\tv_add_f32_dpp v224, v224, v224 row_ror:8 row_mask:0xf bank_mask:0xf\n\ts_nop 1\n\tv_permlane32_swap_b32 v224, v225\n\t" \
        "s_nop 1\n\tv_pk_add_f32 v[226:227], v[216:217], v[218:219]\n\tv_pk_add_f32 v[226:227], v[226:227], v[224:225]\n\tv_add_f32 v224, v226, v227\n\tv_add_f32 v225, v226, v227\n\ts_nop 1\n\t" \
        "v_permlane16_swap_b32 v224, v225\n\tv_add_f32 v225, v224, v225\n\tv_rcp_f32 v226, v225\n\tv_subrev_u32 v225, %[lo], v225\n\ts_cbranch_scc1 3b\n\tv_mov_b32 v225, 0\n\ts_branch 3b\n\t5:\n" \
        "s_cmp_eq_u32 %[s], 0\n\ts_cbranch_scc1 4b\n\tv_max3_u32 v170, v136, v137, v138\n\tv_max3_u32 v170, v170, v139, v139\n\tv_max3_u32 v170, v140, v141, v170\n\t" \
        "v_max3_u32 v170, v170, v143, v142\n\tv_max3_u32 v170, v144, v145, v170\n\tv_max3_u32 v170, v170, v147, v146\n\tv_max3_u32 v170, v148, v149, v170\n\tv_max3_u32 v170, v170, v151, v150\n\t" \
        "v_max3_u32 v170, v152, v153, v170\n\tv_max3_u32 v170, v170, v155, v154\n\tv_max3_u32 v170, v156, v157, v170\n\tv_max3_u32 v170, v170, v159, v158\n\tv_max3_u32 v170, v160, v161, v170\n\t" \
        "v_max3_u32 v170, v170, v163, v162\n\tv_max3_u32 v170, v164, v165, v170\n\tv_max3_u32 v170, v170, v167, v166\n\tv_cmp_eq_u32 vcc, -1, v170\n\ts_cbranch_vccz 4b\n\t" \
        "s_add_u32 %[cnt], %[cnt], 1\n\ts_cmp_gt_u32 %[cnt], %[cap]\n\ts_cselect_b32 %[cap], 0, %[cap]\n\ts_cbranch_scc1 4b\n\ts_barrier\n\ts_barrier\n\ts_branch 1b\n\t8:\n" \
        "ds_write_b32 %[qw], %[tg] offset:4096\n\tv_mov_b32 v168, -1\n\tds_write_b32 %[qo], v168\n\tds_write_b32 %[qo], v168 offset:1024\n\t"
#define ASM_H5 \
        "ds_read_b128 v[176:179], %[kx] offset:0\n\tds_read_b128 v[180:183], %[kx] offset:2048\n\tds_read_b128 v[184:187], %[kx] offset:4096\n\tds_read_b128 v[188:191], %[kx] offset:6144\n\t" \
        "s_mov_b32 %[cnt], 0\n\t1:\nv_mov_b64 v[168:169], 0\n\tv_mov_b64 v[170:171], 0\n\tv_mov_b64 v[172:173], 0\n\tv_mov_b64 v[174:175], 0\n\ts_waitcnt lgkmcnt(0)\n\ts_barrier\n\t" \
        "ds_read_b128 v[136:139], %[ma]\n\tds_read_b128 v[140:143], %[ma] offset:1024\n\tds_read_b128 v[144:147], %[ma] offset:2048\n\tds_read_b128 v[148:151], %[ma] offset:3072\n\ts_barrier\n\t" \
        "s_waitcnt lgkmcnt(3)\n\tv_smfmac_f32_16x16x64_bf16 v[168:171], v[136:139], %[w0], %[ix]\n\tds_read_b128 v[152:155], %[ma] offset:4096\n\t" \
        "v_smfmac_f32_16x16x64_bf16 v[172:175], v[136:139], %[w8], %[ix]\n\tds_read_b128 v[156:159], %[ma] offset:5120\n\ts_waitcnt lgkmcnt(4)\n\t" \
        "v_smfmac_f32_16x16x64_bf16 v[168:171], v[140:143], %[w1], %[ix]\n\tds_read_b128 v[160:163], %[ma] offset:6144\n\tv_smfmac_f32_16x16x64_bf16 v[172:175], v[140:143], %[w9], %[ix]\n\t" \
        "ds_read_b128 v[164:167], %[ma] offset:7168\n\ts_waitcnt lgkmcnt(5)\n\tv_smfmac_f32_16x16x64_bf16 v[168:171], v[144:147], %[w2], %[ix]\n\ts_waitcnt lgkmcnt(4)\n\t" \
        "v_smfmac_f32_16x16x64_bf16 v[168:171], v[148:151], %[w3], %[ix]\n\ts_waitcnt lgkmcnt(3)\n\tv_smfmac_f32_16x16x64_bf16 v[168:171], v[152:155], %[w4], %[ix]\n\ts_waitcnt lgkmcnt(2)\n\t" \
        "v_smfmac_f32_16x16x64_bf16 v[168:171], v[156:159], %[w5], %[ix]\n\ts_waitcnt lgkmcnt(1)\n\tv_smfmac_f32_16x16x64_bf16 v[168:171], v[160:163], %[w6], %[ix]\n\ts_waitcnt lgkmcnt(0)\n\t" \
        "v_smfmac_f32_16x16x64_bf16 v[168:171], v[164:167], %[w7], %[ix]\n\ts_nop 2\n\tv_smfmac_f32_16x16x64_bf16 v[172:175], v[144:147], %[w10], %[ix]\n\ts_nop 2\n\t" \
        "v_smfmac_f32_16x16x64_bf16 v[172:175], v[148:151], %[w11], %[ix]\n\tv_permlane32_swap_b32 v168, v170\n\tv_smfmac_f32_16x16x64_bf16 v[172:175], v[152:155], %[w12], %[ix]\n\t" \
        "v_permlane32_swap_b32 v169, v171\n\tv_smfmac_f32_16x16x64_bf16 v[172:175], v[156:159], %[w13], %[ix]\n\tv_add_f32 v168, v168, v170\n\t" \
        "v_smfmac_f32_16x16x64_bf16 v[172:175], v[160:163], %[w14], %[ix]\n\tv_add_f32 v169, v169, v171\n\tv_smfmac_f32_16x16x64_bf16 v[172:175], v[164:167], %[w15], %[ix]\n\ts_barrier\n\t" \
        "v_cvt_pk_bf16_f32 v170, v168, v169\n\tv_cmp_u_f32 vcc, v168, v169\n\ts_nop 0\n\tds_write_b32 %[qw], v170\n\ts_nop 2\n\tv_permlane32_swap_b32 v172, v174\n\t" \
        "v_permlane32_swap_b32 v173, v175\n\tv_add_f32 v172, v172, v174\n\tv_add_f32 v173, v173, v175\n\tv_cvt_pk_bf16_f32 v174, v172, v173\n\tds_write_b32 %[qw], v174 offset:1024\n\t" \
        "s_cbranch_vccnz 5f\n\t4:\ns_nop 15\n\ts_nop 0\n\tds_read_b64_tr_b16 v[136:137], %[fr] offset:0\n\tds_read_b64_tr_b16 v[138:139], %[fr] offset:256\n\t" \
        "ds_read_b64_tr_b16 v[140:141], %[fr] offset:512\n\tds_read_b64_tr_b16 v[142:143], %[fr] offset:768\n\tds_read_b64_tr_b16 v[144:145], %[fr] offset:1024\n\t" \
        "ds_read_b64_tr_b16 v[146:147], %[fr] offset:1280\n\tds_read_b64_tr_b16 v[148:149], %[fr] offset:1536\n\tds_read_b64_tr_b16 v[150:151], %[fr] offset:1792\n\ts_waitcnt lgkmcnt(6)\n\t" \
        "v_mfma_f32_16x16x32_bf16 v[208:211], v[176:179], v[136:139], 0\n\ts_waitcnt lgkmcnt(4)\n\tv_mfma_f32_16x16x32_bf16 v[208:211], v[180:183], v[140:143], v[208:211]\n\ts_waitcnt lgkmcnt(2)\n\t" \
        "v_mfma_f32_16x16x32_bf16 v[208:211], v[184:187], v[144:147], v[208:211]\n\ts_waitcnt lgkmcnt(0)\n\tv_mfma_f32_16x16x32_bf16 v[208:211], v[188:191], v[148:151], v[208:211]\n\t" \
        "s_mov_b64 exec, %[em]\n\ts_nop 6\n\tv_exp_f32 v208, v208\n\tv_exp_f32 v209, v209\n\ts_barrier\n\tv_exp_f32 v210, v210\n\tv_exp_f32 v211, v211\n\tv_add_f32 v212, v208, v209\n\t" \
        "v_add_f32 v213, v210, v211\n\tds_write_b128 %[ew], v[208:211]\n\tv_add_f32 v212, v212, v213\n\tds_write_b32 %[sw], v212\n\ts_barrier\n\ts_mov_b64 exec, -1\n\ts_sleep 1\n\t" \
        "ds_write_b32 %[qw], %[tg] offset:4096\n\tv_mov_b32 v168, -1\n\tds_write_b32 %[qo], v168\n\tds_write_b32 %[qo], v168 offset:1024\n\tds_write_b32 %[so], v168\n\ts_branch 8f\n\t5:\n" \
        "s_cmp_eq_u32 %[s], 0\n\ts_cbranch_scc1 4b\n\tv_max3_u32 v170, v136, v137, v138\n\tv_max3_u32 v170, v170, v139, v139\n\tv_max3_u32 v170, v140, v141, v170\n\t" \
        "v_max3_u32 v170, v170, v143, v142\n\tv_max3_u32 v170, v144, v145, v170\n\tv_max3_u32 v170, v170, v147, v146\n\tv_max3_u32 v170, v148, v149, v170\n\tv_max3_u32 v170, v170, v151, v150\n\t" \
        "v_max3_u32 v170, v152, v153, v170\n\tv_max3_u32 v170, v170, v155, v154\n\tv_max3_u32 v170, v156, v157, v170\n\tv_max3_u32 v170, v170, v159, v158\n\tv_max3_u32 v170, v160, v161, v170\n\t" \
        "v_max3_u32 v170, v170, v163, v162\n\tv_max3_u32 v170, v164, v165, v170\n\tv_max3_u32 v170, v170, v167, v166\n\tv_cmp_eq_u32 vcc, -1, v170\n\ts_cbranch_vccz 4b\n\t" \
        "s_add_u32 %[cnt], %[cnt], 1\n\ts_cmp_gt_u32 %[cnt], %[cap]\n\ts_cselect_b32 %[cap], 0, %[cap]\n\ts_cbranch_scc1 4b\n\ts_barrier\n\ts_barrier\n\ts_branch 1b\n\t8:\n"
#define ASM_VV \
        "s_mov_b32 %[cnt], 0\n\t1:\nv_mov_b64 v[168:169], 0\n\tv_mov_b64 v[170:171], 0\n\tv_mov_b64 v[172:173], 0\n\tv_mov_b64 v[174:175], 0\n\ts_waitcnt lgkmcnt(0)\n\ts_barrier\n\t" \
        "ds_read_b128 v[136:139], %[ma]\n\tds_read_b128 v[140:143], %[ma] offset:1024\n\tds_read_b128 v[144:147], %[ma] offset:2048\n\tds_read_b128 v[148:151], %[ma] offset:3072\n\ts_barrier\n\t" \
        "s_waitcnt lgkmcnt(3)\n\tv_smfmac_f32_16x16x64_bf16 v[168:171], v[136:139], %[w0], %[ix]\n\tds_read_b128 v[152:155], %[ma] offset:4096\n\t" \
        "v_smfmac_f32_16x16x64_bf16 v[172:175], v[136:139], %[w8], %[ix]\n\tds_read_b128 v[156:159], %[ma] offset:5120\n\ts_waitcnt lgkmcnt(4)\n\t" \
        "v_smfmac_f32_16x16x64_bf16 v[168:171], v[140:143], %[w1], %[ix]\n\tds_read_b128 v[160:163], %[ma] offset:6144\n\tv_smfmac_f32_16x16x64_bf16 v[172:175], v[140:143], %[w9], %[ix]\n\t" \
        "ds_read_b128 v[164:167], %[ma] offset:7168\n\ts_waitcnt lgkmcnt(5)\n\tv_smfmac_f32_16x16x64_bf16 v[168:171], v[144:147], %[w2], %[ix]\n\ts_waitcnt lgkmcnt(4)\n\t" \
        "v_smfmac_f32_16x16x64_bf16 v[168:171], v[148:151], %[w3], %[ix]\n\ts_waitcnt lgkmcnt(3)\n\tv_smfmac_f32_16x16x64_bf16 v[168:171], v[152:155], %[w4], %[ix]\n\ts_waitcnt lgkmcnt(2)\n\t" \
        "v_smfmac_f32_16x16x64_bf16 v[168:171], v[156:159], %[w5], %[ix]\n\ts_waitcnt lgkmcnt(1)\n\tv_smfmac_f32_16x16x64_bf16 v[168:171], v[160:163], %[w6], %[ix]\n\ts_waitcnt lgkmcnt(0)\n\t" \
        "v_smfmac_f32_16x16x64_bf16 v[168:171], v[164:167], %[w7], %[ix]\n\ts_nop 2\n\tv_smfmac_f32_16x16x64_bf16 v[172:175], v[144:147], %[w10], %[ix]\n\ts_nop 2\n\t" \
        "v_smfmac_f32_16x16x64_bf16 v[172:175], v[148:151], %[w11], %[ix]\n\tv_permlane32_swap_b32 v168, v170\n\tv_smfmac_f32_16x16x64_bf16 v[172:175], v[152:155], %[w12], %[ix]\n\t" \
        "v_permlane32_swap_b32 v169, v171\n\tv_smfmac_f32_16x16x64_bf16 v[172:175], v[156:159], %[w13], %[ix]\n\tv_add_f32 v168, v168, v170\n\t" \
        "v_smfmac_f32_16x16x64_bf16 v[172:175], v[160:163], %[w14], %[ix]\n\tv_add_f32 v169, v169, v171\n\tv_smfmac_f32_16x16x64_bf16 v[172:175], v[164:167], %[w15], %[ix]\n\t" \
        "v_cvt_pk_bf16_f32 v170, v168, v169\n\tv_cmp_u_f32 vcc, v168, v169\n\ts_nop 0\n\tds_write_b32 %[qw], v170\n\ts_nop 3\n\tv_permlane32_swap_b32 v172, v174\n\t" \
        "v_permlane32_swap_b32 v173, v175\n\ts_waitcnt lgkmcnt(0)\n\ts_barrier\n\tv_add_f32 v172, v172, v174\n\tv_add_f32 v173, v173, v175\n\tv_cvt_pk_bf16_f32 v174, v172, v173\n\t" \
        "ds_write_b32 %[qw], v174 offset:1024\n\ts_barrier\n\ts_waitcnt lgkmcnt(0)\n\ts_barrier\n\ts_cbranch_vccnz 5f\n\t4:\ns_branch 8f\n\t5:\ns_cmp_eq_u32 %[s], 0\n\ts_cbranch_scc1 4b\n\t" \
        "v_max3_u32 v170, v136, v137, v138\n\tv_max3_u32 v170, v170, v139, v139\n\tv_max3_u32 v170, v140, v141, v170\n\tv_max3_u32 v170, v170, v143, v142\n\tv_max3_u32 v170, v144, v145, v170\n\t" \
        "v_max3_u32 v170, v170, v147, v146\n\tv_max3_u32 v170, v148, v149, v170\n\tv_max3_u32 v170, v170, v151, v150\n\tv_max3_u32 v170, v152, v153, v170\n\tv_max3_u32 v170, v170, v155, v154\n\t" \
        "v_max3_u32 v170, v156, v157, v170\n\tv_max3_u32 v170, v170, v159, v158\n\tv_max3_u32 v170, v160, v161, v170\n\tv_max3_u32 v170, v170, v163, v162\n\tv_max3_u32 v170, v164, v165, v170\n\t" \
        "v_max3_u32 v170, v170, v167, v166\n\tv_cmp_eq_u32 vcc, -1, v170\n\ts_cbranch_vccz 4b\n\ts_add_u32 %[cnt], %[cnt], 1\n\ts_cmp_gt_u32 %[cnt], %[cap]\n\ts_cselect_b32 %[cap], 0, %[cap]\n\t" \
        "s_cbranch_scc1 4b\n\ts_branch 1b\n\t8:\n"
#define ASM_VP \
        "s_mov_b32 %[cnt], 0\n\ts_mov_b32 %[keep], m0\n\ts_cmp_eq_u32 %[s], 0\n\ts_cbranch_scc1 2f\n\t1:\ns_mov_b32 m0, %[lm]\n\ts_add_u32 %[f2], %[fb], 0x1000\n\t" \
        "buffer_load_dwordx4 %[o], %[rs], %[fb] offen sc1 lds\n\tbuffer_load_dwordx4 %[o], %[rs], %[fb] offen offset:1024 sc1 lds\n\t" \
        "buffer_load_dwordx4 %[o], %[rs], %[fb] offen offset:2048 sc1 lds\n\tbuffer_load_dwordx4 %[o], %[rs], %[fb] offen offset:3072 sc1 lds\n\ts_add_u32 m0, m0, 0x1000\n\ts_nop 0\n\t" \
        "buffer_load_dwordx4 %[o], %[rs], %[f2] offen sc1 lds\n\tbuffer_load_dwordx4 %[o], %[rs], %[f2] offen offset:1024 sc1 lds\n\t" \
        "buffer_load_dwordx4 %[o], %[rs], %[f2] offen offset:2048 sc1 lds\n\tbuffer_load_dwordx4 %[o], %[rs], %[f2] offen offset:3072 sc1 lds\n\t2:\nv_mov_b64 v[168:169], 0\n\t" \
        "v_mov_b64 v[170:171], 0\n\tv_mov_b64 v[172:173], 0\n\tv_mov_b64 v[174:175], 0\n\ts_waitcnt vmcnt(4) lgkmcnt(0)\n\ts_barrier\n\tds_read_b128 v[136:139], %[ma]\n\t" \
        "ds_read_b128 v[140:143], %[ma] offset:1024\n\tds_read_b128 v[144:147], %[ma] offset:2048\n\tds_read_b128 v[148:151], %[ma] offset:3072\n\ts_waitcnt vmcnt(0)\n\ts_barrier\n\t" \
        "s_waitcnt lgkmcnt(3)\n\tv_smfmac_f32_16x16x64_bf16 v[168:171], v[136:139], %[w0], %[ix]\n\tds_read_b128 v[152:155], %[ma] offset:4096\n\t" \
        "v_smfmac_f32_16x16x64_bf16 v[172:175], v[136:139], %[w8], %[ix]\n\tds_read_b128 v[156:159], %[ma] offset:5120\n\ts_waitcnt lgkmcnt(4)\n\t" \
        "v_smfmac_f32_16x16x64_bf16 v[168:171], v[140:143], %[w1], %[ix]\n\tds_read_b128 v[160:163], %[ma] offset:6144\n\tv_smfmac_f32_16x16x64_bf16 v[172:175], v[140:143], %[w9], %[ix]\n\t" \
        "ds_read_b128 v[164:167], %[ma] offset:7168\n\ts_waitcnt lgkmcnt(5)\n\tv_smfmac_f32_16x16x64_bf16 v[168:171], v[144:147], %[w2], %[ix]\n\ts_waitcnt lgkmcnt(4)\n\t" \
        "v_smfmac_f32_16x16x64_bf16 v[168:171], v[148:151], %[w3], %[ix]\n\ts_waitcnt lgkmcnt(3)\n\tv_smfmac_f32_16x16x64_bf16 v[168:171], v[152:155], %[w4], %[ix]\n\ts_waitcnt lgkmcnt(2)\n\t" \
        "v_smfmac_f32_16x16x64_bf16 v[168:171], v[156:159], %[w5], %[ix]\n\ts_waitcnt lgkmcnt(1)\n\tv_smfmac_f32_16x16x64_bf16 v[168:171], v[160:163], %[w6], %[ix]\n\ts_waitcnt lgkmcnt(0)\n\t" \
        "v_smfmac_f32_16x16x64_bf16 v[168:171], v[164:167], %[w7], %[ix]\n\ts_nop 2\n\tv_smfmac_f32_16x16x64_bf16 v[172:175], v[144:147], %[w10], %[ix]\n\ts_nop 2\n\t" \
        "v_smfmac_f32_16x16x64_bf16 v[172:175], v[148:151], %[w11], %[ix]\n\tv_permlane32_swap_b32 v168, v170\n\tv_smfmac_f32_16x16x64_bf16 v[172:175], v[152:155], %[w12], %[ix]\n\t" \
        "v_permlane32_swap_b32 v169, v171\n\tv_smfmac_f32_16x16x64_bf16 v[172:175], v[156:159], %[w13], %[ix]\n\tv_add_f32 v168, v168, v170\n\t" \
        "v_smfmac_f32_16x16x64_bf16 v[172:175], v[160:163], %[w14], %[ix]\n\tv_add_f32 v169, v169, v171\n\tv_smfmac_f32_16x16x64_bf16 v[172:175], v[164:167], %[w15], %[ix]\n\t" \
        "v_cvt_pk_bf16_f32 v170, v168, v169\n\tv_cmp_u_f32 vcc, v168, v169\n\ts_nop 0\n\tds_write_b32 %[qw], v170\n\ts_nop 3\n\tv_permlane32_swap_b32 v172, v174\n\t" \
        "v_permlane32_swap_b32 v173, v175\n\ts_waitcnt lgkmcnt(0)\n\ts_barrier\n\tv_add_f32 v172, v172, v174\n\tv_add_f32 v173, v173, v175\n\tv_cvt_pk_bf16_f32 v174, v172, v173\n\t" \
        "ds_write_b32 %[qw], v174 offset:1024\n\ts_barrier\n\ts_cbranch_vccnz 6f\n\t7:\ns_waitcnt lgkmcnt(0)\n\ts_barrier\n\ts_cbranch_vccnz 5f\n\t4:\ns_mov_b32 m0, %[keep]\n\ts_branch 8f\n\t5:\n" \
        "s_cmp_eq_u32 %[s], 0\n\ts_cbranch_scc1 4b\n\tv_max3_u32 v170, v136, v137, v138\n\tv_max3_u32 v170, v170, v139, v139\n\tv_max3_u32 v170, v140, v141, v170\n\t" \
        "v_max3_u32 v170, v170, v143, v142\n\tv_max3_u32 v170, v144, v145, v170\n\tv_max3_u32 v170, v170, v147, v146\n\tv_max3_u32 v170, v148, v149, v170\n\tv_max3_u32 v170, v170, v151, v150\n\t" \
        "v_max3_u32 v170, v152, v153, v170\n\tv_max3_u32 v170, v170, v155, v154\n\tv_max3_u32 v170, v156, v157, v170\n\tv_max3_u32 v170, v170, v159, v158\n\tv_max3_u32 v170, v160, v161, v170\n\t" \
        "v_max3_u32 v170, v170, v163, v162\n\tv_max3_u32 v170, v164, v165, v170\n\tv_max3_u32 v170, v170, v167, v166\n\tv_cmp_eq_u32 vcc, -1, v170\n\ts_cbranch_vccz 4b\n\t" \
        "s_add_u32 %[cnt], %[cnt], 1\n\ts_cmp_gt_u32 %[cnt], %[cap]\n\ts_cselect_b32 %[cap], 0, %[cap]\n\ts_cbranch_scc1 4b\n\ts_branch 2b\n\t6:\ns_cmp_eq_u32 %[s], 0\n\ts_cbranch_scc1 7b\n\t" \
        "s_mov_b32 m0, %[lm]\n\ts_add_u32 %[f2], %[fb], 0x1000\n\tbuffer_load_dwordx4 %[o], %[rs], %[fb] offen sc1 lds\n\tbuffer_load_dwordx4 %[o], %[rs], %[fb] offen offset:1024 sc1 lds\n\t" \
        "buffer_load_dwordx4 %[o], %[rs], %[fb] offen offset:2048 sc1 lds\n\tbuffer_load_dwordx4 %[o], %[rs], %[fb] offen offset:3072 sc1 lds\n\ts_add_u32 m0, m0, 0x1000\n\ts_nop 0\n\t" \
        "buffer_load_dwordx4 %[o], %[rs], %[f2] offen sc1 lds\n\tbuffer_load_dwordx4 %[o], %[rs], %[f2] offen offset:1024 sc1 lds\n\t" \
        "buffer_load_dwordx4 %[o], %[rs], %[f2] offen offset:2048 sc1 lds\n\tbuffer_load_dwordx4 %[o], %[rs], %[f2] offen offset:3072 sc1 lds\n\ts_branch 7b\n\t8:\n"
#define ASM_VO \
        "s_mov_b32 %[cnt], 0\n\t1:\nv_mov_b64 v[192:193], 0\n\tv_mov_b64 v[194:195], 0\n\tv_mov_b64 v[196:197], 0\n\tv_mov_b64 v[198:199], 0\n\tv_mov_b64 v[200:201], 0\n\tv_mov_b64 v[202:203], 0\n\t" \
        "v_mov_b64 v[204:205], 0\n\tv_mov_b64 v[206:207], 0\n\ts_waitcnt lgkmcnt(0)\n\ts_barrier\n\ts_barrier\n\ts_nop 5\n\tds_read_b128 v[136:139], %[ma]\n\t" \
        "ds_read_b128 v[140:143], %[ma] offset:1024\n\tds_read_b128 v[144:147], %[ma] offset:2048\n\tds_read_b128 v[148:151], %[ma] offset:3072\n\tds_read_b128 v[152:155], %[ma] offset:4096\n\t" \
        "ds_read_b128 v[156:159], %[ma] offset:5120\n\tds_read_b128 v[160:163], %[ma] offset:6144\n\tds_read_b128 v[164:167], %[ma] offset:7168\n\ts_waitcnt lgkmcnt(0)\n\ts_barrier\n\t" \
        "ds_read_b64_tr_b16 v[176:177], %[fr] offset:0\n\tds_read_b64_tr_b16 v[178:179], %[fr] offset:256\n\tds_read_b64_tr_b16 v[180:181], %[fr] offset:512\n\t" \
        "ds_read_b64_tr_b16 v[182:183], %[fr] offset:768\n\ts_barrier\n\ts_waitcnt lgkmcnt(2)\n\tv_smfmac_f32_16x16x64_bf16 v[192:195], v[176:179], %[w0], %[ix]\n\t" \
        "v_smfmac_f32_16x16x64_bf16 v[196:199], v[176:179], %[w4], %[ix]\n\ts_waitcnt lgkmcnt(0)\n\tv_smfmac_f32_16x16x64_bf16 v[192:195], v[180:183], %[w1], %[ix]\n\ts_barrier\n\t" \
        "v_smfmac_f32_16x16x64_bf16 v[196:199], v[180:183], %[w5], %[ix]\n\tds_read_b64_tr_b16 v[184:185], %[fr] offset:1024\n\tds_read_b64_tr_b16 v[186:187], %[fr] offset:1280\n\t" \
        "v_smfmac_f32_16x16x64_bf16 v[200:203], v[176:179], %[w8], %[ix]\n\tds_read_b64_tr_b16 v[188:189], %[fr] offset:1536\n\tds_read_b64_tr_b16 v[190:191], %[fr] offset:1792\n\t" \
        "v_smfmac_f32_16x16x64_bf16 v[204:207], v[176:179], %[w12], %[ix]\n\tv_smfmac_f32_16x16x64_bf16 v[200:203], v[180:183], %[w9], %[ix]\n\t" \
        "v_smfmac_f32_16x16x64_bf16 v[204:207], v[180:183], %[w13], %[ix]\n\ts_waitcnt lgkmcnt(2)\n\tv_smfmac_f32_16x16x64_bf16 v[192:195], v[184:187], %[w2], %[ix]\n\t" \
        "v_smfmac_f32_16x16x64_bf16 v[196:199], v[184:187], %[w6], %[ix]\n\ts_waitcnt lgkmcnt(0)\n\tv_smfmac_f32_16x16x64_bf16 v[192:195], v[188:191], %[w3], %[ix]\n\t" \
        "v_smfmac_f32_16x16x64_bf16 v[196:199], v[188:191], %[w7], %[ix]\n\ts_nop 2\n\tv_smfmac_f32_16x16x64_bf16 v[200:203], v[184:187], %[w10], %[ix]\n\ts_nop 2\n\t" \
        "v_smfmac_f32_16x16x64_bf16 v[204:207], v[184:187], %[w14], %[ix]\n\tv_cmp_u_f32 vcc, v192, v193\n\tv_cmp_u_f32 s[94:95], v194, v195\n\t" \
        "v_smfmac_f32_16x16x64_bf16 v[200:203], v[188:191], %[w11], %[ix]\n\tv_cvt_pk_bf16_f32 v208, v192, v193\n\tv_cvt_pk_bf16_f32 v210, v196, v197\n\t" \
        "v_smfmac_f32_16x16x64_bf16 v[204:207], v[188:191], %[w15], %[ix]\n\tv_cvt_pk_bf16_f32 v209, v194, v195\n\tv_cvt_pk_bf16_f32 v211, v198, v199\n\tv_min_u32 v208, -2, v208\n\t" \
        "s_or_b64 vcc, vcc, s[94:95]\n\tv_min_u32 v210, -2, v210\n\tv_min_u32 v209, -2, v209\n\tv_min_u32 v211, -2, v211\n\tv_permlane16_swap_b32 v208, v210\n\ts_nop 0\n\t" \
        "v_permlane16_swap_b32 v209, v211\n\ts_cbranch_vccnz 5f\n\t4:\nbuffer_store_dwordx4 v[208:211], %[so0], %[rs], 0 offen sc1\n\tv_cvt_pk_bf16_f32 v212, v200, v201\n\t" \
        "v_cvt_pk_bf16_f32 v214, v204, v205\n\tv_cvt_pk_bf16_f32 v213, v202, v203\n\tv_cvt_pk_bf16_f32 v215, v206, v207\n\tv_min_u32 v212, -2, v212\n\tv_min_u32 v214, -2, v214\n\t" \
        "v_min_u32 v213, -2, v213\n\tv_min_u32 v215, -2, v215\n\tv_permlane16_swap_b32 v212, v214\n\ts_nop 0\n\tv_permlane16_swap_b32 v213, v215\n\t" \
        "buffer_store_dwordx4 v[212:215], %[so1], %[rs], 0 offen sc1\n\ts_branch 8f\n\t5:\ns_cmp_eq_u32 %[s], 0\n\ts_cbranch_scc1 4b\n\tv_max3_u32 v170, v136, v137, v138\n\t" \
        "v_max3_u32 v170, v170, v139, v139\n\tv_max3_u32 v170, v140, v141, v170\n\tv_max3_u32 v170, v170, v143, v142\n\tv_max3_u32 v170, v144, v145, v170\n\tv_max3_u32 v170, v170, v147, v146\n\t" \
        "v_max3_u32 v170, v148, v149, v170\n\tv_max3_u32 v170, v170, v151, v150\n\tv_max3_u32 v170, v152, v153, v170\n\tv_max3_u32 v170, v170, v155, v154\n\tv_max3_u32 v170, v156, v157, v170\n\t" \
        "v_max3_u32 v170, v170, v159, v158\n\tv_max3_u32 v170, v160, v161, v170\n\tv_max3_u32 v170, v170, v163, v162\n\tv_max3_u32 v170, v164, v165, v170\n\tv_max3_u32 v170, v170, v167, v166\n\t" \
        "v_cmp_eq_u32 vcc, -1, v170\n\ts_cbranch_vccz 4b\n\ts_add_u32 %[cnt], %[cnt], 1\n\ts_cmp_gt_u32 %[cnt], %[cap]\n\ts_cselect_b32 %[cap], 0, %[cap]\n\ts_cbranch_scc1 4b\n\ts_branch 1b\n\t8:\n"
#define ASM_CSTEP \
        "s_mov_b32 %[cnt], 0\n\ts_mov_b32 s94, %[ab]\n\tv_mov_b32 v248, %[fl]\n\tv_mov_b32 v249, %[tg]\n\tv_add_u32 v36, %[lx0], %[l16]\n\tv_add_u32 v68, %[lx1], %[l16]\n\t" \
        "v_add_u32 v100, %[lx2], %[l16]\n\tv_add_u32 v132, %[lx3], %[l16]\n\ts_mov_b32 %[tn], %[pa]\n\t22:\ns_memrealtime vcc\n\ts_waitcnt lgkmcnt(0)\n\ts_lshl_b32 vcc_lo, vcc_lo, 6\n\t" \
        "s_sub_u32 vcc_lo, %[tn], vcc_lo\n\ts_sub_u32 vcc_hi, vcc_lo, 1\n\ts_cmp_gt_u32 vcc_hi, 0x40000\n\ts_cbranch_scc1 23f\n\ts_cmp_lt_u32 vcc_lo, 0x200\n\ts_cbranch_scc1 22b\n\ts_sleep 1\n\t" \
        "s_branch 22b\n\t23:\nds_read_b32 v250, v248\n\ts_waitcnt lgkmcnt(0)\n\ts_buffer_load_dword %[aw], %[rs], s94\n\tv_readfirstlane_b32 s93, v250\n\ts_nop 0\n\ts_cmp_lg_u32 s93, %[tg]\n\t" \
        "s_cbranch_scc1 24f\n\tds_read_b128 v[8:11], v36\n\tds_read_b128 v[12:15], v36 offset:1024\n\tds_read_b128 v[16:19], v36 offset:2048\n\tds_read_b128 v[20:23], v36 offset:3072\n\t" \
        "ds_read_b128 v[24:27], v36 offset:4096\n\tds_read_b128 v[28:31], v36 offset:5120\n\tds_read_b128 v[32:35], v36 offset:6144\n\tds_read_b128 v[36:39], v36 offset:7168\n\t" \
        "ds_read_b128 v[40:43], v68\n\tds_read_b128 v[44:47], v68 offset:1024\n\tds_read_b128 v[48:51], v68 offset:2048\n\tds_read_b128 v[52:55], v68 offset:3072\n\t" \
        "ds_read_b128 v[56:59], v68 offset:4096\n\tds_read_b128 v[60:63], v68 offset:5120\n\tds_read_b128 v[64:67], v68 offset:6144\n\tds_read_b128 v[68:71], v68 offset:7168\n\t" \
        "ds_read_b128 v[72:75], v100\n\tds_read_b128 v[76:79], v100 offset:1024\n\tds_read_b128 v[80:83], v100 offset:2048\n\tds_read_b128 v[84:87], v100 offset:3072\n\t" \
        "ds_read_b128 v[88:91], v100 offset:4096\n\tds_read_b128 v[92:95], v100 offset:5120\n\tds_read_b128 v[96:99], v100 offset:6144\n\tds_read_b128 v[100:103], v100 offset:7168\n\t" \
        "ds_read_b128 v[104:107], v132\n\tds_read_b128 v[108:111], v132 offset:1024\n\tds_read_b128 v[112:115], v132 offset:2048\n\tds_read_b128 v[116:119], v132 offset:3072\n\t" \
        "ds_read_b128 v[120:123], v132 offset:4096\n\tds_read_b128 v[124:127], v132 offset:5120\n\tds_read_b128 v[128:131], v132 offset:6144\n\tds_read_b128 v[132:135], v132 offset:7168\n\t24:\n" \
        "s_waitcnt lgkmcnt(0)\n\ts_andn2_b32 s95, %[aw], 63\n\ts_cmp_eq_u32 %[aw], -1\n\ts_cbranch_scc1 2f\n\t4:\ns_add_u32 %[tn], s95, %[ax]\n\t5:\ns_memrealtime vcc\n\ts_waitcnt lgkmcnt(0)\n\t" \
        "s_lshl_b32 vcc_lo, vcc_lo, 6\n\ts_sub_u32 vcc_lo, %[tn], vcc_lo\n\ts_sub_u32 vcc_hi, vcc_lo, 1\n\ts_cmp_gt_u32 vcc_hi, 0x40000\n\ts_cbranch_scc1 6f\n\ts_cmp_lt_u32 vcc_lo, 0x200\n\t" \
        "s_cbranch_scc1 5b\n\ts_sleep 1\n\ts_branch 5b\n\t6:\nbuffer_load_dwordx4 v[216:219], %[l16], %[rs], %[vb0] offen sc1\n\t" \
        "buffer_load_dwordx4 v[220:223], %[l16], %[rs], %[vb0] offen offset:1024 sc1\n\tbuffer_load_dwordx4 v[224:227], %[l16], %[rs], %[vb1] offen sc1\n\t" \
        "buffer_load_dwordx4 v[228:231], %[l16], %[rs], %[vb1] offen offset:1024 sc1\n\tbuffer_load_dwordx4 v[232:235], %[l16], %[rs], %[vb2] offen sc1\n\t" \
        "buffer_load_dwordx4 v[236:239], %[l16], %[rs], %[vb2] offen offset:1024 sc1\n\tbuffer_load_dwordx4 v[240:243], %[l16], %[rs], %[vb3] offen sc1\n\t" \
        "buffer_load_dwordx4 v[244:247], %[l16], %[rs], %[vb3] offen offset:1024 sc1\n\ts_add_u32 %[tn], s95, %[ap]\n\t7:\ns_memrealtime vcc\n\ts_waitcnt lgkmcnt(0)\n\t" \
        "s_lshl_b32 vcc_lo, vcc_lo, 6\n\ts_sub_u32 vcc_lo, %[tn], vcc_lo\n\ts_sub_u32 vcc_hi, vcc_lo, 1\n\ts_cmp_gt_u32 vcc_hi, 0x40000\n\ts_cbranch_scc1 8f\n\ts_cmp_lt_u32 vcc_lo, 0x200\n\t" \
        "s_cbranch_scc1 7b\n\ts_sleep 1\n\ts_branch 7b\n\t8:\nbuffer_load_dwordx4 v[200:203], %[pm], %[rs], %[pb0] offen offset:1024 sc1\n\t" \
        "buffer_load_dwordx4 v[204:207], %[pm], %[rs], %[pb1] offen offset:1024 sc1\n\tbuffer_load_dwordx4 v[208:211], %[pm], %[rs], %[pb2] offen offset:1024 sc1\n\t" \
        "buffer_load_dwordx4 v[212:215], %[pm], %[rs], %[pb3] offen offset:1024 sc1\n\tbuffer_load_dwordx4 v[136:139], %[l16], %[rs], %[pb0] offen sc1\n\t" \
        "buffer_load_dwordx4 v[140:143], %[l16], %[rs], %[pb0] offen offset:1280 sc1\n\tbuffer_load_dwordx4 v[144:147], %[l16], %[rs], %[pb0] offen offset:2560 sc1\n\t" \
        "buffer_load_dwordx4 v[148:151], %[l16], %[rs], %[pb0] offen offset:3840 sc1\n\tbuffer_load_dwordx4 v[152:155], %[l16], %[rs], %[pb1] offen sc1\n\t" \
        "buffer_load_dwordx4 v[156:159], %[l16], %[rs], %[pb1] offen offset:1280 sc1\n\tbuffer_load_dwordx4 v[160:163], %[l16], %[rs], %[pb1] offen offset:2560 sc1\n\t" \
        "buffer_load_dwordx4 v[164:167], %[l16], %[rs], %[pb1] offen offset:3840 sc1\n\tbuffer_load_dwordx4 v[168:171], %[l16], %[rs], %[pb2] offen sc1\n\t" \
        "buffer_load_dwordx4 v[172:175], %[l16], %[rs], %[pb2] offen offset:1280 sc1\n\tbuffer_load_dwordx4 v[176:179], %[l16], %[rs], %[pb2] offen offset:2560 sc1\n\t" \
        "buffer_load_dwordx4 v[180:183], %[l16], %[rs], %[pb2] offen offset:3840 sc1\n\tbuffer_load_dwordx4 v[184:187], %[l16], %[rs], %[pb3] offen sc1\n\t" \
        "buffer_load_dwordx4 v[188:191], %[l16], %[rs], %[pb3] offen offset:1280 sc1\n\tbuffer_load_dwordx4 v[192:195], %[l16], %[rs], %[pb3] offen offset:2560 sc1\n\t" \
        "buffer_load_dwordx4 v[196:199], %[l16], %[rs], %[pb3] offen offset:3840 sc1\n\tds_write_b32 v248, v249 offset:4\n\ts_mov_b32 %[tn], 0\n\ts_cmp_lg_u32 s93, %[tg]\n\ts_cbranch_scc1 20f\n\t9:\n" \
        "v_mov_b64 v[248:249], 0\n\tv_mov_b64 v[250:251], 0\n\ts_cmp_eq_u32 %[on], 0\n\ts_cbranch_scc1 17f\n\ts_waitcnt vmcnt(19)\n\t" \
        "v_smfmac_f32_16x16x64_bf16 v[248:251], v[200:203], v[216:223], %[ix]\n\ts_waitcnt vmcnt(18)\n\tv_smfmac_f32_16x16x64_bf16 v[248:251], v[204:207], v[224:231], %[ix]\n\ts_waitcnt vmcnt(17)\n\t" \
        "v_smfmac_f32_16x16x64_bf16 v[248:251], v[208:211], v[232:239], %[ix]\n\ts_waitcnt vmcnt(16)\n\tv_smfmac_f32_16x16x64_bf16 v[248:251], v[212:215], v[240:247], %[ix]\n\t" \
        "s_waitcnt vmcnt(15) lgkmcnt(14)\n\tv_smfmac_f32_16x16x64_bf16 v[248:251], v[136:139], v[8:15], %[ix]\n\ts_waitcnt vmcnt(14) lgkmcnt(14)\n\t" \
        "v_smfmac_f32_16x16x64_bf16 v[248:251], v[140:143], v[16:23], %[ix]\n\ts_waitcnt vmcnt(13) lgkmcnt(14)\n\tv_smfmac_f32_16x16x64_bf16 v[248:251], v[144:147], v[24:31], %[ix]\n\t" \
        "s_waitcnt vmcnt(12) lgkmcnt(14)\n\tv_smfmac_f32_16x16x64_bf16 v[248:251], v[148:151], v[32:39], %[ix]\n\ts_waitcnt vmcnt(11) lgkmcnt(14)\n\t" \
        "v_smfmac_f32_16x16x64_bf16 v[248:251], v[152:155], v[40:47], %[ix]\n\ts_waitcnt vmcnt(10) lgkmcnt(14)\n\tv_smfmac_f32_16x16x64_bf16 v[248:251], v[156:159], v[48:55], %[ix]\n\t" \
        "s_waitcnt vmcnt(9) lgkmcnt(14)\n\tv_smfmac_f32_16x16x64_bf16 v[248:251], v[160:163], v[56:63], %[ix]\n\ts_waitcnt vmcnt(8) lgkmcnt(14)\n\t" \
        "v_smfmac_f32_16x16x64_bf16 v[248:251], v[164:167], v[64:71], %[ix]\n\ts_waitcnt vmcnt(7) lgkmcnt(14)\n\tv_smfmac_f32_16x16x64_bf16 v[248:251], v[168:171], v[72:79], %[ix]\n\t" \
        "s_waitcnt vmcnt(6) lgkmcnt(12)\n\tv_smfmac_f32_16x16x64_bf16 v[248:251], v[172:175], v[80:87], %[ix]\n\ts_waitcnt vmcnt(5) lgkmcnt(10)\n\t" \
        "v_smfmac_f32_16x16x64_bf16 v[248:251], v[176:179], v[88:95], %[ix]\n\ts_waitcnt vmcnt(4) lgkmcnt(8)\n\tv_smfmac_f32_16x16x64_bf16 v[248:251], v[180:183], v[96:103], %[ix]\n\t" \
        "s_waitcnt vmcnt(3) lgkmcnt(6)\n\tv_smfmac_f32_16x16x64_bf16 v[248:251], v[184:187], v[104:111], %[ix]\n\ts_waitcnt vmcnt(2) lgkmcnt(4)\n\t" \
        "v_smfmac_f32_16x16x64_bf16 v[248:251], v[188:191], v[112:119], %[ix]\n\ts_waitcnt vmcnt(1) lgkmcnt(2)\n\tv_smfmac_f32_16x16x64_bf16 v[248:251], v[192:195], v[120:127], %[ix]\n\t" \
        "s_waitcnt vmcnt(0) lgkmcnt(0)\n\tv_smfmac_f32_16x16x64_bf16 v[248:251], v[196:199], v[128:135], %[ix]\n\ts_nop 7\n\tv_permlane32_swap_b32 v248, v250\n\tv_permlane32_swap_b32 v249, v251\n\t" \
        "v_pk_add_f32 v[248:249], v[248:249], v[250:251]\n\tv_cvt_pk_bf16_f32 v250, v248, v249\n\tv_cmp_u_f32 vcc, v248, v249\n\ts_nop 0\n\t" \
        "v_mov_b32_dpp v251, v250 quad_perm:[1,0,3,2] row_mask:0xf bank_mask:0xf bound_ctrl:1\n\tv_perm_b32 v248, v251, v250, %[sel]\n\ts_nop 1\n\t" \
        "v_mov_b32_dpp v249, v248 row_shl:2 row_mask:0xf bank_mask:0xf bound_ctrl:1\n\tv_mov_b32_dpp v250, v248 row_shl:4 row_mask:0xf bank_mask:0xf bound_ctrl:1\n\t" \
        "v_mov_b32_dpp v251, v248 row_shl:6 row_mask:0xf bank_mask:0xf bound_ctrl:1\n\ts_cbranch_vccnz 12f\n\t11:\nbuffer_store_dwordx4 v[248:251], %[mo], %[rs], %[mb] offen sc1\n\t15:\ns_barrier\n\t" \
        "v_add_u32 v136, %[vs0], %[l16]\n\tv_add_u32 v137, %[vs1], %[l16]\n\tv_add_u32 v138, %[vs2], %[l16]\n\tv_add_u32 v139, %[vs3], %[l16]\n\tds_write_b128 v136, v[216:219]\n\t" \
        "ds_write_b128 v136, v[220:223] offset:1024\n\tds_write_b128 v137, v[224:227]\n\tds_write_b128 v137, v[228:231] offset:1024\n\tds_write_b128 v138, v[232:235]\n\t" \
        "ds_write_b128 v138, v[236:239] offset:1024\n\tds_write_b128 v139, v[240:243]\n\tds_write_b128 v139, v[244:247] offset:1024\n\ts_branch 19f\n\t12:\ns_cmp_eq_u32 %[tn], 1\n\t" \
        "s_cbranch_scc1 13f\n\ts_add_u32 %[cnt], %[cnt], 1\n\ts_and_b32 vcc_lo, %[cnt], 0xffff\n\ts_cmp_gt_u32 vcc_lo, %[cap]\n\ts_cbranch_scc1 14f\n\ts_cmp_lt_u32 vcc_lo, 6\n\ts_cbranch_scc1 18f\n\t" \
        "v_max3_u32 v248, v136, v137, v137\n\tv_max3_u32 v248, v248, v138, v139\n\tv_max3_u32 v248, v248, v140, v141\n\tv_max3_u32 v248, v248, v142, v143\n\tv_max3_u32 v248, v248, v144, v145\n\t" \
        "v_max3_u32 v248, v248, v146, v147\n\tv_max3_u32 v248, v248, v148, v149\n\tv_max3_u32 v248, v248, v150, v151\n\tv_max3_u32 v248, v248, v152, v153\n\tv_max3_u32 v248, v248, v154, v155\n\t" \
        "v_max3_u32 v248, v248, v156, v157\n\tv_max3_u32 v248, v248, v158, v159\n\tv_max3_u32 v248, v248, v160, v161\n\tv_max3_u32 v248, v248, v162, v163\n\tv_max3_u32 v248, v248, v164, v165\n\t" \
        "v_max3_u32 v248, v248, v166, v167\n\tv_max3_u32 v248, v248, v168, v169\n\tv_max3_u32 v248, v248, v170, v171\n\tv_max3_u32 v248, v248, v172, v173\n\tv_max3_u32 v248, v248, v174, v175\n\t" \
        "v_max3_u32 v248, v248, v176, v177\n\tv_max3_u32 v248, v248, v178, v179\n\tv_max3_u32 v248, v248, v180, v181\n\tv_max3_u32 v248, v248, v182, v183\n\tv_max3_u32 v248, v248, v184, v185\n\t" \
        "v_max3_u32 v248, v248, v186, v187\n\tv_max3_u32 v248, v248, v188, v189\n\tv_max3_u32 v248, v248, v190, v191\n\tv_max3_u32 v248, v248, v192, v193\n\tv_max3_u32 v248, v248, v194, v195\n\t" \
        "v_max3_u32 v248, v248, v196, v197\n\tv_max3_u32 v248, v248, v198, v199\n\tv_max3_u32 v248, v248, v200, v201\n\tv_max3_u32 v248, v248, v202, v203\n\tv_max3_u32 v248, v248, v204, v205\n\t" \
        "v_max3_u32 v248, v248, v206, v207\n\tv_max3_u32 v248, v248, v208, v209\n\tv_max3_u32 v248, v248, v210, v211\n\tv_max3_u32 v248, v248, v212, v213\n\tv_max3_u32 v248, v248, v214, v215\n\t" \
        "v_max3_u32 v248, v248, v216, v217\n\tv_max3_u32 v248, v248, v218, v219\n\tv_max3_u32 v248, v248, v220, v221\n\tv_max3_u32 v248, v248, v222, v223\n\tv_max3_u32 v248, v248, v224, v225\n\t" \
        "v_max3_u32 v248, v248, v226, v227\n\tv_max3_u32 v248, v248, v228, v229\n\tv_max3_u32 v248, v248, v230, v231\n\tv_max3_u32 v248, v248, v232, v233\n\tv_max3_u32 v248, v248, v234, v235\n\t" \
        "v_max3_u32 v248, v248, v236, v237\n\tv_max3_u32 v248, v248, v238, v239\n\tv_max3_u32 v248, v248, v240, v241\n\tv_max3_u32 v248, v248, v242, v243\n\tv_max3_u32 v248, v248, v244, v245\n\t" \
        "v_max3_u32 v248, v248, v246, v247\n\tv_cmp_eq_u32 vcc, -1, v248\n\ts_cbranch_vccnz 18f\n\ts_mov_b32 %[tn], 1\n\ts_branch 9b\n\t14:\ns_mov_b32 %[cap], 0\n\ts_cmp_eq_u32 %[on], 0\n\t" \
        "s_cbranch_scc1 15b\n\t13:\nv_min_u32 v248, -2, v248\n\tv_min_u32 v249, -2, v249\n\tv_min_u32 v250, -2, v250\n\tv_min_u32 v251, -2, v251\n\ts_branch 11b\n\t18:\n" \
        "buffer_load_dwordx4 v[216:219], %[l16], %[rs], %[vb0] offen sc1\n\tbuffer_load_dwordx4 v[220:223], %[l16], %[rs], %[vb0] offen offset:1024 sc1\n\t" \
        "buffer_load_dwordx4 v[224:227], %[l16], %[rs], %[vb1] offen sc1\n\tbuffer_load_dwordx4 v[228:231], %[l16], %[rs], %[vb1] offen offset:1024 sc1\n\t" \
        "buffer_load_dwordx4 v[232:235], %[l16], %[rs], %[vb2] offen sc1\n\tbuffer_load_dwordx4 v[236:239], %[l16], %[rs], %[vb2] offen offset:1024 sc1\n\t" \
        "buffer_load_dwordx4 v[240:243], %[l16], %[rs], %[vb3] offen sc1\n\tbuffer_load_dwordx4 v[244:247], %[l16], %[rs], %[vb3] offen offset:1024 sc1\n\t" \
        "buffer_load_dwordx4 v[200:203], %[pm], %[rs], %[pb0] offen offset:1024 sc1\n\tbuffer_load_dwordx4 v[204:207], %[pm], %[rs], %[pb1] offen offset:1024 sc1\n\t" \
        "buffer_load_dwordx4 v[208:211], %[pm], %[rs], %[pb2] offen offset:1024 sc1\n\tbuffer_load_dwordx4 v[212:215], %[pm], %[rs], %[pb3] offen offset:1024 sc1\n\t" \
        "buffer_load_dwordx4 v[136:139], %[l16], %[rs], %[pb0] offen sc1\n\tbuffer_load_dwordx4 v[140:143], %[l16], %[rs], %[pb0] offen offset:1280 sc1\n\t" \
        "buffer_load_dwordx4 v[144:147], %[l16], %[rs], %[pb0] offen offset:2560 sc1\n\tbuffer_load_dwordx4 v[148:151], %[l16], %[rs], %[pb0] offen offset:3840 sc1\n\t" \
        "buffer_load_dwordx4 v[152:155], %[l16], %[rs], %[pb1] offen sc1\n\tbuffer_load_dwordx4 v[156:159], %[l16], %[rs], %[pb1] offen offset:1280 sc1\n\t" \
        "buffer_load_dwordx4 v[160:163], %[l16], %[rs], %[pb1] offen offset:2560 sc1\n\tbuffer_load_dwordx4 v[164:167], %[l16], %[rs], %[pb1] offen offset:3840 sc1\n\t" \
        "buffer_load_dwordx4 v[168:171], %[l16], %[rs], %[pb2] offen sc1\n\tbuffer_load_dwordx4 v[172:175], %[l16], %[rs], %[pb2] offen offset:1280 sc1\n\t" \
        "buffer_load_dwordx4 v[176:179], %[l16], %[rs], %[pb2] offen offset:2560 sc1\n\tbuffer_load_dwordx4 v[180:183], %[l16], %[rs], %[pb2] offen offset:3840 sc1\n\t" \
        "buffer_load_dwordx4 v[184:187], %[l16], %[rs], %[pb3] offen sc1\n\tbuffer_load_dwordx4 v[188:191], %[l16], %[rs], %[pb3] offen offset:1280 sc1\n\t" \
        "buffer_load_dwordx4 v[192:195], %[l16], %[rs], %[pb3] offen offset:2560 sc1\n\tbuffer_load_dwordx4 v[196:199], %[l16], %[rs], %[pb3] offen offset:3840 sc1\n\ts_branch 9b\n\t20:\n" \
        "ds_read_b32 v250, v248\n\ts_add_u32 %[tn], %[tn], 0x40000\n\ts_waitcnt lgkmcnt(0)\n\tv_readfirstlane_b32 s93, v250\n\ts_cbranch_scc1 21f\n\ts_cmp_lg_u32 s93, %[tg]\n\ts_cbranch_scc1 20b\n\t" \
        "21:\nds_read_b128 v[8:11], v36\n\tds_read_b128 v[12:15], v36 offset:1024\n\tds_read_b128 v[16:19], v36 offset:2048\n\tds_read_b128 v[20:23], v36 offset:3072\n\t" \
        "ds_read_b128 v[24:27], v36 offset:4096\n\tds_read_b128 v[28:31], v36 offset:5120\n\tds_read_b128 v[32:35], v36 offset:6144\n\tds_read_b128 v[36:39], v36 offset:7168\n\t" \
        "ds_read_b128 v[40:43], v68\n\tds_read_b128 v[44:47], v68 offset:1024\n\tds_read_b128 v[48:51], v68 offset:2048\n\tds_read_b128 v[52:55], v68 offset:3072\n\t" \
        "ds_read_b128 v[56:59], v68 offset:4096\n\tds_read_b128 v[60:63], v68 offset:5120\n\tds_read_b128 v[64:67], v68 offset:6144\n\tds_read_b128 v[68:71], v68 offset:7168\n\t" \
        "ds_read_b128 v[72:75], v100\n\tds_read_b128 v[76:79], v100 offset:1024\n\tds_read_b128 v[80:83], v100 offset:2048\n\tds_read_b128 v[84:87], v100 offset:3072\n\t" \
        "ds_read_b128 v[88:91], v100 offset:4096\n\tds_read_b128 v[92:95], v100 offset:5120\n\tds_read_b128 v[96:99], v100 offset:6144\n\tds_read_b128 v[100:103], v100 offset:7168\n\t" \
        "ds_read_b128 v[104:107], v132\n\tds_read_b128 v[108:111], v132 offset:1024\n\tds_read_b128 v[112:115], v132 offset:2048\n\tds_read_b128 v[116:119], v132 offset:3072\n\t" \
        "ds_read_b128 v[120:123], v132 offset:4096\n\tds_read_b128 v[124:127], v132 offset:5120\n\tds_read_b128 v[128:131], v132 offset:6144\n\tds_read_b128 v[132:135], v132 offset:7168\n\t" \
        "s_mov_b32 %[tn], 0\n\ts_branch 9b\n\t17:\ns_waitcnt vmcnt(0) lgkmcnt(0)\n\tv_max3_u32 v248, v216, v217, v217\n\tv_max3_u32 v248, v248, v218, v219\n\tv_max3_u32 v248, v248, v220, v221\n\t" \
        "v_max3_u32 v248, v248, v222, v223\n\tv_max3_u32 v248, v248, v224, v225\n\tv_max3_u32 v248, v248, v226, v227\n\tv_max3_u32 v248, v248, v228, v229\n\tv_max3_u32 v248, v248, v230, v231\n\t" \
        "v_max3_u32 v248, v248, v232, v233\n\tv_max3_u32 v248, v248, v234, v235\n\tv_max3_u32 v248, v248, v236, v237\n\tv_max3_u32 v248, v248, v238, v239\n\tv_max3_u32 v248, v248, v240, v241\n\t" \
        "v_max3_u32 v248, v248, v242, v243\n\tv_max3_u32 v248, v248, v244, v245\n\tv_max3_u32 v248, v248, v246, v247\n\tv_cmp_eq_u32 vcc, -1, v248\n\ts_cbranch_vccz 15b\n\ts_branch 12b\n\t2:\n" \
        "s_add_u32 vcc_hi, %[ab], 704\n\t3:\ns_waitcnt lgkmcnt(0)\n\ts_andn2_b32 s95, %[aw], 63\n\ts_cmp_lg_u32 %[aw], -1\n\ts_cbranch_scc1 4b\n\ts_cmp_eq_u32 s94, vcc_hi\n\ts_cbranch_scc1 1f\n\t" \
        "s_add_u32 s94, s94, 64\n\ts_buffer_load_dword %[aw], %[rs], s94\n\ts_branch 3b\n\t1:\ns_memrealtime vcc\n\ts_waitcnt lgkmcnt(0)\n\ts_lshl_b32 s95, vcc_lo, 6\n\t" \
        "s_sub_u32 s95, s95, 0x40000\n\ts_branch 4b\n\t19:\ns_waitcnt lgkmcnt(0)\n\t"
#define ASM_ATT \
        "s_mov_b32 %[keep], m0\n\ts_mov_b32 %[sk], 0\n\tv_mov_b32 v187, %[fo]\n\tv_mov_b64 v[128:129], 0\n\tv_mov_b64 v[130:131], 0\n\tv_mov_b64 v[132:133], 0\n\tv_mov_b64 v[134:135], 0\n\t" \
        "v_mov_b64 v[136:137], 0\n\tv_mov_b64 v[138:139], 0\n\tv_mov_b64 v[140:141], 0\n\tv_mov_b64 v[142:143], 0\n\tv_mov_b64 v[184:185], 0\n\ts_cmp_lg_u32 %[rem], 0\n\ts_cbranch_scc1 1f\n\t" \
        "v_mov_b32 v190, 0xff800000\n\tv_cmp_le_i32 vcc, 0, %[dq]\n\ts_nop 1\n\tv_cndmask_b32 v128, v190, v128, vcc\n\tv_cmp_le_i32 vcc, 1, %[dq]\n\ts_nop 1\n\tv_cndmask_b32 v129, v190, v129, vcc\n\t" \
        "v_cmp_le_i32 vcc, 2, %[dq]\n\ts_nop 1\n\tv_cndmask_b32 v130, v190, v130, vcc\n\tv_cmp_le_i32 vcc, 3, %[dq]\n\ts_nop 1\n\tv_cndmask_b32 v131, v190, v131, vcc\n\tv_cmp_le_i32 vcc, 8, %[dq]\n\t" \
        "s_nop 1\n\tv_cndmask_b32 v132, v190, v132, vcc\n\tv_cmp_le_i32 vcc, 9, %[dq]\n\ts_nop 1\n\tv_cndmask_b32 v133, v190, v133, vcc\n\tv_cmp_le_i32 vcc, 10, %[dq]\n\ts_nop 1\n\t" \
        "v_cndmask_b32 v134, v190, v134, vcc\n\tv_cmp_le_i32 vcc, 11, %[dq]\n\ts_nop 1\n\tv_cndmask_b32 v135, v190, v135, vcc\n\tv_cmp_le_i32 vcc, 16, %[dq]\n\ts_nop 1\n\t" \
        "v_cndmask_b32 v136, v190, v136, vcc\n\tv_cmp_le_i32 vcc, 17, %[dq]\n\ts_nop 1\n\tv_cndmask_b32 v137, v190, v137, vcc\n\tv_cmp_le_i32 vcc, 18, %[dq]\n\ts_nop 1\n\t" \
        "v_cndmask_b32 v138, v190, v138, vcc\n\tv_cmp_le_i32 vcc, 19, %[dq]\n\ts_nop 1\n\tv_cndmask_b32 v139, v190, v139, vcc\n\tv_cmp_le_i32 vcc, 24, %[dq]\n\ts_nop 1\n\t" \
        "v_cndmask_b32 v140, v190, v140, vcc\n\tv_cmp_le_i32 vcc, 25, %[dq]\n\ts_nop 1\n\tv_cndmask_b32 v141, v190, v141, vcc\n\tv_cmp_le_i32 vcc, 26, %[dq]\n\ts_nop 1\n\t" \
        "v_cndmask_b32 v142, v190, v142, vcc\n\tv_cmp_le_i32 vcc, 27, %[dq]\n\ts_nop 1\n\tv_cndmask_b32 v143, v190, v143, vcc\n\ts_nop 1\n\t1:\ns_barrier\n\tds_read_b128 v[152:155], v187 offset:0\n\t" \
        "ds_read_b128 v[156:159], v187 offset:512\n\tds_read_b128 v[160:163], v187 offset:2048\n\tds_read_b128 v[164:167], v187 offset:2560\n\ts_waitcnt lgkmcnt(3)\n\t" \
        "v_mfma_f32_32x32x16_bf16 v[96:111], v[152:155], v[64:67], v[128:143]\n\tds_read_b128 v[168:171], v187 offset:4096\n\tv_mov_b64 v[0:1], 0\n\tv_mov_b64 v[2:3], 0\n\tv_mov_b64 v[4:5], 0\n\t" \
        "v_mov_b64 v[6:7], 0\n\ts_waitcnt lgkmcnt(3)\n\tv_mfma_f32_32x32x16_bf16 v[96:111], v[156:159], v[68:71], v[96:111]\n\tds_read_b128 v[172:175], v187 offset:4608\n\tv_mov_b64 v[8:9], 0\n\t" \
        "v_mov_b64 v[10:11], 0\n\tv_mov_b64 v[12:13], 0\n\tv_mov_b64 v[14:15], 0\n\ts_waitcnt lgkmcnt(3)\n\tv_mfma_f32_32x32x16_bf16 v[96:111], v[160:163], v[72:75], v[96:111]\n\t" \
        "ds_read_b128 v[176:179], v187 offset:6144\n\tv_mov_b64 v[16:17], 0\n\tv_mov_b64 v[18:19], 0\n\tv_mov_b64 v[20:21], 0\n\tv_mov_b64 v[22:23], 0\n\ts_waitcnt lgkmcnt(3)\n\t" \
        "v_mfma_f32_32x32x16_bf16 v[96:111], v[164:167], v[76:79], v[96:111]\n\tds_read_b128 v[180:183], v187 offset:6656\n\tv_mov_b64 v[24:25], 0\n\tv_mov_b64 v[26:27], 0\n\t" \
        "v_mov_b64 v[28:29], 0\n\tv_mov_b64 v[30:31], 0\n\ts_waitcnt lgkmcnt(3)\n\tv_mfma_f32_32x32x16_bf16 v[96:111], v[168:171], v[80:83], v[96:111]\n\tds_read_b128 v[152:155], v187 offset:8192\n\t" \
        "v_mov_b64 v[32:33], 0\n\tv_mov_b64 v[34:35], 0\n\tv_mov_b64 v[36:37], 0\n\tv_mov_b64 v[38:39], 0\n\ts_waitcnt lgkmcnt(3)\n\t" \
        "v_mfma_f32_32x32x16_bf16 v[96:111], v[172:175], v[84:87], v[96:111]\n\tds_read_b128 v[156:159], v187 offset:8704\n\tv_mov_b64 v[40:41], 0\n\tv_mov_b64 v[42:43], 0\n\t" \
        "v_mov_b64 v[44:45], 0\n\tv_mov_b64 v[46:47], 0\n\ts_waitcnt lgkmcnt(3)\n\tv_mfma_f32_32x32x16_bf16 v[96:111], v[176:179], v[88:91], v[96:111]\n\t" \
        "ds_read_b128 v[160:163], v187 offset:10240\n\tv_mov_b64 v[48:49], 0\n\tv_mov_b64 v[50:51], 0\n\tv_mov_b64 v[52:53], 0\n\tv_mov_b64 v[54:55], 0\n\ts_waitcnt lgkmcnt(3)\n\t" \
        "v_mfma_f32_32x32x16_bf16 v[96:111], v[180:183], v[92:95], v[96:111]\n\tds_read_b128 v[164:167], v187 offset:10752\n\tv_mov_b64 v[56:57], 0\n\tv_mov_b64 v[58:59], 0\n\t" \
        "v_mov_b64 v[60:61], 0\n\tv_mov_b64 v[62:63], 0\n\ts_nop 6\n\tv_max3_f32 v186, v96, v97, v98\n\tv_max3_f32 v186, v186, v99, v100\n\tv_max3_f32 v186, v186, v101, v102\n\t" \
        "v_max3_f32 v186, v186, v103, v104\n\tv_max3_f32 v186, v186, v105, v106\n\tv_max3_f32 v186, v186, v107, v108\n\tv_max3_f32 v186, v186, v109, v110\n\tv_max_f32 v186, v186, v111\n\t" \
        "v_mov_b32 v190, v186\n\ts_nop 1\n\tv_permlane32_swap_b32 v186, v190\n\ts_nop 1\n\tv_max_f32 v186, v186, v190\n\tv_sub_f32 v128, v128, v186\n\tv_sub_f32 v129, v129, v186\n\t" \
        "v_sub_f32 v130, v130, v186\n\tv_sub_f32 v131, v131, v186\n\tv_sub_f32 v132, v132, v186\n\tv_sub_f32 v133, v133, v186\n\tv_sub_f32 v134, v134, v186\n\tv_sub_f32 v135, v135, v186\n\t" \
        "v_sub_f32 v136, v136, v186\n\tv_sub_f32 v137, v137, v186\n\tv_sub_f32 v138, v138, v186\n\tv_sub_f32 v139, v139, v186\n\tv_sub_f32 v140, v140, v186\n\tv_sub_f32 v141, v141, v186\n\t" \
        "v_sub_f32 v142, v142, v186\n\tv_sub_f32 v143, v143, v186\n\tv_sub_f32 v96, v96, v186\n\tv_sub_f32 v97, v97, v186\n\tv_sub_f32 v98, v98, v186\n\tv_sub_f32 v99, v99, v186\n\t" \
        "v_sub_f32 v100, v100, v186\n\tv_sub_f32 v101, v101, v186\n\tv_sub_f32 v102, v102, v186\n\tv_sub_f32 v103, v103, v186\n\tv_sub_f32 v104, v104, v186\n\tv_sub_f32 v105, v105, v186\n\t" \
        "v_sub_f32 v106, v106, v186\n\tv_sub_f32 v107, v107, v186\n\tv_sub_f32 v108, v108, v186\n\tv_sub_f32 v109, v109, v186\n\tv_sub_f32 v110, v110, v186\n\tv_sub_f32 v111, v111, v186\n\t" \
        "s_branch 40f\n\t2:\ns_waitcnt lgkmcnt(6)\n\tv_mfma_f32_32x32x16_bf16 v[0:15], v[152:155], v[144:147], v[0:15]\n\tds_read_b64_tr_b16 v[168:169], v188 offset:9216\n\t" \
        "ds_read_b64_tr_b16 v[170:171], v189 offset:9216\n\tv_cvt_pk_bf16_f32 v148, v120, v121\n\tv_exp_f32 v123, v123\n\tv_exp_f32 v124, v124\n\ts_waitcnt lgkmcnt(6)\n\t" \
        "v_mfma_f32_32x32x16_bf16 v[16:31], v[156:159], v[144:147], v[16:31]\n\tds_read_b64_tr_b16 v[172:173], v188 offset:11264\n\tds_read_b64_tr_b16 v[174:175], v189 offset:11264\n\t" \
        "v_cvt_pk_bf16_f32 v149, v122, v123\n\tv_exp_f32 v125, v125\n\ts_waitcnt lgkmcnt(6)\n\tv_mfma_f32_32x32x16_bf16 v[32:47], v[160:163], v[144:147], v[32:47]\n\t" \
        "ds_read_b64_tr_b16 v[176:177], v188 offset:13312\n\tds_read_b64_tr_b16 v[178:179], v189 offset:13312\n\tv_exp_f32 v126, v126\n\tv_cvt_pk_bf16_f32 v150, v124, v125\n\tv_exp_f32 v127, v127\n\t" \
        "s_waitcnt lgkmcnt(6)\n\tv_mfma_f32_32x32x16_bf16 v[48:63], v[164:167], v[144:147], v[48:63]\n\tds_read_b64_tr_b16 v[180:181], v188 offset:15360\n\t" \
        "ds_read_b64_tr_b16 v[182:183], v189 offset:15360\n\tv_add_f32 v185, v185, v119\n\tv_cvt_pk_bf16_f32 v151, v126, v127\n\tv_add_f32 v184, v184, v120\n\tv_add_f32 v185, v185, v121\n\t" \
        "s_waitcnt lgkmcnt(6)\n\tv_mfma_f32_32x32x16_bf16 v[0:15], v[168:171], v[148:151], v[0:15]\n\tds_read_b128 v[152:155], v187 offset:8192\n\tv_max3_f32 v186, v96, v97, v98\n\t" \
        "v_add_f32 v184, v184, v122\n\tv_max3_f32 v186, v186, v99, v100\n\tv_max3_f32 v186, v186, v101, v102\n\ts_waitcnt lgkmcnt(5)\n\t" \
        "v_mfma_f32_32x32x16_bf16 v[16:31], v[172:175], v[148:151], v[16:31]\n\tds_read_b128 v[156:159], v187 offset:8704\n\tv_add_f32 v185, v185, v123\n\tv_max3_f32 v186, v186, v103, v104\n\t" \
        "v_max3_f32 v186, v186, v105, v106\n\ts_waitcnt lgkmcnt(4)\n\tv_mfma_f32_32x32x16_bf16 v[32:47], v[176:179], v[148:151], v[32:47]\n\tds_read_b128 v[160:163], v187 offset:10240\n\t" \
        "v_add_f32 v184, v184, v124\n\tv_max3_f32 v186, v186, v107, v108\n\tv_max3_f32 v186, v186, v109, v110\n\tv_add_f32 v185, v185, v125\n\ts_waitcnt lgkmcnt(3)\n\t" \
        "v_mfma_f32_32x32x16_bf16 v[48:63], v[180:183], v[148:151], v[48:63]\n\tds_read_b128 v[164:167], v187 offset:10752\n\tv_max_f32 v186, v186, v111\n\tv_add_f32 v184, v184, v126\n\t" \
        "v_cmp_lt_f32 vcc, 0x41000000, v186\n\tv_add_f32 v185, v185, v127\n\ts_cbranch_vccnz 41f\n\t40:\ns_cmp_lt_i32 %[tb], 1\n\ts_cbranch_scc1 31f\n\ts_waitcnt vmcnt(6)\n\ts_barrier\n\t30:\n" \
        "s_cmp_gt_i32 %[tb], 0\n\ts_cselect_b64 vcc, -1, 0\n\ts_sub_u32 %[tb], %[tb], 1\n\ts_cmp_lt_u32 %[rem], 2\n\ts_cbranch_scc1 43f\n\t42:\ns_sub_u32 %[rem], %[rem], 1\n\ts_waitcnt lgkmcnt(3)\n\t" \
        "v_mfma_f32_32x32x16_bf16 v[112:127], v[152:155], v[64:67], v[128:143]\n\tds_read_b128 v[168:171], v187 offset:12288\n\tv_exp_f32 v96, v96\n\tv_exp_f32 v97, v97\n\ts_waitcnt lgkmcnt(3)\n\t" \
        "v_mfma_f32_32x32x16_bf16 v[112:127], v[156:159], v[68:71], v[112:127]\n\tds_read_b128 v[172:175], v187 offset:12800\n\tv_add_f32 v184, v184, v96\n\tv_exp_f32 v98, v98\n\t" \
        "v_add_f32 v185, v185, v97\n\ts_cbranch_vccz 32f\n\ts_sub_u32 %[t0], %[sk], 0x8000\n\ts_and_b32 %[t0], %[t0], 0x18000\n\ts_add_u32 m0, %[t0], %[lb]\n\ts_add_u32 %[t0], %[so], 0x1000000\n\t" \
        "buffer_load_dwordx4 %[ko], %[rs], %[so] offen sc1 lds\n\t32:\ns_waitcnt lgkmcnt(3)\n\tv_mfma_f32_32x32x16_bf16 v[112:127], v[160:163], v[72:75], v[112:127]\n\t" \
        "ds_read_b128 v[176:179], v187 offset:14336\n\tv_exp_f32 v99, v99\n\tv_add_f32 v184, v184, v98\n\tv_exp_f32 v100, v100\n\ts_waitcnt lgkmcnt(3)\n\t" \
        "v_mfma_f32_32x32x16_bf16 v[112:127], v[164:167], v[76:79], v[112:127]\n\tds_read_b128 v[180:183], v187 offset:14848\n\tv_cvt_pk_bf16_f32 v144, v96, v97\n\tv_exp_f32 v101, v101\n\t" \
        "s_cbranch_vccz 33f\n\tbuffer_load_dwordx4 %[ko], %[rs], %[so] offen offset:1024 sc1 lds\n\t33:\ns_waitcnt lgkmcnt(3)\n\t" \
        "v_mfma_f32_32x32x16_bf16 v[112:127], v[168:171], v[80:83], v[112:127]\n\tv_add_u32 v188, %[sk], %[vb]\n\tv_xor_b32 v189, 0x80, v188\n\tds_read_b64_tr_b16 v[152:153], v188 offset:0\n\t" \
        "ds_read_b64_tr_b16 v[154:155], v189 offset:0\n\tv_add_f32 v185, v185, v99\n\tv_cvt_pk_bf16_f32 v145, v98, v99\n\tv_exp_f32 v102, v102\n\tv_add_f32 v184, v184, v100\n\t" \
        "s_waitcnt lgkmcnt(4)\n\tv_mfma_f32_32x32x16_bf16 v[112:127], v[172:175], v[84:87], v[112:127]\n\tds_read_b64_tr_b16 v[156:157], v188 offset:2048\n\t" \
        "ds_read_b64_tr_b16 v[158:159], v189 offset:2048\n\tv_cvt_pk_bf16_f32 v146, v100, v101\n\tv_exp_f32 v103, v103\n\tv_add_f32 v185, v185, v101\n\ts_cbranch_vccz 34f\n\t" \
        "s_add_u32 m0, m0, 0x4000\n\ts_add_u32 %[so], %[so], 0x80000\n\tbuffer_load_dwordx4 %[vo], %[rs], %[t0] offen sc1 lds\n\t34:\ns_waitcnt lgkmcnt(5)\n\t" \
        "v_mfma_f32_32x32x16_bf16 v[112:127], v[176:179], v[88:91], v[112:127]\n\tds_read_b64_tr_b16 v[160:161], v188 offset:4096\n\tds_read_b64_tr_b16 v[162:163], v189 offset:4096\n\t" \
        "v_exp_f32 v104, v104\n\tv_add_f32 v184, v184, v102\n\tv_cvt_pk_bf16_f32 v147, v102, v103\n\ts_waitcnt lgkmcnt(6)\n\tv_mfma_f32_32x32x16_bf16 v[112:127], v[180:183], v[92:95], v[112:127]\n\t" \
        "ds_read_b64_tr_b16 v[164:165], v188 offset:6144\n\tds_read_b64_tr_b16 v[166:167], v189 offset:6144\n\tv_exp_f32 v105, v105\n\tv_exp_f32 v106, v106\n\ts_cbranch_vccz 35f\n\t" \
        "buffer_load_dwordx4 %[vo], %[rs], %[t0] offen offset:1024 sc1 lds\n\t35:\ns_add_u32 %[sk], %[sk], 0x8000\n\ts_and_b32 %[sk], %[sk], 0x18000\n\ts_waitcnt lgkmcnt(6)\n\t" \
        "v_mfma_f32_32x32x16_bf16 v[0:15], v[152:155], v[144:147], v[0:15]\n\tds_read_b64_tr_b16 v[168:169], v188 offset:1024\n\tds_read_b64_tr_b16 v[170:171], v189 offset:1024\n\t" \
        "v_cvt_pk_bf16_f32 v148, v104, v105\n\tv_exp_f32 v107, v107\n\tv_exp_f32 v108, v108\n\ts_waitcnt lgkmcnt(6)\n\tv_mfma_f32_32x32x16_bf16 v[16:31], v[156:159], v[144:147], v[16:31]\n\t" \
        "ds_read_b64_tr_b16 v[172:173], v188 offset:3072\n\tds_read_b64_tr_b16 v[174:175], v189 offset:3072\n\tv_cvt_pk_bf16_f32 v149, v106, v107\n\tv_exp_f32 v109, v109\n\ts_waitcnt lgkmcnt(6)\n\t" \
        "v_mfma_f32_32x32x16_bf16 v[32:47], v[160:163], v[144:147], v[32:47]\n\tds_read_b64_tr_b16 v[176:177], v188 offset:5120\n\tds_read_b64_tr_b16 v[178:179], v189 offset:5120\n\t" \
        "v_exp_f32 v110, v110\n\tv_cvt_pk_bf16_f32 v150, v108, v109\n\tv_exp_f32 v111, v111\n\ts_waitcnt lgkmcnt(6)\n\tv_mfma_f32_32x32x16_bf16 v[48:63], v[164:167], v[144:147], v[48:63]\n\t" \
        "ds_read_b64_tr_b16 v[180:181], v188 offset:7168\n\tds_read_b64_tr_b16 v[182:183], v189 offset:7168\n\tv_add_f32 v185, v185, v103\n\tv_cvt_pk_bf16_f32 v151, v110, v111\n\t" \
        "v_add_f32 v184, v184, v104\n\tv_add_f32 v185, v185, v105\n\ts_waitcnt lgkmcnt(6)\n\tv_mfma_f32_32x32x16_bf16 v[0:15], v[168:171], v[148:151], v[0:15]\n\tv_add_u32 v187, %[sk], %[fo]\n\t" \
        "ds_read_b128 v[152:155], v187 offset:0\n\tv_max3_f32 v186, v112, v113, v114\n\tv_add_f32 v184, v184, v106\n\tv_max3_f32 v186, v186, v115, v116\n\tv_max3_f32 v186, v186, v117, v118\n\t" \
        "s_waitcnt lgkmcnt(5)\n\tv_mfma_f32_32x32x16_bf16 v[16:31], v[172:175], v[148:151], v[16:31]\n\tds_read_b128 v[156:159], v187 offset:512\n\tv_add_f32 v185, v185, v107\n\t" \
        "v_max3_f32 v186, v186, v119, v120\n\tv_max3_f32 v186, v186, v121, v122\n\ts_waitcnt lgkmcnt(4)\n\tv_mfma_f32_32x32x16_bf16 v[32:47], v[176:179], v[148:151], v[32:47]\n\t" \
        "ds_read_b128 v[160:163], v187 offset:2048\n\tv_add_f32 v184, v184, v108\n\tv_max3_f32 v186, v186, v123, v124\n\tv_max3_f32 v186, v186, v125, v126\n\tv_add_f32 v185, v185, v109\n\t" \
        "s_waitcnt lgkmcnt(3)\n\tv_mfma_f32_32x32x16_bf16 v[48:63], v[180:183], v[148:151], v[48:63]\n\tds_read_b128 v[164:167], v187 offset:2560\n\tv_max_f32 v186, v186, v127\n\t" \
        "v_add_f32 v184, v184, v110\n\tv_cmp_lt_f32 vcc, 0x41000000, v186\n\tv_add_f32 v185, v185, v111\n\ts_cbranch_vccnz 61f\n\t60:\ns_cmp_lt_u32 %[rem], 2\n\ts_cbranch_scc1 63f\n\t62:\n" \
        "s_sub_u32 %[rem], %[rem], 1\n\ts_waitcnt lgkmcnt(3)\n\tv_mfma_f32_32x32x16_bf16 v[96:111], v[152:155], v[64:67], v[128:143]\n\tds_read_b128 v[168:171], v187 offset:4096\n\t" \
        "v_exp_f32 v112, v112\n\tv_exp_f32 v113, v113\n\ts_waitcnt lgkmcnt(3)\n\tv_mfma_f32_32x32x16_bf16 v[96:111], v[156:159], v[68:71], v[96:111]\n\tds_read_b128 v[172:175], v187 offset:4608\n\t" \
        "v_add_f32 v184, v184, v112\n\tv_exp_f32 v114, v114\n\tv_add_f32 v185, v185, v113\n\ts_waitcnt lgkmcnt(3)\n\tv_mfma_f32_32x32x16_bf16 v[96:111], v[160:163], v[72:75], v[96:111]\n\t" \
        "ds_read_b128 v[176:179], v187 offset:6144\n\tv_exp_f32 v115, v115\n\tv_add_f32 v184, v184, v114\n\tv_exp_f32 v116, v116\n\ts_waitcnt lgkmcnt(3)\n\t" \
        "v_mfma_f32_32x32x16_bf16 v[96:111], v[164:167], v[76:79], v[96:111]\n\tds_read_b128 v[180:183], v187 offset:6656\n\tv_cvt_pk_bf16_f32 v144, v112, v113\n\tv_exp_f32 v117, v117\n\t" \
        "s_waitcnt lgkmcnt(3)\n\tv_mfma_f32_32x32x16_bf16 v[96:111], v[168:171], v[80:83], v[96:111]\n\tds_read_b64_tr_b16 v[152:153], v188 offset:8192\n\t" \
        "ds_read_b64_tr_b16 v[154:155], v189 offset:8192\n\tv_add_f32 v185, v185, v115\n\tv_cvt_pk_bf16_f32 v145, v114, v115\n\tv_exp_f32 v118, v118\n\tv_add_f32 v184, v184, v116\n\t" \
        "s_waitcnt lgkmcnt(4)\n\tv_mfma_f32_32x32x16_bf16 v[96:111], v[172:175], v[84:87], v[96:111]\n\tds_read_b64_tr_b16 v[156:157], v188 offset:10240\n\t" \
        "ds_read_b64_tr_b16 v[158:159], v189 offset:10240\n\tv_cvt_pk_bf16_f32 v146, v116, v117\n\tv_exp_f32 v119, v119\n\tv_add_f32 v185, v185, v117\n\ts_waitcnt lgkmcnt(5)\n\t" \
        "v_mfma_f32_32x32x16_bf16 v[96:111], v[176:179], v[88:91], v[96:111]\n\tds_read_b64_tr_b16 v[160:161], v188 offset:12288\n\tds_read_b64_tr_b16 v[162:163], v189 offset:12288\n\t" \
        "v_exp_f32 v120, v120\n\tv_add_f32 v184, v184, v118\n\tv_cvt_pk_bf16_f32 v147, v118, v119\n\ts_waitcnt lgkmcnt(6)\n\tv_mfma_f32_32x32x16_bf16 v[96:111], v[180:183], v[92:95], v[96:111]\n\t" \
        "ds_read_b64_tr_b16 v[164:165], v188 offset:14336\n\tds_read_b64_tr_b16 v[166:167], v189 offset:14336\n\tv_exp_f32 v121, v121\n\tv_exp_f32 v122, v122\n\ts_branch 2b\n\t31:\n" \
        "s_cmp_lt_i32 %[tb], 0\n\ts_cbranch_scc1 30b\n\ts_waitcnt vmcnt(0)\n\ts_barrier\n\ts_branch 30b\n\t41:\nv_mov_b32 v190, v186\n\ts_nop 1\n\tv_permlane32_swap_b32 v186, v190\n\ts_nop 1\n\t" \
        "v_max_f32 v186, v186, v190\n\tv_max_f32 v186, 0, v186\n\tv_sub_f32 v128, v128, v186\n\tv_sub_f32 v129, v129, v186\n\tv_sub_f32 v130, v130, v186\n\tv_sub_f32 v131, v131, v186\n\t" \
        "v_sub_f32 v132, v132, v186\n\tv_sub_f32 v133, v133, v186\n\tv_sub_f32 v134, v134, v186\n\tv_sub_f32 v135, v135, v186\n\tv_sub_f32 v136, v136, v186\n\tv_sub_f32 v137, v137, v186\n\t" \
        "v_sub_f32 v138, v138, v186\n\tv_sub_f32 v139, v139, v186\n\tv_sub_f32 v140, v140, v186\n\tv_sub_f32 v141, v141, v186\n\tv_sub_f32 v142, v142, v186\n\tv_sub_f32 v143, v143, v186\n\t" \
        "v_sub_f32 v96, v96, v186\n\tv_sub_f32 v97, v97, v186\n\tv_sub_f32 v98, v98, v186\n\tv_sub_f32 v99, v99, v186\n\tv_sub_f32 v100, v100, v186\n\tv_sub_f32 v101, v101, v186\n\t" \
        "v_sub_f32 v102, v102, v186\n\tv_sub_f32 v103, v103, v186\n\tv_sub_f32 v104, v104, v186\n\tv_sub_f32 v105, v105, v186\n\tv_sub_f32 v106, v106, v186\n\tv_sub_f32 v107, v107, v186\n\t" \
        "v_sub_f32 v108, v108, v186\n\tv_sub_f32 v109, v109, v186\n\tv_sub_f32 v110, v110, v186\n\tv_sub_f32 v111, v111, v186\n\tv_exp_f32 v190, -v186\n\ts_nop 0\n\tv_mul_f32 v184, v184, v190\n\t" \
        "v_mul_f32 v185, v185, v190\n\tv_mul_f32 v0, v0, v190\n\tv_mul_f32 v1, v1, v190\n\tv_mul_f32 v2, v2, v190\n\tv_mul_f32 v3, v3, v190\n\tv_mul_f32 v4, v4, v190\n\tv_mul_f32 v5, v5, v190\n\t" \
        "v_mul_f32 v6, v6, v190\n\tv_mul_f32 v7, v7, v190\n\tv_mul_f32 v8, v8, v190\n\tv_mul_f32 v9, v9, v190\n\tv_mul_f32 v10, v10, v190\n\tv_mul_f32 v11, v11, v190\n\tv_mul_f32 v12, v12, v190\n\t" \
        "v_mul_f32 v13, v13, v190\n\tv_mul_f32 v14, v14, v190\n\tv_mul_f32 v15, v15, v190\n\tv_mul_f32 v16, v16, v190\n\tv_mul_f32 v17, v17, v190\n\tv_mul_f32 v18, v18, v190\n\t" \
        "v_mul_f32 v19, v19, v190\n\tv_mul_f32 v20, v20, v190\n\tv_mul_f32 v21, v21, v190\n\tv_mul_f32 v22, v22, v190\n\tv_mul_f32 v23, v23, v190\n\tv_mul_f32 v24, v24, v190\n\t" \
        "v_mul_f32 v25, v25, v190\n\tv_mul_f32 v26, v26, v190\n\tv_mul_f32 v27, v27, v190\n\tv_mul_f32 v28, v28, v190\n\tv_mul_f32 v29, v29, v190\n\tv_mul_f32 v30, v30, v190\n\t" \
        "v_mul_f32 v31, v31, v190\n\tv_mul_f32 v32, v32, v190\n\tv_mul_f32 v33, v33, v190\n\tv_mul_f32 v34, v34, v190\n\tv_mul_f32 v35, v35, v190\n\tv_mul_f32 v36, v36, v190\n\t" \
        "v_mul_f32 v37, v37, v190\n\tv_mul_f32 v38, v38, v190\n\tv_mul_f32 v39, v39, v190\n\tv_mul_f32 v40, v40, v190\n\tv_mul_f32 v41, v41, v190\n\tv_mul_f32 v42, v42, v190\n\t" \
        "v_mul_f32 v43, v43, v190\n\tv_mul_f32 v44, v44, v190\n\tv_mul_f32 v45, v45, v190\n\tv_mul_f32 v46, v46, v190\n\tv_mul_f32 v47, v47, v190\n\tv_mul_f32 v48, v48, v190\n\t" \
        "v_mul_f32 v49, v49, v190\n\tv_mul_f32 v50, v50, v190\n\tv_mul_f32 v51, v51, v190\n\tv_mul_f32 v52, v52, v190\n\tv_mul_f32 v53, v53, v190\n\tv_mul_f32 v54, v54, v190\n\t" \
        "v_mul_f32 v55, v55, v190\n\tv_mul_f32 v56, v56, v190\n\tv_mul_f32 v57, v57, v190\n\tv_mul_f32 v58, v58, v190\n\tv_mul_f32 v59, v59, v190\n\tv_mul_f32 v60, v60, v190\n\t" \
        "v_mul_f32 v61, v61, v190\n\tv_mul_f32 v62, v62, v190\n\tv_mul_f32 v63, v63, v190\n\ts_branch 40b\n\t43:\ns_cmp_eq_u32 %[rem], 0\n\ts_cbranch_scc1 44f\n\ts_mov_b64 %[sv], vcc\n\t" \
        "v_mov_b32 v190, 0xff800000\n\tv_cmp_le_i32 vcc, 0, %[dq]\n\ts_nop 1\n\tv_cndmask_b32 v128, v190, v128, vcc\n\tv_cmp_le_i32 vcc, 1, %[dq]\n\ts_nop 1\n\tv_cndmask_b32 v129, v190, v129, vcc\n\t" \
        "v_cmp_le_i32 vcc, 2, %[dq]\n\ts_nop 1\n\tv_cndmask_b32 v130, v190, v130, vcc\n\tv_cmp_le_i32 vcc, 3, %[dq]\n\ts_nop 1\n\tv_cndmask_b32 v131, v190, v131, vcc\n\tv_cmp_le_i32 vcc, 8, %[dq]\n\t" \
        "s_nop 1\n\tv_cndmask_b32 v132, v190, v132, vcc\n\tv_cmp_le_i32 vcc, 9, %[dq]\n\ts_nop 1\n\tv_cndmask_b32 v133, v190, v133, vcc\n\tv_cmp_le_i32 vcc, 10, %[dq]\n\ts_nop 1\n\t" \
        "v_cndmask_b32 v134, v190, v134, vcc\n\tv_cmp_le_i32 vcc, 11, %[dq]\n\ts_nop 1\n\tv_cndmask_b32 v135, v190, v135, vcc\n\tv_cmp_le_i32 vcc, 16, %[dq]\n\ts_nop 1\n\t" \
        "v_cndmask_b32 v136, v190, v136, vcc\n\tv_cmp_le_i32 vcc, 17, %[dq]\n\ts_nop 1\n\tv_cndmask_b32 v137, v190, v137, vcc\n\tv_cmp_le_i32 vcc, 18, %[dq]\n\ts_nop 1\n\t" \
        "v_cndmask_b32 v138, v190, v138, vcc\n\tv_cmp_le_i32 vcc, 19, %[dq]\n\ts_nop 1\n\tv_cndmask_b32 v139, v190, v139, vcc\n\tv_cmp_le_i32 vcc, 24, %[dq]\n\ts_nop 1\n\t" \
        "v_cndmask_b32 v140, v190, v140, vcc\n\tv_cmp_le_i32 vcc, 25, %[dq]\n\ts_nop 1\n\tv_cndmask_b32 v141, v190, v141, vcc\n\tv_cmp_le_i32 vcc, 26, %[dq]\n\ts_nop 1\n\t" \
        "v_cndmask_b32 v142, v190, v142, vcc\n\tv_cmp_le_i32 vcc, 27, %[dq]\n\ts_nop 1\n\tv_cndmask_b32 v143, v190, v143, vcc\n\ts_nop 1\n\ts_mov_b64 vcc, %[sv]\n\ts_branch 42b\n\t44:\n" \
        "v_exp_f32 v96, v96\n\tv_exp_f32 v97, v97\n\tv_add_f32 v184, v184, v96\n\tv_exp_f32 v98, v98\n\tv_add_f32 v185, v185, v97\n\ts_cbranch_vccz 82f\n\ts_sub_u32 %[t0], %[sk], 0x8000\n\t" \
        "s_and_b32 %[t0], %[t0], 0x18000\n\ts_add_u32 m0, %[t0], %[lb]\n\ts_add_u32 %[t0], %[so], 0x1000000\n\tbuffer_load_dwordx4 %[ko], %[rs], %[so] offen sc1 lds\n\t82:\nv_exp_f32 v99, v99\n\t" \
        "v_add_f32 v184, v184, v98\n\tv_exp_f32 v100, v100\n\tv_cvt_pk_bf16_f32 v144, v96, v97\n\tv_exp_f32 v101, v101\n\ts_cbranch_vccz 83f\n\t" \
        "buffer_load_dwordx4 %[ko], %[rs], %[so] offen offset:1024 sc1 lds\n\t83:\nv_add_u32 v188, %[sk], %[vb]\n\tv_xor_b32 v189, 0x80, v188\n\tds_read_b64_tr_b16 v[152:153], v188 offset:0\n\t" \
        "ds_read_b64_tr_b16 v[154:155], v189 offset:0\n\tv_add_f32 v185, v185, v99\n\tv_cvt_pk_bf16_f32 v145, v98, v99\n\tv_exp_f32 v102, v102\n\tv_add_f32 v184, v184, v100\n\t" \
        "ds_read_b64_tr_b16 v[156:157], v188 offset:2048\n\tds_read_b64_tr_b16 v[158:159], v189 offset:2048\n\tv_cvt_pk_bf16_f32 v146, v100, v101\n\tv_exp_f32 v103, v103\n\t" \
        "v_add_f32 v185, v185, v101\n\ts_cbranch_vccz 84f\n\ts_add_u32 m0, m0, 0x4000\n\ts_add_u32 %[so], %[so], 0x80000\n\tbuffer_load_dwordx4 %[vo], %[rs], %[t0] offen sc1 lds\n\t84:\n" \
        "ds_read_b64_tr_b16 v[160:161], v188 offset:4096\n\tds_read_b64_tr_b16 v[162:163], v189 offset:4096\n\tv_exp_f32 v104, v104\n\tv_add_f32 v184, v184, v102\n\t" \
        "v_cvt_pk_bf16_f32 v147, v102, v103\n\tds_read_b64_tr_b16 v[164:165], v188 offset:6144\n\tds_read_b64_tr_b16 v[166:167], v189 offset:6144\n\tv_exp_f32 v105, v105\n\tv_exp_f32 v106, v106\n\t" \
        "s_cbranch_vccz 85f\n\tbuffer_load_dwordx4 %[vo], %[rs], %[t0] offen offset:1024 sc1 lds\n\t85:\ns_add_u32 %[sk], %[sk], 0x8000\n\ts_and_b32 %[sk], %[sk], 0x18000\n\ts_waitcnt lgkmcnt(6)\n\t" \
        "v_mfma_f32_32x32x16_bf16 v[0:15], v[152:155], v[144:147], v[0:15]\n\tds_read_b64_tr_b16 v[168:169], v188 offset:1024\n\tds_read_b64_tr_b16 v[170:171], v189 offset:1024\n\t" \
        "v_cvt_pk_bf16_f32 v148, v104, v105\n\tv_exp_f32 v107, v107\n\tv_exp_f32 v108, v108\n\ts_waitcnt lgkmcnt(6)\n\tv_mfma_f32_32x32x16_bf16 v[16:31], v[156:159], v[144:147], v[16:31]\n\t" \
        "ds_read_b64_tr_b16 v[172:173], v188 offset:3072\n\tds_read_b64_tr_b16 v[174:175], v189 offset:3072\n\tv_cvt_pk_bf16_f32 v149, v106, v107\n\tv_exp_f32 v109, v109\n\ts_waitcnt lgkmcnt(6)\n\t" \
        "v_mfma_f32_32x32x16_bf16 v[32:47], v[160:163], v[144:147], v[32:47]\n\tds_read_b64_tr_b16 v[176:177], v188 offset:5120\n\tds_read_b64_tr_b16 v[178:179], v189 offset:5120\n\t" \
        "v_exp_f32 v110, v110\n\tv_cvt_pk_bf16_f32 v150, v108, v109\n\tv_exp_f32 v111, v111\n\ts_waitcnt lgkmcnt(6)\n\tv_mfma_f32_32x32x16_bf16 v[48:63], v[164:167], v[144:147], v[48:63]\n\t" \
        "ds_read_b64_tr_b16 v[180:181], v188 offset:7168\n\tds_read_b64_tr_b16 v[182:183], v189 offset:7168\n\tv_add_f32 v185, v185, v103\n\tv_cvt_pk_bf16_f32 v151, v110, v111\n\t" \
        "v_add_f32 v184, v184, v104\n\tv_add_f32 v185, v185, v105\n\ts_waitcnt lgkmcnt(6)\n\tv_mfma_f32_32x32x16_bf16 v[0:15], v[168:171], v[148:151], v[0:15]\n\tv_add_f32 v184, v184, v106\n\t" \
        "s_waitcnt lgkmcnt(4)\n\tv_mfma_f32_32x32x16_bf16 v[16:31], v[172:175], v[148:151], v[16:31]\n\tv_add_f32 v185, v185, v107\n\tv_add_f32 v184, v184, v108\n\ts_waitcnt lgkmcnt(2)\n\t" \
        "v_mfma_f32_32x32x16_bf16 v[32:47], v[176:179], v[148:151], v[32:47]\n\tv_add_f32 v185, v185, v109\n\ts_waitcnt lgkmcnt(0)\n\t" \
        "v_mfma_f32_32x32x16_bf16 v[48:63], v[180:183], v[148:151], v[48:63]\n\tv_add_f32 v184, v184, v110\n\tv_add_f32 v185, v185, v111\n\ts_branch 99f\n\t61:\nv_mov_b32 v190, v186\n\ts_nop 1\n\t" \
        "v_permlane32_swap_b32 v186, v190\n\ts_nop 1\n\tv_max_f32 v186, v186, v190\n\tv_max_f32 v186, 0, v186\n\tv_sub_f32 v128, v128, v186\n\tv_sub_f32 v129, v129, v186\n\t" \
        "v_sub_f32 v130, v130, v186\n\tv_sub_f32 v131, v131, v186\n\tv_sub_f32 v132, v132, v186\n\tv_sub_f32 v133, v133, v186\n\tv_sub_f32 v134, v134, v186\n\tv_sub_f32 v135, v135, v186\n\t" \
        "v_sub_f32 v136, v136, v186\n\tv_sub_f32 v137, v137, v186\n\tv_sub_f32 v138, v138, v186\n\tv_sub_f32 v139, v139, v186\n\tv_sub_f32 v140, v140, v186\n\tv_sub_f32 v141, v141, v186\n\t" \
        "v_sub_f32 v142, v142, v186\n\tv_sub_f32 v143, v143, v186\n\tv_sub_f32 v112, v112, v186\n\tv_sub_f32 v113, v113, v186\n\tv_sub_f32 v114, v114, v186\n\tv_sub_f32 v115, v115, v186\n\t" \
        "v_sub_f32 v116, v116, v186\n\tv_sub_f32 v117, v117, v186\n\tv_sub_f32 v118, v118, v186\n\tv_sub_f32 v119, v119, v186\n\tv_sub_f32 v120, v120, v186\n\tv_sub_f32 v121, v121, v186\n\t" \
        "v_sub_f32 v122, v122, v186\n\tv_sub_f32 v123, v123, v186\n\tv_sub_f32 v124, v124, v186\n\tv_sub_f32 v125, v125, v186\n\tv_sub_f32 v126, v126, v186\n\tv_sub_f32 v127, v127, v186\n\t" \
        "v_exp_f32 v190, -v186\n\ts_nop 0\n\tv_mul_f32 v184, v184, v190\n\tv_mul_f32 v185, v185, v190\n\tv_mul_f32 v0, v0, v190\n\tv_mul_f32 v1, v1, v190\n\tv_mul_f32 v2, v2, v190\n\t" \
        "v_mul_f32 v3, v3, v190\n\tv_mul_f32 v4, v4, v190\n\tv_mul_f32 v5, v5, v190\n\tv_mul_f32 v6, v6, v190\n\tv_mul_f32 v7, v7, v190\n\tv_mul_f32 v8, v8, v190\n\tv_mul_f32 v9, v9, v190\n\t" \
        "v_mul_f32 v10, v10, v190\n\tv_mul_f32 v11, v11, v190\n\tv_mul_f32 v12, v12, v190\n\tv_mul_f32 v13, v13, v190\n\tv_mul_f32 v14, v14, v190\n\tv_mul_f32 v15, v15, v190\n\t" \
        "v_mul_f32 v16, v16, v190\n\tv_mul_f32 v17, v17, v190\n\tv_mul_f32 v18, v18, v190\n\tv_mul_f32 v19, v19, v190\n\tv_mul_f32 v20, v20, v190\n\tv_mul_f32 v21, v21, v190\n\t" \
        "v_mul_f32 v22, v22, v190\n\tv_mul_f32 v23, v23, v190\n\tv_mul_f32 v24, v24, v190\n\tv_mul_f32 v25, v25, v190\n\tv_mul_f32 v26, v26, v190\n\tv_mul_f32 v27, v27, v190\n\t" \
        "v_mul_f32 v28, v28, v190\n\tv_mul_f32 v29, v29, v190\n\tv_mul_f32 v30, v30, v190\n\tv_mul_f32 v31, v31, v190\n\tv_mul_f32 v32, v32, v190\n\tv_mul_f32 v33, v33, v190\n\t" \
        "v_mul_f32 v34, v34, v190\n\tv_mul_f32 v35, v35, v190\n\tv_mul_f32 v36, v36, v190\n\tv_mul_f32 v37, v37, v190\n\tv_mul_f32 v38, v38, v190\n\tv_mul_f32 v39, v39, v190\n\t" \
        "v_mul_f32 v40, v40, v190\n\tv_mul_f32 v41, v41, v190\n\tv_mul_f32 v42, v42, v190\n\tv_mul_f32 v43, v43, v190\n\tv_mul_f32 v44, v44, v190\n\tv_mul_f32 v45, v45, v190\n\t" \
        "v_mul_f32 v46, v46, v190\n\tv_mul_f32 v47, v47, v190\n\tv_mul_f32 v48, v48, v190\n\tv_mul_f32 v49, v49, v190\n\tv_mul_f32 v50, v50, v190\n\tv_mul_f32 v51, v51, v190\n\t" \
        "v_mul_f32 v52, v52, v190\n\tv_mul_f32 v53, v53, v190\n\tv_mul_f32 v54, v54, v190\n\tv_mul_f32 v55, v55, v190\n\tv_mul_f32 v56, v56, v190\n\tv_mul_f32 v57, v57, v190\n\t" \
        "v_mul_f32 v58, v58, v190\n\tv_mul_f32 v59, v59, v190\n\tv_mul_f32 v60, v60, v190\n\tv_mul_f32 v61, v61, v190\n\tv_mul_f32 v62, v62, v190\n\tv_mul_f32 v63, v63, v190\n\ts_branch 60b\n\t63:\n" \
        "s_cmp_eq_u32 %[rem], 0\n\ts_cbranch_scc1 64f\n\ts_mov_b64 %[sv], vcc\n\tv_mov_b32 v190, 0xff800000\n\tv_cmp_le_i32 vcc, 0, %[dq]\n\ts_nop 1\n\tv_cndmask_b32 v128, v190, v128, vcc\n\t" \
        "v_cmp_le_i32 vcc, 1, %[dq]\n\ts_nop 1\n\tv_cndmask_b32 v129, v190, v129, vcc\n\tv_cmp_le_i32 vcc, 2, %[dq]\n\ts_nop 1\n\tv_cndmask_b32 v130, v190, v130, vcc\n\tv_cmp_le_i32 vcc, 3, %[dq]\n\t" \
        "s_nop 1\n\tv_cndmask_b32 v131, v190, v131, vcc\n\tv_cmp_le_i32 vcc, 8, %[dq]\n\ts_nop 1\n\tv_cndmask_b32 v132, v190, v132, vcc\n\tv_cmp_le_i32 vcc, 9, %[dq]\n\ts_nop 1\n\t" \
        "v_cndmask_b32 v133, v190, v133, vcc\n\tv_cmp_le_i32 vcc, 10, %[dq]\n\ts_nop 1\n\tv_cndmask_b32 v134, v190, v134, vcc\n\tv_cmp_le_i32 vcc, 11, %[dq]\n\ts_nop 1\n\t" \
        "v_cndmask_b32 v135, v190, v135, vcc\n\tv_cmp_le_i32 vcc, 16, %[dq]\n\ts_nop 1\n\tv_cndmask_b32 v136, v190, v136, vcc\n\tv_cmp_le_i32 vcc, 17, %[dq]\n\ts_nop 1\n\t" \
        "v_cndmask_b32 v137, v190, v137, vcc\n\tv_cmp_le_i32 vcc, 18, %[dq]\n\ts_nop 1\n\tv_cndmask_b32 v138, v190, v138, vcc\n\tv_cmp_le_i32 vcc, 19, %[dq]\n\ts_nop 1\n\t" \
        "v_cndmask_b32 v139, v190, v139, vcc\n\tv_cmp_le_i32 vcc, 24, %[dq]\n\ts_nop 1\n\tv_cndmask_b32 v140, v190, v140, vcc\n\tv_cmp_le_i32 vcc, 25, %[dq]\n\ts_nop 1\n\t" \
        "v_cndmask_b32 v141, v190, v141, vcc\n\tv_cmp_le_i32 vcc, 26, %[dq]\n\ts_nop 1\n\tv_cndmask_b32 v142, v190, v142, vcc\n\tv_cmp_le_i32 vcc, 27, %[dq]\n\ts_nop 1\n\t" \
        "v_cndmask_b32 v143, v190, v143, vcc\n\ts_nop 1\n\ts_mov_b64 vcc, %[sv]\n\ts_branch 62b\n\t64:\nv_exp_f32 v112, v112\n\tv_exp_f32 v113, v113\n\tv_add_f32 v184, v184, v112\n\t" \
        "v_exp_f32 v114, v114\n\tv_add_f32 v185, v185, v113\n\tv_exp_f32 v115, v115\n\tv_add_f32 v184, v184, v114\n\tv_exp_f32 v116, v116\n\tv_cvt_pk_bf16_f32 v144, v112, v113\n\t" \
        "v_exp_f32 v117, v117\n\tds_read_b64_tr_b16 v[152:153], v188 offset:8192\n\tds_read_b64_tr_b16 v[154:155], v189 offset:8192\n\tv_add_f32 v185, v185, v115\n\t" \
        "v_cvt_pk_bf16_f32 v145, v114, v115\n\tv_exp_f32 v118, v118\n\tv_add_f32 v184, v184, v116\n\tds_read_b64_tr_b16 v[156:157], v188 offset:10240\n\t" \
        "ds_read_b64_tr_b16 v[158:159], v189 offset:10240\n\tv_cvt_pk_bf16_f32 v146, v116, v117\n\tv_exp_f32 v119, v119\n\tv_add_f32 v185, v185, v117\n\t" \
        "ds_read_b64_tr_b16 v[160:161], v188 offset:12288\n\tds_read_b64_tr_b16 v[162:163], v189 offset:12288\n\tv_exp_f32 v120, v120\n\tv_add_f32 v184, v184, v118\n\t" \
        "v_cvt_pk_bf16_f32 v147, v118, v119\n\tds_read_b64_tr_b16 v[164:165], v188 offset:14336\n\tds_read_b64_tr_b16 v[166:167], v189 offset:14336\n\tv_exp_f32 v121, v121\n\tv_exp_f32 v122, v122\n\t" \
        "s_waitcnt lgkmcnt(6)\n\tv_mfma_f32_32x32x16_bf16 v[0:15], v[152:155], v[144:147], v[0:15]\n\tds_read_b64_tr_b16 v[168:169], v188 offset:9216\n\t" \
        "ds_read_b64_tr_b16 v[170:171], v189 offset:9216\n\tv_cvt_pk_bf16_f32 v148, v120, v121\n\tv_exp_f32 v123, v123\n\tv_exp_f32 v124, v124\n\ts_waitcnt lgkmcnt(6)\n\t" \
        "v_mfma_f32_32x32x16_bf16 v[16:31], v[156:159], v[144:147], v[16:31]\n\tds_read_b64_tr_b16 v[172:173], v188 offset:11264\n\tds_read_b64_tr_b16 v[174:175], v189 offset:11264\n\t" \
        "v_cvt_pk_bf16_f32 v149, v122, v123\n\tv_exp_f32 v125, v125\n\ts_waitcnt lgkmcnt(6)\n\tv_mfma_f32_32x32x16_bf16 v[32:47], v[160:163], v[144:147], v[32:47]\n\t" \
        "ds_read_b64_tr_b16 v[176:177], v188 offset:13312\n\tds_read_b64_tr_b16 v[178:179], v189 offset:13312\n\tv_exp_f32 v126, v126\n\tv_cvt_pk_bf16_f32 v150, v124, v125\n\tv_exp_f32 v127, v127\n\t" \
        "s_waitcnt lgkmcnt(6)\n\tv_mfma_f32_32x32x16_bf16 v[48:63], v[164:167], v[144:147], v[48:63]\n\tds_read_b64_tr_b16 v[180:181], v188 offset:15360\n\t" \
        "ds_read_b64_tr_b16 v[182:183], v189 offset:15360\n\tv_add_f32 v185, v185, v119\n\tv_cvt_pk_bf16_f32 v151, v126, v127\n\tv_add_f32 v184, v184, v120\n\tv_add_f32 v185, v185, v121\n\t" \
        "s_waitcnt lgkmcnt(6)\n\tv_mfma_f32_32x32x16_bf16 v[0:15], v[168:171], v[148:151], v[0:15]\n\tv_add_f32 v184, v184, v122\n\ts_waitcnt lgkmcnt(4)\n\t" \
        "v_mfma_f32_32x32x16_bf16 v[16:31], v[172:175], v[148:151], v[16:31]\n\tv_add_f32 v185, v185, v123\n\tv_add_f32 v184, v184, v124\n\ts_waitcnt lgkmcnt(2)\n\t" \
        "v_mfma_f32_32x32x16_bf16 v[32:47], v[176:179], v[148:151], v[32:47]\n\tv_add_f32 v185, v185, v125\n\ts_waitcnt lgkmcnt(0)\n\t" \
        "v_mfma_f32_32x32x16_bf16 v[48:63], v[180:183], v[148:151], v[48:63]\n\tv_add_f32 v184, v184, v126\n\tv_add_f32 v185, v185, v127\n\ts_branch 99f\n\t99:\ns_mov_b32 m0, %[keep]\n\t" \
        "v_add_f32 v184, v184, v185\n\ts_nop 10\n\t"
#define CL8(a, b, c, d, e, f, g, h) "v" #a, "v" #b, "v" #c, "v" #d, "v" #e, "v" #f, "v" #g, "v" #h
#define CL128_159 CL8(128, 129, 130, 131, 132, 133, 134, 135), CL8(136, 137, 138, 139, 140, 141, 142, 143), CL8(144, 145, 146, 147, 148, 149, 150, 151), CL8(152, 153, 154, 155, 156, 157, 158, 159)
#define CL160_191 CL8(160, 161, 162, 163, 164, 165, 166, 167), CL8(168, 169, 170, 171, 172, 173, 174, 175), CL8(176, 177, 178, 179, 180, 181, 182, 183), CL8(184, 185, 186, 187, 188, 189, 190, 191)
#define CL16_63 CL8(16, 17, 18, 19, 20, 21, 22, 23), CL8(24, 25, 26, 27, 28, 29, 30, 31), CL8(32, 33, 34, 35, 36, 37, 38, 39), CL8(40, 41, 42, 43, 44, 45, 46, 47), CL8(48, 49, 50, 51, 52, 53, 54, 55), CL8(56, 57, 58, 59, 60, 61, 62, 63)
#define CL64_127 CL8(64, 65, 66, 67, 68, 69, 70, 71), CL8(72, 73, 74, 75, 76, 77, 78, 79), CL8(80, 81, 82, 83, 84, 85, 86, 87), CL8(88, 89, 90, 91, 92, 93, 94, 95), \
                 CL8(96, 97, 98, 99, 100, 101, 102, 103), CL8(104, 105, 106, 107, 108, 109, 110, 111), CL8(112, 113, 114, 115, 116, 117, 118, 119), CL8(120, 121, 122, 123, 124, 125, 126, 127)
#define CL224_251 CL8(224, 225, 226, 227, 228, 229, 230, 231), CL8(232, 233, 234, 235, 236, 237, 238, 239), CL8(240, 241, 242, 243, 244, 245, 246, 247), "v248", "v249", "v250", "v251"
#define CL192_223 CL8(192, 193, 194, 195, 196, 197, 198, 199), CL8(200, 201, 202, 203, 204, 205, 206, 207), CL8(208, 209, 210, 211, 212, 213, 214, 215), CL8(216, 217, 218, 219, 220, 221, 222, 223)
#define W128(w) "{v[0:15]}"(w[0]), "{v[16:31]}"(w[1]), "{v[32:47]}"(w[2]), "{v[48:63]}"(w[3]), "{v[64:79]}"(w[4]), "{v[80:95]}"(w[5]), "{v[96:111]}"(w[6]), "{v[112:127]}"(w[7])
__device__ __forceinline__ u32x16 cat16(bf16x8 a, bf16x8 b, bf16x8 c, bf16x8 d) {
    const u32x8 x = __builtin_shufflevector(__builtin_bit_cast(u32x4, a), __builtin_bit_cast(u32x4, b), 0, 1, 2, 3, 4, 5, 6, 7);
    const u32x8 y = __builtin_shufflevector(__builtin_bit_cast(u32x4, c), __builtin_bit_cast(u32x4, d), 0, 1, 2, 3, 4, 5, 6, 7);
    return __builtin_shufflevector(x, y, 0, 1, 2, 3, 4, 5, 6, 7, 8, 9, 10, 11, 12, 13, 14, 15); }
#define XIMG(n) ((((unsigned)(n) + 3u) & 3u) * 32768u + 32768u)
template <bool PUB>
__device__ __forceinline__ void proj_ws(const bf16* __restrict__ A, const bf16* __restrict__ W, bf16* __restrict__ out, int c0, int cs,
                                        unsigned* prog, float sc, const Hello& hi, char* lds, int tid) {
    const int wid = __builtin_amdgcn_readfirstlane(tid >> 6), lane = tid & 63, l16 = lane & 15, g4 = lane >> 4;
    const unsigned lbase = (unsigned)(uintptr_t)lds;
    unsigned long long hv = 0;
    const u32x4 rsA = mk_rs(A, SEQ * DM * 2);
    const unsigned dvo = (unsigned)(((wid >> 1) * 8 + (lane & 7)) * 1024 + ((wid & 1) * 32 + (lane >> 3)) * 16);
    auto fetch = [&](int ch, int n) {
        const unsigned so = (unsigned)(min(ch, NSEG - 1) * 32768);
#pragma unroll
        for (int i = 0; i < 4; ++i) dma16<false>(rsA, dvo, so + i * 128, lbase + XIMG(n) + (4 * wid + i) * 1024); };
    fetch(c0, 0);
    const int fl = WFLIP ? c0 : 0;
    bf16x8 wf[2][16];
    u32x4 st[2][16];
#pragma unroll
    for (int i = 0; i < 32; ++i) st[i >> 4][i & 15] = ldw(W, (tid >> 4) * (HD * 2) + (tid & 15) * 16, (i & 15) * (HD * 64) + ((i >> 4) ^ fl) * 256);
#pragma unroll
    for (int rnd = 0; rnd < 2; ++rnd) {
        if (rnd) __syncthreads();
#pragma unroll
        for (int i = 0; i < 16; ++i) { const int id = tid + 512 * i; const int r = id >> 4, c = id & 15; *(u32x4*)(lds + (r >> 5) * 8192 + off_b(r & 31, c)) = st[rnd][i]; }
        __syncthreads();
        if (PUB && wid == 0) { if (rnd == 0) hv = hi.peek(); else hi.wait(hv); }
        if ((wid >> 2) == (rnd ^ fl)) {
            const unsigned q = (lane & 15) >> 2, pp = lane & 3;
#pragma unroll
            for (int t = 0; t < 2; ++t)
#pragma unroll
                for (int ks = 0; ks < 16; ++ks)
                    wf[t][ks] = cat8(tr_rd(lds + ks * 8192 + off_b(8 * g4 + q, 4 * (wid & 3) + pp) + 8 * t), tr_rd(lds + ks * 8192 + off_b(8 * g4 + 4 + q, 4 * (wid & 3) + pp) + 8 * t));
        }
    }
    __syncthreads();
    fetch(c0 + cs, 1); fetch(c0 + 2 * cs, 2);
    u32x16 w[8];
#pragma unroll
    for (int i = 0; i < 8; ++i) w[i] = cat16(wf[i >> 2][(i & 3) * 4], wf[i >> 2][(i & 3) * 4 + 1], wf[i >> 2][(i & 3) * 4 + 2], wf[i >> 2][(i & 3) * 4 + 3]);
    VMWAIT(8);
    __builtin_amdgcn_s_barrier(); asm volatile("" ::: "memory");
    PRB(PUB ? 8 : 3);
    unsigned n, keep, im, t0, t1, sp = (unsigned)(c0 - cs) << 18, so = (unsigned)(c0 + 3 * cs) << 15;
#define PROJ_OPS : [n] "=&s"(n), [keep] "=&s"(keep), [im] "=&s"(im), [t0] "=&s"(t0), [t1] "=&s"(t1), [sp] "+s"(sp), [so] "+s"(so) \
                 : W128(w), [xb] "v"(lbase + 32768 + (l16 >> 3) * 8192 + g4 * 128 + (l16 & 7) * 16), [lb] "s"(pin_s(lbase + 32768 + wid * 4096)), \
                   [co] "v"((unsigned)(wid * 2048 + lane * 16)), [rc] "s"(mk_rs(out, SEQ * HD * 2)), [ra] "s"(rsA), [dv] "v"(dvo), \
                   [em] "s"(__builtin_amdgcn_ballot_w64(tid == 0)), [pg] "s"(prog), [sc] "s"(sc) \
                 : "memory", "scc", CL128_159, CL160_191
    if (PUB) asm volatile(ASM_PROJ_PUB PROJ_OPS); else asm volatile(ASM_PROJ_A PROJ_OPS);
    VMWAIT(0);
    __syncthreads();
    if (PUB && tid == 0) __hip_atomic_store(prog, (unsigned)n, __ATOMIC_RELAXED, __HIP_MEMORY_SCOPE_AGENT);
}

namespace attn {
constexpr float C2 = 1.4426950408889634f * ATT_SCALE;
#define SBAR() __builtin_amdgcn_sched_barrier(0)
template <class T>
__device__ __forceinline__ u32x4 pk4(const T& P, int B) {
    const unsigned a0 = cvtpk(P[B + 0], P[B + 1]), a1 = cvtpk(P[B + 2], P[B + 3]), b0 = cvtpk(P[B + 4], P[B + 5]), b1 = cvtpk(P[B + 6], P[B + 7]);
    const auto r0 = __builtin_amdgcn_permlane32_swap(a0, b0, false, false), r1 = __builtin_amdgcn_permlane32_swap(a1, b1, false, false);
    return u32x4{r0[0], r1[0], r0[1], r1[1]}; }
__device__ __forceinline__ void listed(unsigned* list, int b, unsigned c) {
    if (c != (b < 24 ? 5u : 6u)) return;
    list += (b >> 3) * 64;
    const unsigned p = __hip_atomic_fetch_add(list, 1u, __ATOMIC_RELAXED, __HIP_MEMORY_SCOPE_AGENT);
    __hip_atomic_fetch_add(list + 32, ((unsigned)(b & 7) + 1u) << (p * 4), __ATOMIC_RELAXED, __HIP_MEMORY_SCOPE_AGENT); }
__device__ __forceinline__ int pick(unsigned v, int own) {
    int best = 8, k = -1;
#pragma unroll
    for (int i = 0; i < 5; ++i) { const int c = (int)__builtin_amdgcn_readlane(v, i), lv = min(3 + (i < 3 ? c / 6 : c / 7), 8) - (i == own ? SLACK : 0);
        if (c < (i < 3 ? 30 : 35) && (lv < best || (lv == best && i == own))) { best = lv; k = i; } }
    return k; }
__device__ __forceinline__ int causal_block(int h, int qb, char* qkv, char* lds, unsigned* queue, int own, unsigned* qc, unsigned* list, int prev, volatile LAS int* lq, int seq) {
    int tid = threadIdx.x; asm volatile("" : "+v"(tid));
    const int wid = __builtin_amdgcn_readfirstlane(tid >> 6), lane = tid & 63, r32 = lane & 31, hi = lane >> 5;
    const int rho = wid < 4 ? wid : 11 - wid;
    const unsigned lbase = (unsigned)(uintptr_t)lds;
    const u32x4 rs = mk_rs(qkv, (unsigned)(3 * SZ_ACT));
    const __amdgpu_buffer_rsrc_t rsQ = __builtin_amdgcn_make_buffer_rsrc((void*)(qkv + 3 * SZ_ACT), 0, (int)SZ_ACT, 0x00020000);
    const unsigned ko = lane * 16, vo = (lane & 48) * 16 + ((lane & 15) ^ ((lane >> 4) << 2)) * 16;
    const unsigned sb = (unsigned)(((wid >> 2) * 32 + h) * 8192 + (wid & 3) * 2048) + (unsigned)SZ_ACT;
    const unsigned fo = (r32 >> 4) * 1024 + hi * 256 + (r32 & 15) * 16, qso = (unsigned)(((qb * 8 + rho) * 32 + h) * 8192);
    u32x16 q0, q1;
    unsigned cq = 0;
    asm volatile("s_mov_b64 exec, %3\n\tglobal_atomic_add %0, %1, %2, off sc0 sc1\n\ts_mov_b64 exec, -1"
                 : "+v"(cq) : "v"(qc + max(prev, 0) * 32), "v"(1u), "s"(__builtin_amdgcn_ballot_w64(tid == 0 && prev >= 0)) : "memory");
    {   unsigned keep, so = sb, t0;
#define QLD(r, v, o) "buffer_load_dwordx4 v[" r "], %[" v "], %[rs], %[qs] offen offset:" o " sc1\n\t"
#define KVLD "s_add_u32 %[t0], %[so], 0x1000000\n\tbuffer_load_dwordx4 %[ko], %[rs], %[so] offen sc1 lds\n\tbuffer_load_dwordx4 %[ko], %[rs], %[so] offen offset:1024 sc1 lds\n\t" \
             "s_add_u32 m0, m0, 0x4000\n\ts_add_u32 %[so], %[so], 0x80000\n\tbuffer_load_dwordx4 %[vo], %[rs], %[t0] offen sc1 lds\n\tbuffer_load_dwordx4 %[vo], %[rs], %[t0] offen offset:1024 sc1 lds\n\t" \
             "s_add_u32 m0, m0, 0x4000\n\t"
        asm volatile("s_mov_b32 %[keep], m0\n\ts_mov_b32 m0, %[lb]\n\t"
                     QLD("64:67", "f0", "0") QLD("68:71", "f0", "512") QLD("72:75", "f0", "2048") QLD("76:79", "f0", "2560")
                     QLD("80:83", "f1", "0") QLD("84:87", "f1", "512") QLD("88:91", "f1", "2048") QLD("92:95", "f1", "2560")
                     KVLD KVLD KVLD "s_mov_b32 m0, %[keep]\n\ts_waitcnt vmcnt(10)"
                     : "=&{v[64:79]}"(q0), "=&{v[80:95]}"(q1), [keep] "=&s"(keep), [so] "+s"(so), [t0] "=&s"(t0), "+v"(cq)
                     : [f0] "v"(fo), [f1] "v"(fo + 4096), [rs] "s"(rs), [qs] "s"(qso), [ko] "v"(ko), [vo] "v"(vo), [lb] "s"(pin_s(lbase + wid * 2048)) : "memory", "scc"); }
    listed(list, prev, cq);
    const unsigned g4v = ((lane >> 3) & 2) | ((lane >> 1) & 1);
    f32x16 o[4]; float l_reg;
    {   unsigned keep, sk, t0, so = sb + 3 * 524288u, rem = (unsigned)(8 * qb + rho), tb = (unsigned)(4 * qb + 1); unsigned long long sv;
        asm volatile(ASM_ATT
                     : "=&{v[0:15]}"(o[0]), "=&{v[16:31]}"(o[1]), "=&{v[32:47]}"(o[2]), "=&{v[48:63]}"(o[3]), "=&{v184}"(l_reg),
                       [keep] "=&s"(keep), [sk] "=&s"(sk), [t0] "=&s"(t0), [sv] "=&s"(sv), [so] "+s"(so), [rem] "+s"(rem), [tb] "+s"(tb)
                     : "{v[64:79]}"(q0), "{v[80:95]}"(q1), [fo] "v"(lbase + fo), [vb] "v"(lbase + 16384 + g4v * 256 + (hi ^ g4v) * 64 + ((lane >> 2) & 3) * 16 + (lane & 1) * 8),
                       [ko] "v"(ko), [vo] "v"(vo), [rs] "s"(rs), [lb] "s"(pin_s(lbase + wid * 2048)), [dq] "v"(r32 - 4 * hi)
                     : "memory", "scc", "vcc", CL8(96, 97, 98, 99, 100, 101, 102, 103), CL8(104, 105, 106, 107, 108, 109, 110, 111), CL8(112, 113, 114, 115, 116, 117, 118, 119),
                       CL8(120, 121, 122, 123, 124, 125, 126, 127), CL128_159, CL8(160, 161, 162, 163, 164, 165, 166, 167), CL8(168, 169, 170, 171, 172, 173, 174, 175),
                       CL8(176, 177, 178, 179, 180, 181, 182, 183), "v185", "v186", "v187", "v188", "v189", "v190"); }
    if (wid < 2) { VMWAIT(0); __builtin_amdgcn_s_barrier(); asm volatile("" ::: "memory"); }
    {
        auto rr = __builtin_amdgcn_permlane32_swap(__float_as_uint(l_reg), __float_as_uint(l_reg), false, false);
        const float rl = __builtin_amdgcn_rcpf(__uint_as_float(rr[0]) + __uint_as_float(rr[1]));
#pragma unroll
        for (int d = 0; d < 4; ++d) {
            o[d] *= rl;
            __builtin_amdgcn_raw_buffer_store_b128(pk4(o[d], 0), rsQ, fo + d * 2048, qso, 16); __builtin_amdgcn_raw_buffer_store_b128(pk4(o[d], 8), rsQ, fo + d * 2048 + 512, qso, 16);
        }
    }
    if (wid == 0) {
        const unsigned v = __hip_atomic_load(queue + min(lane, 5) * 32, __ATOMIC_RELAXED, __HIP_MEMORY_SCOPE_AGENT), pa = __builtin_amdgcn_readlane(v, 5);
        int k = (pa & 0xFFFFu) >= NPA ? pick(v, own) : -2;
        k = k == -1 && pa < (32u << 16) ? -2 : k;
        if (lane == 0) {
            lq[2] = seq * 8 + k + 2;
            if (k != -1) lq[0] = k < 0 ? 64 : (int)__hip_atomic_fetch_add(queue + k * 32, 1u, __ATOMIC_RELAXED, __HIP_MEMORY_SCOPE_AGENT); } }
    int k;
#pragma unroll 1
    for (int i = 0; i < (1 << 20); ++i) { k = __builtin_amdgcn_readfirstlane(lq[2]); if ((k >> 3) == seq) break; __builtin_amdgcn_s_sleep(2); }
    k = (k & 7) - 2;
    if (k == -1) return k;
    VMWAIT(0);
    return k;
}
__device__ __forceinline__ int attn_phase(char* qkv, unsigned* queue, unsigned* qcnt, unsigned* list, int own, int k, int L, unsigned* tmo, char* lds, volatile LAS int* lq) {
    int prev = -1, seq = 0;
    if (threadIdx.x == 0) lq[2] = 0;
#pragma unroll 1
    for (;;) {
        if (L < 0) {
            __syncthreads();
            if (threadIdx.x < 64) {
                if (threadIdx.x == 0 && prev >= 0) listed(list, prev, __hip_atomic_fetch_add(qcnt + prev * 32, 1u, __ATOMIC_RELAXED, __HIP_MEMORY_SCOPE_AGENT));
                wave_wait<8>(queue + 160, NPA, tmo, 0x30000u, 0xFFFFu);
                for (;;) {
                    k = pick(__hip_atomic_load(queue + min((int)threadIdx.x, 5) * 32, __ATOMIC_RELAXED, __HIP_MEMORY_SCOPE_AGENT), own);
                    if (k < 0) break;
                    if (threadIdx.x == 0) L = (int)__hip_atomic_fetch_add(queue + k * 32, 1u, __ATOMIC_RELAXED, __HIP_MEMORY_SCOPE_AGENT);
                    L = __builtin_amdgcn_readfirstlane(L);
                    if (L < (k < 3 ? 30 : 35)) break; }
                if (k < 0) wave_wait<8>(queue + 160, 32u << 16, tmo, 0x30002u);
                if (threadIdx.x == 0) { lq[0] = L; lq[1] = k; } }
            prev = -1;
            __syncthreads();
            k = __builtin_amdgcn_readfirstlane(lq[1]);
            if (k < 0) return -1;
            L = __builtin_amdgcn_readfirstlane(lq[0]) + (k < 3 ? 18 : 21);
        }
        const int r = k < 3 ? L / 6 : L / 7;
        const int kn = causal_block(k < 3 ? 6 * (k - r) + L : 7 * (k - r) - 3 + L, 7 - r, qkv, lds, queue, own, qcnt, list, prev, lq, ++seq);
        prev = 8 * k + 7 - r;
        if (kn == -1) return prev;
        __syncthreads();
        k = kn;
        L = __builtin_amdgcn_readfirstlane(lq[0]);
        L = L < (k < 3 ? 30 : 35) ? L + (k < 3 ? 18 : 21) : -1;
    }
}
}

constexpr int AUX_SC1 = 16;
constexpr int CHAIN_LDS = 163840;
__device__ __forceinline__ unsigned umax4(u32x4 v) { return max(max(v[0], v[1]), max(v[2], v[3])); }
__device__ __forceinline__ unsigned uor4(u32x4 v) { return (v[0] | v[1]) | (v[2] | v[3]); }
__device__ __forceinline__ unsigned canon(unsigned w) { return min(w, 0xFFFFFFFEu); }
__device__ __forceinline__ s16x4 pack4(f32x4 v) { const u32x2 w = {cvtpk(v[0], v[1]), cvtpk(v[2], v[3])}; return __builtin_bit_cast(s16x4, w); }
__device__ __forceinline__ f32x4 mfma32(u32x4 a, u32x4 b, f32x4 c) {
    return __builtin_amdgcn_mfma_f32_16x16x32_bf16(__builtin_bit_cast(bf16x8, a), __builtin_bit_cast(bf16x8, b), c, 0, 0, 0); }
__device__ __forceinline__ float xg_max(float x) {
    { auto r = __builtin_amdgcn_permlane16_swap(__float_as_uint(x), __float_as_uint(x), false, false); x = fmaxf(__uint_as_float(r[0]), __uint_as_float(r[1])); }
    { auto r = __builtin_amdgcn_permlane32_swap(__float_as_uint(x), __float_as_uint(x), false, false); x = fmaxf(__uint_as_float(r[0]), __uint_as_float(r[1])); }
    return x; }
__device__ __forceinline__ float xg_sum(float x) {
    { auto r = __builtin_amdgcn_permlane16_swap(__float_as_uint(x), __float_as_uint(x), false, false); x = __uint_as_float(r[0]) + __uint_as_float(r[1]); }
    { auto r = __builtin_amdgcn_permlane32_swap(__float_as_uint(x), __float_as_uint(x), false, false); x = __uint_as_float(r[0]) + __uint_as_float(r[1]); }
    return x; }
template <int IMM>
__device__ __forceinline__ void mload(u32x4& v, unsigned long long m, unsigned off, u32x4 rs, unsigned so) {
    asm volatile("s_mov_b64 exec, %1\n\tbuffer_load_dwordx4 %0, %2, %3, %4 offen offset:%5 sc1\n\ts_mov_b64 exec, -1"
                 : "+v"(v) : "s"(m), "v"(off), "s"(rs), "s"(so), "i"(IMM) : "memory");
}
template <int IMM>
__device__ __forceinline__ void aload(u32x4& v, unsigned off, u32x4 rs, unsigned so) {
    asm volatile("buffer_load_dwordx4 %0, %1, %2, %3 offen offset:%4 sc1" : "=v"(v) : "v"(off), "s"(rs), "s"(so), "i"(IMM) : "memory");
}
template <int N, int PRE, int... IMM>
__device__ __forceinline__ unsigned poll16(u32x4 rs, const unsigned (&off)[N], const unsigned (&sa)[N], u32x4 (&v)[N],
                                           unsigned* tmo, unsigned code, unsigned long long live = ~0ull) {
    constexpr int imm[N] = {IMM...};
    unsigned long long m[N];
    if (PRE >= 0) {
        unsigned x = 0, y = 0;
#pragma unroll
        for (int i = 0; i < N; ++i) { if (i < PRE) x = max(x, umax4(v[i])); else y = max(y, umax4(v[i])); }
        if (__builtin_expect((__builtin_amdgcn_ballot_w64(x == 0xFFFFFFFFu) | (__builtin_amdgcn_ballot_w64(y == 0xFFFFFFFFu) & live)) == 0, 1)) return 0;
    }
#pragma unroll
    for (int i = 0; i < N; ++i) m[i] = PRE < 0 ? ~0ull : __builtin_amdgcn_ballot_w64(umax4(v[i]) == 0xFFFFFFFFu) & (i < PRE ? ~0ull : live);
    unsigned spins = PRE >= 0 ? 1 : 0;
    for (;; ++spins) {
        [&]<int... I>(std::integer_sequence<int, I...>) { (mload<imm[I]>(v[I], m[I], off[I], rs, sa[I]), ...); }(std::make_integer_sequence<int, N>{});
        unsigned long long any = 0;
#pragma unroll
        for (int i = 0; i < N; ++i) {
            asm volatile("s_waitcnt vmcnt(0)" : "+v"(v[i]) :: "memory");
            m[i] = __builtin_amdgcn_ballot_w64(umax4(v[i]) == 0xFFFFFFFFu) & m[i]; any |= m[i]; }
        if (any == 0) break;
        if (bail(spins, SPIN_CAP, tmo, code)) break;
    }
    return spins;
}
__device__ __forceinline__ void until(unsigned t0, unsigned dt) {
    for (;;) { const unsigned e = (unsigned)__builtin_amdgcn_s_memtime() - t0; if (e >= dt) break; if (dt - e > 160u) __builtin_amdgcn_s_sleep(1); } }
constexpr unsigned ANCHOR = 5120;
__device__ __forceinline__ void until_a(unsigned aw, int s, unsigned da, unsigned t0, unsigned dt) {
    if (!ANCH || (aw & 63u) != (unsigned)(s & 63) || aw == ~0u) { until(t0, dt); return; }
    const unsigned tg = (aw & ~63u) + (da << 6);
    for (int i = 0; i < 200; ++i) {
        const int e = (int)(tg - ((unsigned)__builtin_amdgcn_s_memrealtime() << 6));
        if (e <= 0) break;
        if (e > (8 << 6)) __builtin_amdgcn_s_sleep(1); } }
__device__ __forceinline__ void cnt_add(unsigned* w) { __hip_atomic_fetch_add(w, 1u, __ATOMIC_RELAXED, __HIP_MEMORY_SCOPE_AGENT); }
#define DUR(k, v) do {} while (0)
#define DURT() 0u
#define DUROUT(r) do {} while (0)
#define ST(i) do {} while (0)
#define SP(i, x) x
#define GST(i) do {} while (0)

__device__ __forceinline__ void oproj_ws(int kq, int nq, unsigned* queue, const bf16* __restrict__ src, const bf16* __restrict__ W, float* __restrict__ part,
                                         int prev, unsigned* tmo, char* lds, int tid) {
    const int wid = __builtin_amdgcn_readfirstlane(tid >> 6), lane = tid & 63;
    volatile LAS unsigned* lf = (volatile LAS unsigned*)(lds + 140032);
    unsigned* const list = tmo + W_LIST + 64 * kq + 32, * const fin = tmo + W_FA;
    const int hb = kq < 3 ? 6 * kq : 7 * kq - 3;
    const u32x4 rsA = mk_rs(src, (unsigned)SZ_ACT), rsC = mk_rs(part + (size_t)kq * SEQ * DM, SEQ * DM * 4);
    const unsigned lbase = (unsigned)(uintptr_t)lds, co = (unsigned)((nq * 8 + wid) * 2048 + lane * 16), sb = pin_s(hb * 8192 + wid * 1024);
    auto fetch = [&](int ch, unsigned im) {
#pragma unroll
        for (int i = 0; i < 7; ++i) if (i < 6 || kq >= 3) dma16<false>(rsA, (unsigned)lane * 16u, (unsigned)(ch * 262144 + i * 8192) + sb, lbase + im * 65536 + (i * 8 + wid) * 1024); };
    int ch = 0, last = -1, dr = -1;
    unsigned pos = 0, e = 0, im = 1;
    u32x16 w[7];
    {   unsigned c = 0, keep, so;
        asm volatile(ASM_OPRE : "=&{v[0:15]}"(w[0]), "=&{v[16:31]}"(w[1]), "=&{v[32:47]}"(w[2]), "=&{v[48:63]}"(w[3]), "=&{v[64:79]}"(w[4]), "=&{v[80:95]}"(w[5]), "=&{v[96:111]}"(w[6]),
                       [c] "+v"(c), [keep] "=&s"(keep), [pos] "=&s"(pos), [e] "=&s"(e), [nx] "=&s"(ch), [so] "=&s"(so)
                     : [em] "s"(__builtin_amdgcn_ballot_w64(tid == 0)), [ep] "s"(__builtin_amdgcn_ballot_w64(tid == 0 && prev >= 0)), [qu] "s"(queue), [li] "s"(list),
                       [qc] "s"(tmo + W_QCNT + max(prev, 0) * 32), [wb] "s"(pin_s(((nq * 8 + wid) * 128 + hb * 4) * 1024)), [rw] "s"(mk_rs(W, HD * DM * 2)), [dv] "v"((unsigned)lane * 16u),
                       [rem] "s"(0), [fr] "v"(lbase + 140040), [lb] "s"(pin_s(lbase + 65536 + wid * 1024)), [sb] "s"(sb), [kq] "s"(kq), [ra] "s"(rsA)
                     : "memory", "scc", "v168", "v169", "v172", "v173");
        attn::listed(tmo + W_LIST, prev, c); }
#pragma unroll 1
    for (;; im ^= 1u) {
        if (e == 0u) {
            VMWAIT(0);
            LDS_BARRIER();
            if (tid == 0) { if (dr >= 0) cnt_add(fin + (dr >> 2)); if (last >= 0) cnt_add(fin + (last >> 2)); }
            dr = -1; last = -1;
            if (pos >= (unsigned)NSEG) break;
            if (wid == 0) {
                const unsigned lw = wave_wait<20>(list, 1u, tmo, 0x60000u + pos, 15u << ((pos >> 3) * 4));
                if (lane == 0) lf[im * 2 + 1] = lw; }
            LDS_BARRIER();
            ch = (int)(((__builtin_amdgcn_readfirstlane(lf[im * 2 + 1]) >> ((pos >> 3) * 4)) - 1u) & 7u) * 8 + (int)(pos & 7u);
            fetch(ch, im);
            VMWAIT(0);
        }
        __builtin_amdgcn_s_barrier(); asm volatile("" ::: "memory");
        unsigned keep, nx, so;
        asm volatile(ASM_OPROJ : [keep] "=&s"(keep), [pos] "=&s"(pos), [e] "=&s"(e), [nx] "=&s"(nx), [so] "=&s"(so)
                     : "{v[0:15]}"(w[0]), "{v[16:31]}"(w[1]), "{v[32:47]}"(w[2]), "{v[48:63]}"(w[3]), "{v[64:79]}"(w[4]), "{v[80:95]}"(w[5]), "{v[96:111]}"(w[6]),
                       [xa] "v"(lbase + im * 65536 + lane * 16), [fr] "v"(lbase + 140032 + im * 8), [fw] "v"(lbase + 140040 - im * 8),
                       [em] "s"(__builtin_amdgcn_ballot_w64(tid == 0)), [dr] "s"(dr), [fin] "s"(fin), [qu] "s"(queue), [li] "s"(list),
                       [lb] "s"(pin_s(lbase + 65536 - im * 65536 + wid * 1024)), [sb] "s"(sb), [ch] "s"(ch), [kq] "s"(kq), [dv] "v"((unsigned)lane * 16u),
                       [ra] "s"(rsA), [co] "v"(co), [rc] "s"(rsC)
                     : "memory", "scc", "vcc", CL128_159, CL160_191);
        dr = last; last = ch * 128; ch = (int)nx;
    }
}
__device__ __forceinline__ unsigned slot_off(unsigned j, unsigned pv) {
    const unsigned o = j * 16384u; return o + (((pv >> (((pv & 0x1FC000u) + o) >> 21)) & 1u) << 13); }
__device__ __forceinline__ unsigned calibrate(__amdgpu_buffer_rsrc_t rs, unsigned pg, unsigned pv0, unsigned* dbg) {
    const unsigned first = pg == 0 ? 0u : (pg << 21) - pv0;
    if (first + 16384u > (unsigned)SZ_RING) return 0u;
    unsigned best[2] = {~0u, ~0u};
    if (CALA) {
        unsigned v = 0;
        if ((threadIdx.x & 63) < 2 * CALN) v = (unsigned)__builtin_amdgcn_raw_ptr_buffer_atomic_add_i32(0, rs, first + (threadIdx.x & 1) * 8192u + ((threadIdx.x & 63) >> 1) * 384u, 0, AUX_SC1);
        asm volatile("s_waitcnt vmcnt(0)" : "+v"(v) :: "memory"); }
#pragma unroll 1
    for (int i = 0; i < CALN; ++i) {
#pragma unroll
        for (int b = 0; b < 2; ++b) {
            const unsigned long long t0 = __builtin_amdgcn_s_memtime();
            asm volatile("s_waitcnt lgkmcnt(0)" ::: "memory");
            unsigned v = __builtin_amdgcn_raw_buffer_load_b32(rs, first + b * 8192u + (unsigned)i * 384u, 0, AUX_SC1);
            asm volatile("s_waitcnt vmcnt(0)" : "+v"(v) :: "memory");
            const unsigned long long t1 = __builtin_amdgcn_s_memtime();
            asm volatile("s_waitcnt lgkmcnt(0)" ::: "memory");
            best[b] = min(best[b], (unsigned)(t1 - t0));
        }
    }
    return best[1] < best[0] ? 1u : 0u;
}
__host__ __device__ __forceinline__ void role_map(int b, int& xg, int& jb) {
    const int g = (GMAP >> ((b & 7) * 4)) & 7, j = b >> 3;
    if (g >= 3) { xg = 3; jb = j * 5 + g - 3; }
    else if (MIX == 2) { xg = g == 2 ? 2 : (j + g) & 1; jb = g == 2 ? j : (j & ~1) + g; }
    else { xg = MIX ? (j + g) % 3 : g; jb = j; }
}
__device__ __forceinline__ void load_w(const bf16* __restrict__ W, u32x4 (&st)[16], int tid) {
#pragma unroll
    for (int i = 0; i < 16; ++i) st[i] = ldw(W, (tid >> 4) * (HD * 2) + (tid & 15) * 16, i * (HD * 64));
}
__device__ __forceinline__ void stage_w(const u32x4 (&st)[16], char* lds, int tid) {
#pragma unroll
    for (int i = 0; i < 16; ++i) { const int id = tid + 512 * i; const int r = id >> 4, c = id & 15; *(u32x4*)(lds + (r >> 5) * 8192 + off_b(r & 31, c)) = st[i]; }
}
__device__ __forceinline__ void tiles_w(u32x8 (&w)[16], const char* lds, int wid, int lane) {
    const unsigned g4 = lane >> 4, q2 = (lane & 15) >> 2, p2 = lane & 3;
#pragma unroll
    for (int t = 0; t < 2; ++t) {
        const unsigned c = 4 * (2 * t + ((wid >> 1) & 1)) + p2;
        const char* b0 = lds + off_b(4 * g4 + q2, c) + 8 * (wid & 1), * b1 = lds + off_b(16 + 4 * g4 + q2, c) + 8 * (wid & 1);
#pragma unroll
        for (int j = 0; j < 8; ++j) w[8 * t + j] = ilv(tr_rdu(b0 + 2 * j * 8192), tr_rdu(b0 + (2 * j + 1) * 8192), tr_rdu(b1 + 2 * j * 8192), tr_rdu(b1 + (2 * j + 1) * 8192)); }
}
#define W16(w) [w0] "v"(w[0]), [w1] "v"(w[1]), [w2] "v"(w[2]), [w3] "v"(w[3]), [w4] "v"(w[4]), [w5] "v"(w[5]), [w6] "v"(w[6]), [w7] "v"(w[7]), \
               [w8] "v"(w[8]), [w9] "v"(w[9]), [w10] "v"(w[10]), [w11] "v"(w[11]), [w12] "v"(w[12]), [w13] "v"(w[13]), [w14] "v"(w[14]), [w15] "v"(w[15])
__device__ __forceinline__ void vox_ws(const bf16* __restrict__ x, const bf16* __restrict__ Wv, const bf16* __restrict__ Wo, int h,
                                       __amdgpu_buffer_rsrc_t rsW, u32x4 rsWu, unsigned* done, const Hello& hi, char* lds, int tid) {
    const int wid = __builtin_amdgcn_readfirstlane(tid >> 6), lane = tid & 63, l16 = lane & 15, g4 = lane >> 4;
    const unsigned lbase = (unsigned)(uintptr_t)lds;
    const u32x4 rsA = mk_rs(x, SEQ * DM * 2);
    const unsigned dvo = (unsigned)(((wid >> 1) * 8 + (lane & 7)) * 1024 + ((wid & 1) * 32 + (lane >> 3)) * 16);
    auto fetch = [&](int n) {
        const unsigned so = (unsigned)(min(n, NSEG - 1) * 32768);
#pragma unroll
        for (int i = 0; i < 4; ++i) dma16<false>(rsA, dvo, so + i * 128, lbase + XIMG(n) + (4 * wid + i) * 1024); };
    fetch(0);
    bf16x8 wv[16], wo[4][4];
    {   u32x4 st[2][16];
        load_w(Wv + h * DH, st[0], tid);
#pragma unroll
        for (int i = 0; i < 16; ++i) st[1][i] = ldw(Wo + h * DH * DM, tid * 16, i * 8192);
        stage_w(st[0], lds, tid);
        __syncthreads();
        unsigned long long hv = 0;
        if (wid == 0) hv = hi.peek();
        const unsigned q = l16 >> 2, pp = lane & 3;
#pragma unroll
        for (int ks = 0; ks < 16; ++ks)
            wv[ks] = cat8(tr_rd(lds + ks * 8192 + off_b(8 * g4 + q, 4 * (wid >> 1) + pp) + 8 * (wid & 1)), tr_rd(lds + ks * 8192 + off_b(8 * g4 + 4 + q, 4 * (wid >> 1) + pp) + 8 * (wid & 1)));
        __syncthreads();
#pragma unroll
        for (int i = 0; i < 16; ++i) { const int id = tid + 512 * i; const int r = id >> 6, c = id & 63; *(u32x4*)(lds + ((r >> 5) * 4 + (c >> 4)) * 8192 + off_b(r & 31, c & 15)) = st[1][i]; }
        if (wid == 0) hi.wait(hv);
        __syncthreads();
#pragma unroll
        for (int t = 0; t < 4; ++t)
#pragma unroll
            for (int ks = 0; ks < 4; ++ks) { const char* img = lds + (ks * 4 + (wid >> 1)) * 8192;
                wo[t][ks] = cat8(tr_rd(img + tr_addr16(lane, 4 * (wid & 1) + t, 0)), tr_rd(img + tr_addr16(lane, 4 * (wid & 1) + t, 1))); }
    }
    __syncthreads();
    fetch(1); fetch(2); fetch(3);
    const unsigned xb = lbase + (l16 >> 3) * 16384 + g4 * 128 + (l16 & 7) * 16;
    const unsigned st = (unsigned)(OFF_VOX + (4 * wid * 32 + h) * 1024 + lane * 16);
    unsigned vw = lbase + (ASM_VOX_N0 & 1) * 8192 + (wid >> 1) * 2048 + lane * 16 + (wid & 1) * 8, vi = lbase + (~ASM_VOX_N0 & 1) * 8192 + lane * 16;
    u32x16 w[8];
#pragma unroll
    for (int i = 0; i < 4; ++i) { w[i] = cat16(wv[4 * i], wv[4 * i + 1], wv[4 * i + 2], wv[4 * i + 3]); w[4 + i] = cat16(wo[i][0], wo[i][1], wo[i][2], wo[i][3]); }
    auto wsub = [&](int i) { const u32x16 v = w[i >> 2]; const int b = (i & 3) * 4; return u32x4{v[b], v[b + 1], v[b + 2], v[b + 3]}; };
    auto first = [&](int n) {
        const unsigned xa = xb + XIMG(n);
        f32x4 a0 = f32x4{}, a1 = f32x4{};
        u32x4 xf[4][2];
#pragma unroll
        for (int k = 0; k < 3; ++k) { LRD(xf[k][0], xa, k * 512); LRD(xf[k][1], xa, 8192 + k * 512); }
#pragma unroll
        for (int ks = 0; ks < 16; ++ks) {
            if (ks + 3 < 16) { LRD(xf[(ks + 3) & 3][0], xa, (ks + 3) * 512); LRD(xf[(ks + 3) & 3][1], xa, 8192 + (ks + 3) * 512); LWAIT2(6, xf[ks & 3][0], xf[ks & 3][1]); }
            else if (ks + 2 < 16) LWAIT2(4, xf[ks & 3][0], xf[ks & 3][1]);
            else if (ks + 1 < 16) LWAIT2(2, xf[ks & 3][0], xf[ks & 3][1]);
            else LWAIT2(0, xf[ks & 3][0], xf[ks & 3][1]);
            a0 = mfma32(wsub(ks), xf[ks & 3][0], a0); a1 = mfma32(wsub(ks), xf[ks & 3][1], a1);
        }
        char* vo = lds + (n & 1) * 8192 + (wid >> 1) * 2048 + lane * 16 + (wid & 1) * 8;
        *(u32x2*)vo = u32x2{cvtpk(a0[0], a0[1]), cvtpk(a0[2], a0[3])};
        *(u32x2*)(vo + 1024) = u32x2{cvtpk(a1[0], a1[1]), cvtpk(a1[2], a1[3])}; };
    auto second = [&](int n) {
        const char* vp = lds + (n & 1) * 8192 + lane * 16;
        u32x4 vf[4][2];
#pragma unroll
        for (int i = 0; i < 8; ++i) vf[i >> 1][i & 1] = *(const u32x4*)(vp + i * 1024);
#pragma unroll
        for (int t = 0; t < 4; ++t) {
            f32x4 o0 = f32x4{}, o1 = f32x4{};
#pragma unroll
            for (int ks = 0; ks < 4; ++ks) { o0 = mfma32(vf[ks][0], wsub(16 + 4 * t + ks), o0); o1 = mfma32(vf[ks][1], wsub(16 + 4 * t + ks), o1); }
            __builtin_amdgcn_raw_buffer_store_b128(u32x4{cvtpk(o0[0], o0[1]), cvtpk(o0[2], o0[3]), cvtpk(o1[0], o1[1]), cvtpk(o1[2], o1[3])}, rsW, st + t * 32768, (unsigned)(n << 20), AUX_SC1);
        } };
    PRB(2);
#pragma unroll 1
    for (int m = 0;; ++m) {
        first(m);
        if (m == ASM_VOX_N0 - 1) break;
        if (m > 1) VMWAIT(0);
        LDS_BARRIER();
        if (m > 1 && tid == 0) cnt_add(done + 32 * (m - 1));
        fetch(m + 4);
        second(m);
        if (m == 0) { VMWAIT(0); LDS_BARRIER(); if (tid == 0) cnt_add(done); }
    }
    unsigned n, keep, im, t0, t1, t2, cnt = ASM_VOX_N0 > 2 ? ASM_VOX_N0 - 2 : 1, sp = (ASM_VOX_N0 - 1u) << 20, so = (ASM_VOX_N0 + 3u) << 15;
    asm volatile(ASM_VOX
                 : [n] "=&s"(n), [keep] "=&s"(keep), [im] "=&s"(im), [t0] "=&s"(t0), [t1] "=&s"(t1), [t2] "=&s"(t2), [cnt] "+s"(cnt), [sp] "+s"(sp), [so] "+s"(so), [vi] "+v"(vi), [vw] "+v"(vw)
                 : W128(w), [xb] "v"(xb + 32768), [lb] "s"(pin_s(lbase + 32768 + wid * 4096)), [st] "v"(st), [rw] "s"(rsWu), [ra] "s"(rsA), [dv] "v"(dvo),
                   [em] "s"(__builtin_amdgcn_ballot_w64(tid == 0)), [dn] "s"(done)
                 : "memory", "scc", CL128_159, CL160_191, CL192_223, "v224", "v225", "v226");
    LDS_BARRIER();
    second(NSEG - 1);
    VMWAIT(0);
    __syncthreads();
    if (tid == 0) for (; cnt < (unsigned)NSEG; ++cnt) cnt_add(done + 32 * cnt);
}
__device__ __forceinline__ u32x2 mix4(f32x4 a, f32x4 b, float g) {
    const float og = rbf(1.f - g);
    float r[4];
#pragma unroll
    for (int j = 0; j < 4; ++j) r[j] = rbf(g * rbf(a[j])) + rbf(og * b[j]);
    return u32x2{cvtpk(r[0], r[1]), cvtpk(r[2], r[3])}; }
__global__ __launch_bounds__(512, 2) void k_chain(const bf16* __restrict__ mem0, const bf16* __restrict__ Wq, const bf16* __restrict__ Wk,
                                                  const bf16* __restrict__ Wv, const bf16* __restrict__ Wo,
                                                  const bf16* __restrict__ x, char* __restrict__ ws,
                                                  const bf16* __restrict__ ckv, const bf16* __restrict__ awq, const bf16* __restrict__ awuk,
                                                  const bf16* __restrict__ awuv, const bf16* __restrict__ awo,
                                                  const bf16* __restrict__ gw, bf16* __restrict__ out) {
    extern __shared__ __attribute__((aligned(16))) char lds[];
    const int tid = threadIdx.x, wid = __builtin_amdgcn_readfirstlane(tid >> 6), lane = tid & 63;
    unsigned* tmo = (unsigned*)(ws + OFF_TMO);
    int xg, jb; role_map(blockIdx.x, xg, jb);
    char* const wsP = ws + ((0u - (unsigned)(uintptr_t)ws) & 16383u);
    unsigned pv = (unsigned)(uintptr_t)wsP & 0x1FC000u;
    const __amdgpu_buffer_rsrc_t rsP = __builtin_amdgcn_make_buffer_rsrc((void*)wsP, 0, (int)SZ_PREG, 0x00020000);
    const u32x4 rsPu = mk_rs(wsP, (unsigned)SZ_PREG);
    const __amdgpu_buffer_rsrc_t rsW = __builtin_amdgcn_make_buffer_rsrc((void*)ws, 0, (int)WS_END, 0x00020000);
    const unsigned lbase = (unsigned)(uintptr_t)lds;
    const u32x4 PZ = {0xFFFFFFFFu, 0xFFFFFFFFu, 0xFFFFFFFFu, 0xFFFFFFFFu};
    auto anchor_off = [&](int s) { return slot_off((unsigned)(s & (NR - 1)) * SLOTS + SL_P + ((s >> 2) & 3), pv) + ANCHOR + (s >> 4) * 768; };
    auto anchor = [&](int s, int tries) {
        unsigned v = 0;
        if (ANCH) {
            const unsigned o = anchor_off(s);
#pragma unroll 1
            for (int i = 0; i < tries; ++i) {
                v = (unsigned)__builtin_amdgcn_readfirstlane(__builtin_amdgcn_raw_buffer_load_b32(rsP, 0, o, AUX_SC1));
                if (v != ~0u) break; } }
        return v; };
    const Hello hi = {(unsigned long long*)(ws + OFF_MAIL + 32768) + blockIdx.x, __builtin_amdgcn_s_memrealtime()};
    if (tid < 16) __hip_atomic_store((unsigned long long*)(ws + OFF_MAIL) + blockIdx.x * 16 + tid, hi.num, __ATOMIC_RELAXED, __HIP_MEMORY_SCOPE_AGENT);
    GST(0);
    PRB(1);
    if (xg == 3) {
        const int w = jb, j = blockIdx.x >> 3, g5 = w - 5 * j;
        int tz = threadIdx.x; asm volatile("" : "+v"(tz));
        int k0 = g5, L0 = -1;
        if (w < 32) vox_ws(x, Wv, Wo, w, rsW, mk_rs(ws, (unsigned)WS_END), tmo + W_VO, hi, lds, tz);
        else if (w < 64) {
            const int z = (w >> 4) & 1, panel = w & 15;
            proj_ws<true>(x, (z == 0 ? Wq : Wk) + panel * 256, (bf16*)(ws + OFF_QB + (size_t)z * SZ_ACT) + panel * 8192, 0, 1, tmo + W_PROG + 32 * (w - 32), z == 1 ? attn::C2 : 1.f, hi, lds, tz);
            u32x4 st[16];
#pragma unroll
            for (int i = 0; i < 16; ++i) st[i] = ldw(awo + (w - 32) * DH * DM, tid * 16, i * 8192);
#pragma unroll
            for (int i = 0; i < 16; ++i) { const int id = tid + 512 * i; const int r = id >> 6, c = id & 63; *(u32x4*)(lds + ((r >> 5) * 4 + (c >> 4)) * 8192 + off_b(r & 31, c & 15)) = st[i]; }
            __syncthreads();
#pragma unroll
            for (int i = 0; i < 16; ++i) { const char* img = lds + ((i & 3) * 4 + (wid >> 1)) * 8192;
                __builtin_amdgcn_raw_buffer_store_b128(__builtin_bit_cast(u32x4, cat8(tr_rd(img + tr_addr16(lane, 4 * (wid & 1) + (i >> 2), 0)), tr_rd(img + tr_addr16(lane, 4 * (wid & 1) + (i >> 2), 1)))),
                                                       rsW, lane * 16 + (i & 3) * 1024, (unsigned)OFF_WT + ((4 * wid + (i >> 2)) * 128 + 4 * (w - 32)) * 1024, AUX_SC1); }
            VMWAIT(0);
            __syncthreads();
            if (tid == 0) __hip_atomic_fetch_add(tmo + W_CTRA, 0x10000u, __ATOMIC_RELAXED, __HIP_MEMORY_SCOPE_AGENT);
        } else {
            const int i = j == 12 ? 19 : j - 13, e = g5 + i - 18;
            const int pair = i < 18 ? 3 * g5 + (g5 == 4) + i / 6 : 12, z = i < 18 ? (i % 6) >> 1 : e >> 1, half = (i < 18 ? i : e) & 1, n0 = pair * 256;
            if (TCH && w > 64 && w < 129) {
                const int t = w - 65;
                const u32x4 rt = mk_rs(t < 32 ? Wo + t * DH * DM : (t < 48 ? Wq : Wk) + (t & 15) * 256 + 128, t < 32 ? DH * DM * 2 : DM * HD * 2 - 8192 + 256);
                const unsigned vt = t < 32 ? (unsigned)tid * 16u : (unsigned)((tid >> 4) * (HD * 2) + (tid & 15) * 16), it = t < 32 ? 8192u : HD * 64u;
#pragma unroll
                for (int q = 0; q < 16; ++q) dma16<false>(rt, vt, q * it, lbase + wid * 1024); }
            if (XT && w > 128 && w < 129 + 5 * XT) {
#pragma unroll
                for (int q = 0; q < 4; ++q) dma16<false>(mk_rs(x, SEQ * DM * 2), (unsigned)lane * 16u, (unsigned)(((w - 124) / 5) * 32768 + (wid * 4 + q) * 1024), lbase + wid * 1024); }
            if (WARM && w < 128) {
#pragma unroll
                for (int q = 0; q < 4; ++q) dma16<false>(mk_rs(ckv, SEQ * DM * 2), (unsigned)lane * 16u, (unsigned)((w - 64) * 32768 + (wid * 4 + q) * 1024), lbase + wid * 1024); }
            const unsigned ts = (unsigned)__builtin_amdgcn_s_memtime();
            if (w == MASTER) {
#pragma unroll
                for (int q = 0; q < 8; ++q) __builtin_amdgcn_raw_buffer_store_b128(u32x4{}, rsW, tid * 16 + q * 8192, (unsigned)OFF_TMO, AUX_SC1);
                VMWAIT(0);
                __syncthreads();
                if (wid < 4) {
                    unsigned long long was = 0;
#pragma unroll 1
                    for (unsigned i = 0; i < SPIN_CAP; ++i) {
                        const unsigned long long v = __hip_atomic_load((unsigned long long*)(ws + OFF_MAIL) + tid * 16, __ATOMIC_RELAXED, __HIP_MEMORY_SCOPE_AGENT);
                        if (i == 0 || v != was) __hip_atomic_store(hi.rep - blockIdx.x + tid, v, __ATOMIC_RELAXED, __HIP_MEMORY_SCOPE_AGENT);
                        was = v;
                        if (__builtin_amdgcn_ballot_w64(v + 5000u - hi.num > (1u << 24)) == 0) break; } }
            } else if (wid == 0) { until(ts, ADELAY / 2); hi.wait(hi.peek()); }
            if (ADELAY) until(ts, ADELAY);
            VMWAIT(0);
            proj_ws<false>(z == 0 ? x : ckv, (z == 0 ? awq : z == 1 ? awuk : awuv) + n0, (bf16*)(ws + OFF_QA + (size_t)z * SZ_ACT) + n0 * 32, half, 2, nullptr, z == 0 ? attn::C2 : 1.f, hi, lds, tz);
            if (tid == 0) { cnt_add(tmo + W_CTRP + 32 * pair); cnt_add(tmo + W_CTRA); }
            if (g5 < 3 && i == 18) { k0 = g5 < 2 ? 3 : 4; L0 = g5 == 0 ? 19 : 20; } else L0 = i;
            GST(1);
            if (wid == 0) wave_wait<8>(tmo + W_CTRP + 32 * ((k0 < 3 ? 6 * k0 + L0 % 6 : 7 * k0 - 3 + L0 % 7) >> 1), 6u, tmo, 0x30001u);
            __syncthreads();
        }
        GST(2);
        const int prev = attn::attn_phase(ws + OFF_QA, tmo + W_AQ, tmo + W_QCNT, tmo + W_LIST, g5, k0, L0, tmo, lds, (volatile LAS int*)(lds + 147456));
        GST(3);
        PRB(12);
        int tq = threadIdx.x; asm volatile("" : "+v"(tq));
        oproj_ws(g5, j & 3, tmo + W_OQ + (g5 * 4 + (j & 3)) * 32, (const bf16*)(ws + OFF_OA), (const bf16*)(ws + OFF_WT), (float*)(ws + OFF_PART), prev, tmo, lds, tq);
        GST(4);
        PRB(11);
        if (w < 2 * SPLIT) {
            const int wq = __builtin_amdgcn_readfirstlane(tq >> 6), lq = tq & 63, row = w * 16 + (lq & 15);
            if (wq == 0) wave_wait<20>(tmo + W_FA + 32 * (w >> 1), w < 2 * FSP ? 20u : 52u, tmo, 0xA0000u + w);
            if (wq == 1 && w < 2 * FSP) wave_wait<20>(tmo + W_PG, (w >> 1) + 5u, tmo, 0xA1000u + w);
            __syncthreads();
            const unsigned ti = (unsigned)(((w >> 1) * 32 + 4 * wq) * 2 + (w & 1));
            f32x4 qa[4][5]; u32x2 bb[4];
#pragma unroll
            for (int c = 0; c < 4; ++c) {
#pragma unroll
                for (int k = 0; k < 5; ++k) qa[c][k] = __builtin_bit_cast(f32x4, __builtin_amdgcn_raw_buffer_load_b128(rsW, (unsigned)OFF_PART + (ti + 2 * c) * 1024 + lq * 16, (unsigned)(k * SEQ * DM * 4), AUX_SC1));
                bb[c] = __builtin_amdgcn_raw_buffer_load_b64(rsW, (unsigned)OFF_BB + (ti + 2 * c) * 512 + lq * 8, 0, AUX_SC1); }
            const float g = __uint_as_float(__builtin_amdgcn_raw_buffer_load_b32(rsW, (unsigned)(OFF_G + row * 4), 0, AUX_SC1));
#pragma unroll
            for (int c = 0; c < 4; ++c) {
                const f32x4 bs = {__uint_as_float(bb[c][0] << 16), __uint_as_float(bb[c][0] & 0xFFFF0000u), __uint_as_float(bb[c][1] << 16), __uint_as_float(bb[c][1] & 0xFFFF0000u)};
                *(u32x2*)(out + (size_t)row * DM + (4 * wq + c) * 16 + 4 * (lq >> 4)) = mix4(((qa[c][0] + qa[c][1]) + (qa[c][2] + qa[c][3])) + qa[c][4], bs, g); }
        }
    } else if (xg == 0) {
        int tid = threadIdx.x; asm volatile("" : "+v"(tid));
        const int lane = tid & 63, l16 = lane & 15, g4 = lane >> 4, sidx = l16 < 8 ? 0x4444 : 0xEEEE;
        const int h = jb;
        const unsigned q2 = l16 >> 2, p2 = lane & 3;
        u32x8 w[16];
        {   u32x4 st[2][16];
            load_w(Wq + h * DH, st[0], tid); load_w(Wk + h * DH, st[1], tid);
            unsigned long long hv = 0;
#pragma unroll
            for (int rnd = 0; rnd < 2; ++rnd) {
                if (rnd) __syncthreads();
                stage_w(st[rnd], lds, tid);
                __syncthreads();
                if (wid == 0) { if (rnd == 0) hv = hi.peek(); else hi.wait(hv); }
                if ((wid >> 2) != rnd) tiles_w(w, lds, wid, lane);
            } }
        __syncthreads();
        constexpr unsigned LM = 0, LQ = 8192, LE = 24576, LK = 32768;
        const unsigned r8 = l16 & 7, pr8 = (r8 & 1) | ((r8 & 2) << 1) | ((r8 & 4) >> 1);
        const unsigned ma = lbase + LM + (4 * (l16 >> 3) + g4) * 128 + pr8 * 16;
        GST(1);
        PRB(9);
        *(u32x4*)(lds + LM + tid * 16) = *(const u32x4*)(mem0 + (tid & 7) * DM + (tid >> 3) * 8);
#pragma unroll
        for (int i = 0; i < 3; ++i) *(u32x4*)(lds + LQ + i * 8192 + tid * 16) = PZ;
        pv |= wave_wait<8>(tmo + W_CAL + (h & 15) * 32, (KPOIS ? NH : NPAGE) << 16, tmo, 0xB0000u) & 0x3FFFu;
        GST(2);
        const unsigned qw = lbase + LQ + (wid < 4 ? 2048 : 0) + (16 * (wid & 3) + l16) * 16 + (2 * (g4 & 1) + (g4 >> 1)) * 4;
        const unsigned fr = lbase + LQ + (4 * g4 + q2) * 16 + (p2 & 1) * 8;
        const unsigned kxa = lbase + LK + (l16 >> 3) * 1024 + g4 * 256 + (8 * ((l16 >> 1) & 1) + 2 * ((l16 >> 2) & 1) + (l16 & 1)) * 16;
        unsigned cap = SPIN_CAP >> 2, cnt;
#define H_OUT [cnt] "=&s"(cnt), [cap] "+s"(cap)
#define H_OPS W16(w), [ix] "v"(sidx), [ma] "v"(ma), [qw] "v"(qw + (s & 1) * 8192), [qo] "v"(qw + (~s & 1) * 8192), [tg] "v"(s + 1), [s] "s"(s)
#define H_CLOB "memory", "vcc", "scc", "s94", "s95", CL8(136, 137, 138, 139, 140, 141, 142, 143), CL8(144, 145, 146, 147, 148, 149, 150, 151), CL8(152, 153, 154, 155, 156, 157, 158, 159), \
                CL8(160, 161, 162, 163, 164, 165, 166, 167), CL8(168, 169, 170, 171, 172, 173, 174, 175)
        if (wid >= 4) __builtin_amdgcn_s_setprio(1);
        if (wid == 7) {
            unsigned fb = 0, ao = anchor_off(0), due = 0, dl = 0;
            int lastf = 0;
            LDS_BARRIER();
#pragma unroll 1
            for (int s = 0; s < NSEG; ++s) {
                ST(0);
                unsigned keep, f2, tm;
                asm volatile(ASM_HP : H_OUT, [keep] "=&s"(keep), [f2] "=&s"(f2), [tm] "=&s"(tm)
                             : H_OPS, [o] "v"((unsigned)lane * 16u), [rs] "s"(rsPu), [fb] "s"(fb), [lm] "s"(lbase + LM), [due] "s"(due), [dp] "s"(pin_s(due + s + dl)),
                               [k0] "s"(pin_s((APEN << 6) - s - dl)), [k1] "s"(pin_s(((APEN - AFM) << 6) - s - dl)), [l1] "s"((APEN + 16u) << 6),
                               [av] "v"(h == 0 ? min(lane, 11) * 64u : 0x7FFFFFFFu), [ao] "s"(ao) : H_CLOB);
                DUR(1, tm >> 6);
                if (ADS && cnt) {
                    if (s >= AS0 && (unsigned)(s - lastf - 2) <= ADW - 2u) dl = min(dl + ((dl == 0 && s < 16 ? ADS0 : ADS) << 6), ADM << 6);
                    lastf = s; }
                ST(3);
                SP(12, cnt);
                if (__builtin_expect(cap == 0, 0)) __hip_atomic_store(tmo, 0x10000u + h, __ATOMIC_RELAXED, __HIP_MEMORY_SCOPE_AGENT);
                const unsigned t0 = (unsigned)__builtin_amdgcn_s_memtime();
                if (s < JIT) LDS_BARRIER();
                fb = pin_s(slot_off((unsigned)((s + 1) & (NR - 1)) * SLOTS + SL_MEM, pv));
                ao = pin_s(anchor_off(min(s + 1, NSEG - 1)));
                until(t0, ADT);
                const unsigned aw = anchor(s, 4);
                due = pin_s((aw & ~63u) + (AM << 6));
                until_a(aw, s, AM, t0, MDT);
            }
        } else if (wid == 4) {
            unsigned kb = 0, cn = 1u << 16;
            LDS_BARRIER();
            const unsigned pb = ((h & 7) >> 1) * 1280;
            const unsigned pxo = pb + (h & 1) * 512 + g4 * 128 + r8 * 16 + (l16 >> 3) * 8, pmo = lane & 40 ? 0x7FFFFFFFu : pb + 1024 + (g4 & 1) * 128 + r8 * 16 + (h & 1) * 8;
#pragma unroll 1
            for (int s = 0; s + 1 < NSEG; ++s) {
                const unsigned po = slot_off((unsigned)(s & (NR - 1)) * SLOTS + SL_P + (h >> 3), pv), pq = slot_off((unsigned)((s - 1) & (NR - 1)) * SLOTS + SL_P + (h >> 3), pv);
                ST(0);
                VMWAIT(0);
                asm volatile(ASM_H4 : H_OUT, [cn] "+s"(cn)
                             : H_OPS, [kx] "v"(kxa + kb), [ta] "v"(lbase + LQ + 4096 + (lane & 3) * 256 + (lane & 4) * 512 + (s & 1) * 8192), [fr] "v"(fr + (s & 1) * 8192),
                               [er] "v"(lbase + LE + (l16 >> 3) * 512 + g4 * 128 + r8 * 16), [sr] "v"(lbase + LE + 1024 + (s & 1) * 256 + r8 * 32 + (g4 & 1) * 16),
                               [px] "v"(pxo), [pm] "v"(pmo), [rs] "s"(rsPu), [po] "s"(po), [pq] "s"(pq), [lo] "s"(0x21800000u), [sp] "s"(0x3c000000u)
                             : H_CLOB, CL8(176, 177, 178, 179, 180, 181, 182, 183), CL8(184, 185, 186, 187, 188, 189, 190, 191), CL192_223, "v224", "v225", "v226", "v227");
                ST(5);
                kb = kb == 32768 ? 0 : kb + 16384;
                if (s < JIT) LDS_BARRIER();
            }
            { const int s = NSEG - 1; asm volatile(ASM_HQ : H_OUT : H_OPS : H_CLOB); }
        } else if (wid > 4) {
            const int rt = wid - 5;
            const unsigned kra = LK + (l16 >> 3) * 1024 + g4 * 256 + r8 * 16, qa = LK + 8192 + rt * 1024 + lane * 16;
            const unsigned sw = lbase + LE + 1024 + r8 * 32 + (g4 & 1) * 16 + rt * 8 + (g4 >> 1) * 4;
            const int i = rt * 16 + l16 - (16 * (g4 >> 1) + 4 * (g4 & 1));
            const f32x4 NEG4 = {-__builtin_inff(), -__builtin_inff(), -__builtin_inff(), -__builtin_inff()};
            unsigned kb = 0;
            f32x4 X[2];
            auto own = [&]() {
                X[0] = f32x4{}; X[1] = f32x4{};
#pragma unroll
                for (int ks = 0; ks < 4; ++ks) { const u32x4 qf = *(const u32x4*)(lds + qa + kb + ks * 2048);
                    X[0] = mfma32(*(const u32x4*)(lds + kra + kb + ks * 2048), qf, X[0]); X[1] = mfma32(*(const u32x4*)(lds + kra + kb + ks * 2048 + 128), qf, X[1]); }
#pragma unroll
                for (int r = 0; r < 4; ++r) { if (r > i) X[0][r] = NEG4[0]; if (8 + r > i) X[1][r] = NEG4[0]; }
                asm volatile("" : "+v"(X[0]), "+v"(X[1])); };
            LDS_BARRIER();
            own();
#pragma unroll 1
            for (int s = 0; s < NSEG; ++s) {
                ST(0);
                asm volatile(ASM_H5 : H_OUT
                             : H_OPS, [kx] "v"(kxa + rt * 64 + kb), [fr] "v"(fr + (s & 1) * 8192), [em] "s"(0x00FF00FF00FF00FFull),
                               [ew] "v"(lbase + LE + rt * 512 + g4 * 128 + r8 * 16), [sw] "v"(sw + (s & 1) * 256), [so] "v"(sw + (~s & 1) * 256)
                             : H_CLOB, CL8(176, 177, 178, 179, 180, 181, 182, 183), CL8(184, 185, 186, 187, 188, 189, 190, 191), CL8(208, 209, 210, 211, 212, 213, 214, 215));
                u32x4 qf[4];
#pragma unroll
                for (int ks = 0; ks < 4; ++ks) qf[ks] = *(const u32x4*)(lds + qa + kb + ks * 2048);
                for (int n = 0; n < (1 << 14) && __builtin_amdgcn_ballot_w64(*(volatile LAS int*)(size_t)(lbase + LQ + 6144 + (lane & 3) * 256 + (s & 1) * 8192) != s + 1) != 0; ++n) {}
                asm volatile("" ::: "memory");
                f32x4 S[3] = {f32x4{}, X[0], X[1]};
#pragma unroll
                for (int ks = 0; ks < 4; ++ks) {
                    const char* p = lds + (fr - lbase) + 2048 + (s & 1) * 8192 + ks * 512;
                    const u32x2 a = tr_rdu(p), b = tr_rdu(p + 256);
                    S[0] = mfma32(u32x4{a[0], a[1], b[0], b[1]}, qf[ks], S[0]); }
                kb = kb == 32768 ? 0 : kb + 16384;
                if (g4 < 2) S[0] = NEG4;
                {
                    float mx = fmaxf(fmaxf(S[0][0], S[0][1]), fmaxf(S[0][2], S[0][3]));
#pragma unroll
                    for (int t = 1; t < 3; ++t) mx = fmaxf(mx, fmaxf(fmaxf(S[t][0], S[t][1]), fmaxf(S[t][2], S[t][3])));
                    mx = xg_max(mx);
#pragma unroll
                    for (int t = 0; t < 3; ++t)
#pragma unroll
                        for (int r = 0; r < 4; ++r) S[t][r] = __builtin_amdgcn_exp2f(S[t][r] - mx);
                    const f32x4 s4 = (S[0] + S[1]) + S[2];
                    const float inv = __builtin_amdgcn_rcpf(xg_sum((s4[0] + s4[1]) + (s4[2] + s4[3])));
#pragma unroll
                    for (int t = 0; t < 3; ++t) S[t] *= inv; }
                const unsigned po = (unsigned)(OFF_PR + (size_t)(s * 4 + (h >> 3)) * 16384);
                if (rt || lane < 32)
                    __builtin_amdgcn_raw_buffer_store_b128(u32x4{cvtpk(S[1][0], S[1][1]), cvtpk(S[1][2], S[1][3]), cvtpk(S[2][0], S[2][1]), cvtpk(S[2][2], S[2][3])}, rsW,
                                                           (rt ? (h & 7) * 1024 : 8192 + (h & 7) * 512) + lane * 16, po, AUX_SC1);
                const auto sv = __builtin_amdgcn_permlane16_swap(cvtpk(S[0][0], S[0][1]), cvtpk(S[0][2], S[0][3]), false, false);
                if (g4 >= 2) __builtin_amdgcn_raw_buffer_store_b64(u32x2{sv[0], sv[1]}, rsW, 12288 + rt * 2048 + (h & 7) * 256 + l16 * 16 + (g4 & 1) * 8, po, AUX_SC1);
                ST(5);
                if (s < JIT) LDS_BARRIER();
                own();
                VMWAIT(0);
                if (lane == 0 && s >= NSEG - 2) cnt_add(tmo + W_RDY + s * 32);
            }
        } else {
            const u32x4 rsK = mk_rs(ws + OFF_QB, (unsigned)(2 * SZ_ACT));
            unsigned kn = 0, kb = 0, tf = 0;
            unsigned* const pk = tmo + W_PROG + 32 * (h >> 1) + (lane & 1) * 512;
            auto look = [&]() { const unsigned v = __hip_atomic_load(pk, __ATOMIC_RELAXED, __HIP_MEMORY_SCOPE_AGENT);
                kn = min((unsigned)__builtin_amdgcn_readlane(v, 0), (unsigned)__builtin_amdgcn_readlane(v, 1)); };
            auto fetch = [&](int sg) {
#pragma unroll 1
                for (unsigned spins = 0; kn <= (unsigned)sg; ++spins) {
                    if (spins) __builtin_amdgcn_s_sleep(1);
                    look();
                    if (bail(spins, SPIN_CAP << 1, tmo, 0x51000u + (unsigned)(sg << 8) + h)) break; }
#pragma unroll
                for (int i = 0; i < 16; ++i) dma16<true>(rsK, (unsigned)lane * 16u, (unsigned)((i < 8 ? SZ_ACT : 0) + (sg * 32 + h) * 8192 + (i & 7) * 1024), lbase + LK + kb + i * 1024);
                kb = kb == 32768 ? 0 : kb + 16384; };
            if (wid == 3) { fetch(0); if (LOOK) look(); VMWAIT(0); }
            LDS_BARRIER();
#pragma unroll 1
            for (int s = 0; s < NSEG; ++s) {
                VMWAIT(0);
                if (wid == 3) DUR(0, DURT() - tf);
                ST(0);
                asm volatile(ASM_HK : H_OUT : H_OPS, [c2] "s"(attn::C2) : H_CLOB);
                if (wid == 3) {
                    if (FDT) until((unsigned)__builtin_amdgcn_s_memtime(), FDT);
                    if (h == 1 && lane == 0) __hip_atomic_store(tmo + W_PG, (unsigned)s, __ATOMIC_RELAXED, __HIP_MEMORY_SCOPE_AGENT);
                    if (s < JIT) { fetch(s + 1); if (LOOK) look(); VMWAIT(0); } }
                if (s < JIT) LDS_BARRIER();
                tf = DURT();
                if (wid == 3 && s + 1 >= JIT && s + 2 < NSEG) { fetch(s + 2); if (kn <= (unsigned)s + 3u) look(); }
            }
        }
        DUROUT(0);
        GST(3);
        PRB(5);
    } else if (xg == 1) {
        int tid = threadIdx.x; asm volatile("" : "+v"(tid));
        const int lane = tid & 63, l16 = lane & 15, g4 = lane >> 4, sidx = l16 < 8 ? 0x4444 : 0xEEEE;
        const int h = jb;
        if (h < NPAGE) {
            if (wid == 0) { const unsigned b = calibrate(rsP, h, pv, (unsigned*)(ws + WS_END + 79000 * 8));
                hi.wait(hi.peek());
                if (lane < 16) __hip_atomic_fetch_add(tmo + W_CAL + lane * 32, 0x10000u + (b << h), __ATOMIC_RELAXED, __HIP_MEMORY_SCOPE_AGENT); }
            __syncthreads();
        } else if (KPOIS) {
#pragma unroll 1
            for (unsigned o = (unsigned)(h - NPAGE) * 8192u; o < (unsigned)SZ_RING; o += (NH - NPAGE) * 8192u) __builtin_amdgcn_raw_buffer_store_b128(PZ, rsP, tid * 16, o, AUX_SC1);
            VMWAIT(0);
            if (wid == 0) hi.wait(hi.peek());
            __syncthreads();
            if (tid < 16) __hip_atomic_fetch_add(tmo + W_CAL + lane * 32, 0x10000u, __ATOMIC_RELAXED, __HIP_MEMORY_SCOPE_AGENT);
        }
        GST(1);
        {
            const int row0 = (h * 8 + wid) * 8;
            const bf16x8 gv = *(const bf16x8*)(gw + lane * 8);
            float gl = 0.f;
#pragma unroll
            for (int r = 0; r < 8; ++r) {
                const bf16x8 xv = *(const bf16x8*)(x + (size_t)(row0 + r) * DM + lane * 8);
                float sg = 0.f;
#pragma unroll
                for (int j = 0; j < 8; ++j) sg += rbf(bf2f((unsigned short)xv[j]) * bf2f((unsigned short)gv[j]));
#pragma unroll
                for (int o = 32; o >= 1; o >>= 1) sg += __shfl_xor(sg, o);
                if (lane == r) gl = sg;
            }
            gl = rbf(gl);
            gl = rbf(1.f / (1.f + __expf(-gl)));
            if (lane < 8) __builtin_amdgcn_raw_buffer_store_b32(__float_as_uint(gl), rsW, (unsigned)(OFF_G + (row0 + lane) * 4), 0, AUX_SC1);
        }
        constexpr unsigned LF = 0, LP = 1024, LV = LP + 16384, LA = LV + 16384, LB = LA + 65536;
        static_assert(LAG == 2, "slots");
        pv |= wave_wait<8>(tmo + W_CAL + (h & 15) * 32, (KPOIS ? NH : NPAGE) << 16, tmo, 0xB0002u) & 0x3FFFu;
        volatile LAS unsigned* const lf = (volatile LAS unsigned*)(size_t)(lbase + LF);
        if (tid < 2) lf[tid] = 0u;
        if (wid == 1) wave_wait<4>(tmo + W_VO, 32u, tmo, 0x50000u + h);
        VMWAIT(0);
        __syncthreads();
        GST(2);
        const unsigned rot = ROT ? (MIX == 2 ? h >> 1 : MIX ? h / 3 : h) : 0;
        unsigned lb3 = 0;
        if (wid < 4) {
            if (wid == 0) {
                const unsigned mo = lane & 6 ? 0x7FFFFFFFu : h * 256 + (l16 >> 3) * 128 + (2 * g4 + (l16 & 1)) * 16;
                unsigned cap = SPIN_CAP >> 2, pa = (unsigned)__builtin_amdgcn_s_memrealtime() << 6;
#pragma unroll 1
                for (int s = 0; s < NSEG; ++s) {
                    const unsigned rg = (unsigned)(s & (NR - 1)) * SLOTS, vb = slot_off(rg + SL_VM + h, pv);
                    unsigned pb[4], vg[4], lx[4], vs[4];
#pragma unroll
                    for (int g = 0; g < 4; ++g) { const unsigned k = (g + rot) & 3u;
                        pb[g] = pin_s(slot_off(rg + SL_P + k, pv)); vg[g] = pin_s(vb + k * 2048);
                        lx[g] = pin_s(lbase + (k < 2 ? LA + (s & 3) * 16384 : LB + lb3 - 16384) + k * 8192); vs[g] = pin_s(lbase + LV + (s & 1) * 8192 + k * 2048); }
                    ST(0);
                    unsigned aw, tn, cnt; unsigned long long tp = 0;
                    asm volatile(ASM_CSTEP
                                 : [aw] "=&s"(aw), [tn] "=&s"(tn), [cnt] "=&s"(cnt), [cap] "+s"(cap)
                                 : [l16] "v"((unsigned)lane * 16u), [pm] "v"(g4 * 1280 + l16 * 16), [ix] "v"(sidx), [sel] "v"(lane & 1 ? 0x03020706u : 0x05040100u), [mo] "v"(mo),
                                   [rs] "s"(rsPu), [pb0] "s"(pb[0]), [pb1] "s"(pb[1]), [pb2] "s"(pb[2]), [pb3] "s"(pb[3]), [vb0] "s"(vg[0]), [vb1] "s"(vg[1]), [vb2] "s"(vg[2]), [vb3] "s"(vg[3]),
                                   [lx0] "s"(lx[0]), [lx1] "s"(lx[1]), [lx2] "s"(lx[2]), [lx3] "s"(lx[3]), [vs0] "s"(vs[0]), [vs1] "s"(vs[1]), [vs2] "s"(vs[2]), [vs3] "s"(vs[3]),
                                   [fl] "s"(lbase + LF), [tg] "s"(s + 1), [pa] "s"(pin_s(pa)), [ab] "s"(pin_s(anchor_off(s))), [ax] "s"(AX << 6), [ap] "s"(AP << 6),
                                   [on] "s"(pin_s(NSEG - 1 - s)), [mb] "s"(pin_s(slot_off((unsigned)((s + 1) & (NR - 1)) * SLOTS + SL_MEM, pv)))
                                 : "memory", "vcc", "scc", "s93", "s94", "s95", CL8(8, 9, 10, 11, 12, 13, 14, 15), CL16_63, CL64_127, CL128_159, CL160_191, CL192_223, CL224_251);
                    pa = pin_s((aw & ~63u) + ((AM + AA) << 6));
                    if (__builtin_expect(cap == 0, 0)) __hip_atomic_store(tmo, 0x20000u + h, __ATOMIC_RELAXED, __HIP_MEMORY_SCOPE_AGENT);
                    if (s == 0) PRB(4);
                    if (s == 10) PRB(10);
                    lb3 = lb3 == 32768u ? 0u : lb3 + 16384u;
                }
            } else if (wid == 1) {
                const u32x4 rsX = mk_rs(ws + OFF_VOX, (unsigned)(4 * SZ_ACT));
                auto half = [&](int sg, unsigned hf, unsigned la) { dma16k(rsX, (unsigned)lane * 16u, ((unsigned)sg << 20) + (h * 32 + hf * 16) * 1024, lbase + la); };
                bool early = false;
#pragma unroll 1
                for (int s = 0; s < NSEG; ++s) {
                    const unsigned tf = DURT();
                    if (XDT) until((unsigned)__builtin_amdgcn_s_memtime(), XDT);
                    if (!early) half(s, 0, LA + (s & 3) * 16384);
                    half(s, 1, LB + lb3);
                    unsigned* const fw = tmo + W_VO + 32 * min(s + 1, NSEG - 1);
                    early = EARLY && __builtin_amdgcn_readfirstlane(__hip_atomic_load(fw, __ATOMIC_RELAXED, __HIP_MEMORY_SCOPE_AGENT)) >= 32u && s + 1 < NSEG;
                    VMWAIT(0);
                    lf[0] = s + 1;
                    DUR(3, DURT() - tf);
                    if (early) {
                        for (int n = 0; n < (1 << 20) && __builtin_amdgcn_readfirstlane(lf[1]) != (unsigned)s + 1u; ++n) __builtin_amdgcn_s_sleep(1);
                        half(s + 1, 0, LA + ((s + 1) & 3) * 16384);
                    } else wave_wait<4>(fw, 32u, tmo, 0x50000u + (unsigned)(s << 8) + h);
                    LDS_BARRIER();
                    lb3 = lb3 == 32768u ? 0u : lb3 + 16384u;
                }
            }
            static_assert(SPLIT <= NSEG - 2 && FSP <= SPLIT && FSP + 4 < NSEG, "tail");
            int lane;
            asm volatile("v_mbcnt_lo_u32_b32 %0, -1, 0\n\tv_mbcnt_hi_u32_b32 %0, -1, %0" : "=v"(lane));
            f32x4 qs = f32x4{}; float g = 0.f;
            auto branch_a = [&](int tau) {
                wave_wait<4>(tmo + W_FA + 32 * tau, 20u, tmo, 0x55000u + h);
                f32x4 qa[5];
#pragma unroll
                for (int k = 0; k < 5; ++k) qa[k] = __builtin_bit_cast(f32x4, __builtin_amdgcn_raw_buffer_load_b128(rsW, (unsigned)OFF_PART + ((tau * 32 + h) * 2 + (wid & 1)) * 1024 + lane * 16, (unsigned)(k * SEQ * DM * 4), AUX_SC1));
                g = __uint_as_float(__builtin_amdgcn_raw_buffer_load_b32(rsW, (unsigned)(OFF_G + (tau * SEGL + (wid & 1) * 16 + (lane & 15)) * 4), 0, AUX_SC1));
                qs = ((qa[0] + qa[1]) + (qa[2] + qa[3])) + qa[4]; };
            auto finish = [&](int tau) {
                const char* p = lds + LP + (tau & 1) * 8192 + (wid & 1) * 1024 + lane * 16;
                const f32x4 bs = (*(const f32x4*)p + *(const f32x4*)(p + 2048)) + (*(const f32x4*)(p + 4096) + *(const f32x4*)(p + 6144));
                if (tau < SPLIT) __builtin_amdgcn_raw_buffer_store_b64(u32x2{cvtpk(bs[0], bs[1]), cvtpk(bs[2], bs[3])}, rsW, (unsigned)OFF_BB + ((tau * 32 + h) * 2 + (wid & 1)) * 512 + lane * 8, 0, AUX_SC1);
                else *(u32x2*)(out + (size_t)(tau * SEGL + (wid & 1) * 16 + (lane & 15)) * DM + h * 16 + 4 * (lane >> 4)) = mix4(qs, f32x4{rbf(bs[0]), rbf(bs[1]), rbf(bs[2]), rbf(bs[3])}, g); };
            if (wid < 2) {
                branch_a(NSEG - 1);
                LDS_BARRIER(); LDS_BARRIER();
                finish(NSEG - 1);
            } else {
#pragma unroll 1
                for (int s = 0; s < NSEG + LAG; ++s) {
                    const int tau = s - LAG - 1;
                    const unsigned tf = DURT();
                    if (HDT) until((unsigned)__builtin_amdgcn_s_memtime(), HDT);
                    if (s > 0 && s + NR <= NSEG) {
                        const unsigned rg = (unsigned)((s - 1) & (NR - 1)) * SLOTS;
                        const unsigned o = slot_off(rg + SL_VM + h, pv) + (wid & 1) * 4096;
#pragma unroll
                        for (int i = 0; i < 4; ++i) __builtin_amdgcn_raw_buffer_store_b128(PZ, rsP, lane * 16 + i * 1024, o, AUX_SC1);
                        if (wid == 3 && s > 1 && lane < 16) __builtin_amdgcn_raw_buffer_store_b128(PZ, rsP, h * 256 + lane * 16, slot_off(rg + SL_MEM, pv), AUX_SC1);
                    }
                    if (tau >= 0) finish(tau);
                    if (wid == 2 && lane == 0 && tau > FSP && tau <= SPLIT) cnt_add(tmo + W_FA + 32 * (tau - 1));
                    if (tau + 1 >= SPLIT && tau + 2 < NSEG) branch_a(tau + 1);
                    if (tau < SPLIT) VMWAIT(0);
                    if (wid == 3 && s < NSEG) DUR(0, DURT() - tf);
                    LDS_BARRIER();
                }
            }
        } else {
            const unsigned po = (unsigned)(OFF_PR + (wid - 4) * 16384 + lane * 16);
            auto fr = [&](int i) { return i < 8 ? (i + rot) & 7u : i < 12 ? (i + rot) & 3u : (i + rot) & 1u; };
            LDS_BARRIER(); LDS_BARRIER();
#pragma unroll 1
            for (int sg = 0; sg < NSEG; ++sg) {
                const int s = sg + LAG;
                const unsigned tf = DURT();
                if (s >= NSEG) wave_wait<2>(tmo + W_RDY + 32 * sg, 64u, tmo, 0x53000u + (unsigned)(s << 8) + h);
                else if (PDL) until((unsigned)__builtin_amdgcn_s_memtime(), PDL);
                u32x4 pf[16];
#pragma unroll
                for (int i = 0; i < 16; ++i) pf[i] = __builtin_amdgcn_raw_buffer_load_b128(rsW, po, (unsigned)(sg * 65536) + ((i < 8 ? 0 : i < 12 ? 8 : i & 14) + fr(i)) * 1024, AUX_SC1);
                __builtin_amdgcn_sched_barrier(0);
                const char* gp = lds + (wid < 6 ? LA + (sg & 3) * 16384 : LB + lb3) + (wid & 1) * 8192 + lane * 16, * gv = lds + LV + (sg & 1) * 8192 + (wid - 4) * 2048 + lane * 16;
                f32x4 a0 = f32x4{}, a1 = f32x4{};
#pragma unroll
                for (int i = 0; i < 8; ++i) a1 = mfma32(*(const u32x4*)(gp + fr(i) * 1024), pf[i], a1);
#pragma unroll
                for (int i = 8; i < 12; ++i) a0 = mfma32(*(const u32x4*)(gp + fr(i) * 2048 + (g4 >> 1) * 512), pf[i], a0);
#pragma unroll
                for (int i = 12; i < 14; ++i) { const u32x4 vf = *(const u32x4*)(gv + fr(i) * 1024); a0 = mfma32(vf, pf[i], a0); a1 = mfma32(vf, pf[i + 2], a1); }
                char* p = lds + LP + (sg & 1) * 8192 + (wid - 4) * 2048 + lane * 16;
                *(f32x4*)p = a0; *(f32x4*)(p + 1024) = a1;
                lb3 = lb3 == 32768u ? 0u : lb3 + 16384u;
                if (wid == 7 && s < NSEG) DUR(1, DURT() - tf);
                ST(5);
                LDS_BARRIER();
            }
        }
        DUROUT(1);
        GST(3);
        PRB(7);
    } else {
        int tid = threadIdx.x; asm volatile("" : "+v"(tid));
        const int lane = tid & 63, l16 = lane & 15, g4 = lane >> 4, sidx = l16 < 8 ? 0x4444 : 0xEEEE;
        const int h = jb;
        const unsigned q2 = l16 >> 2, p2 = lane & 3;
        constexpr unsigned LM = 0, LI = 8192;
        u32x8 w[16];
        { u32x4 st[2][16];
          load_w(Wv + h * DH, st[0], tid);
#pragma unroll
          for (int i = 0; i < 16; ++i) st[1][i] = ldw(Wo + h * DH * DM, tid * 16, i * 8192);
          stage_w(st[0], lds, tid);
          __syncthreads();
          unsigned long long hv = 0;
          if (wid == 0) hv = hi.peek();
          if (wid >= 4) tiles_w(w, lds, wid, lane);
          __syncthreads();
#pragma unroll
          for (int i = 0; i < 16; ++i) { const int id = tid + 512 * i; const int r = id >> 6, c = id & 63; *(u32x4*)(lds + ((r >> 5) * 4 + (c >> 4)) * 8192 + off_b(r & 31, c & 15)) = st[1][i]; }
          if (wid == 0) hi.wait(hv); }
        __syncthreads();
        if (wid < 4) {
#pragma unroll
            for (int pr = 0; pr < 4; ++pr)
#pragma unroll
                for (int ks = 0; ks < 4; ++ks) { const char* img = lds + (ks * 4 + wid) * 8192 + 8 * (p2 & 1);
                    const unsigned r0 = 4 * g4 + q2, c0 = 4 * pr + (p2 >> 1);
                    w[4 * pr + ks] = ilv(tr_rdu(img + off_b(r0, c0)), tr_rdu(img + off_b(r0, c0 + 2)), tr_rdu(img + off_b(r0 + 16, c0)), tr_rdu(img + off_b(r0 + 16, c0 + 2))); } }
        __syncthreads();
        const unsigned ma = lbase + (4 * (l16 >> 3) + g4) * 128 + (l16 & 7) * 16;
        *(u32x4*)(lds + LM + tid * 16) = *(const u32x4*)(mem0 + (tid & 7) * DM + (tid >> 3) * 8);
        GST(1);
        pv |= wave_wait<8>(tmo + W_CAL + (h & 15) * 32, (KPOIS ? NH : NPAGE) << 16, tmo, 0xB0001u) & 0x3FFFu;
        GST(2);
        unsigned fb = 0, cap = SPIN_CAP >> 2;
        const unsigned vw = lbase + LI + (16 * (wid & 3) + l16) * 16 + (2 * (g4 & 1) + (g4 >> 1)) * 4, fr = lbase + LI + (4 * g4 + q2) * 16 + (p2 & 1) * 8;
        if (wid >= 4) __builtin_amdgcn_s_setprio(1);
        const u32x4 rsWu = mk_rs(ws, (unsigned)WS_END);
        auto prefetch = [&](int s) {
                const unsigned pc = (unsigned)(h / 3 * 8 + 2 * wid), la = lbase + 16384 + wid * 1024;
                if (s + 1 >= LAG && pc < 64u) {
                    dma16<true>(rsWu, (unsigned)lane * 16u, (unsigned)OFF_PR + (unsigned)(s + 1 - LAG) * 65536u + pc * 1024u, la);
                    dma16<true>(rsWu, (unsigned)lane * 16u, (unsigned)OFF_PR + (unsigned)(s + 1 - LAG) * 65536u + pc * 1024u + 1024u, la); }
            };
#pragma unroll 1
        for (int s = 0; s < NSEG; ++s) {
            const unsigned rg = (unsigned)(s & (NR - 1)) * SLOTS;
            ST(0);
            unsigned cnt;
#define V_OPS W16(w), [ix] "v"(sidx), [ma] "v"(ma), [s] "s"(s)
            if (wid < 4) {
                const unsigned vb = 8 * wid + 2 * (g4 & 1) + (g4 >> 1), vo = h * 256 + l16 * 16;
                asm volatile(ASM_VO : H_OUT : V_OPS, [fr] "v"(fr), [rs] "s"(rsPu), [so0] "v"(slot_off(rg + SL_VM + vb, pv) + vo), [so1] "v"(slot_off(rg + SL_VM + vb + 4, pv) + vo)
                             : H_CLOB, CL8(176, 177, 178, 179, 180, 181, 182, 183), CL8(184, 185, 186, 187, 188, 189, 190, 191), CL192_223);
                ST(4);
                if (PF & 1) prefetch(s);
            } else if (wid < 7) asm volatile(ASM_VV : H_OUT : V_OPS, [qw] "v"(vw) : H_CLOB);
            else {
                unsigned keep, f2;
                asm volatile(ASM_VP : H_OUT, [keep] "=&s"(keep), [f2] "=&s"(f2)
                             : V_OPS, [qw] "v"(vw), [o] "v"((unsigned)lane * 16u), [rs] "s"(rsPu), [fb] "s"(fb), [lm] "s"(lbase + LM) : H_CLOB);
                SP(12, cnt);
                if (__builtin_expect(cap == 0, 0)) __hip_atomic_store(tmo, 0x90000u + h, __ATOMIC_RELAXED, __HIP_MEMORY_SCOPE_AGENT);
                const unsigned t0 = (unsigned)__builtin_amdgcn_s_memtime();
                fb = pin_s(slot_off((unsigned)((s + 1) & (NR - 1)) * SLOTS + SL_MEM, pv));
                until(t0, VAD);
                until_a(anchor(s, 4), s, AV, t0, VPD);
            }
        }
        VMWAIT(0);
        GST(3);
    }
    GST(5);
}

extern "C" void kernel_launch(void* const* d_in, const int* in_sizes, int n_in,
                              void* d_out, int out_size, void* d_ws, size_t ws_size,
                              hipStream_t stream) {
    (void)in_sizes; (void)n_in; (void)out_size; (void)ws_size;
    const bf16* x = (const bf16*)d_in[0];
    const bf16* ckv = (const bf16*)d_in[1];
    const bf16* a_wq = (const bf16*)d_in[2];
    const bf16* a_wuk = (const bf16*)d_in[3];
    const bf16* a_wuv = (const bf16*)d_in[4];
    const bf16* a_wo = (const bf16*)d_in[5];
    const bf16* mem0 = (const bf16*)d_in[7];
    const bf16* bWq = (const bf16*)d_in[8];
    const bf16* bWk = (const bf16*)d_in[9];
    const bf16* bWv = (const bf16*)d_in[10];
    const bf16* bWo = (const bf16*)d_in[11];
    const bf16* gw = (const bf16*)d_in[13];
    char* ws = (char*)d_ws;
    bf16* out = (bf16*)d_out;

    static bool attr = false;
    if (!attr) {
        attr = true;
        (void)hipFuncSetAttribute((const void*)k_chain, hipFuncAttributeMaxDynamicSharedMemorySize, CHAIN_LDS);
    }
    if (!KPOIS) (void)hipMemsetAsync(ws, 0xFF, SZ_POISON, stream);
    k_chain<<<256, 512, CHAIN_LDS, stream>>>(mem0, bWq, bWk, bWv, bWo, x, ws, ckv, a_wq, a_wuk, a_wuv, a_wo, gw, out);
}
```

```cpp
#include <hip/hip_runtime.h>
#include <hip/hip_bf16.h>
#include <cstdint>
#include <type_traits>
#include <utility>

using bf16 = __hip_bfloat16;
typedef short bf16x8 __attribute__((ext_vector_type(8)));
typedef short s16x4 __attribute__((ext_vector_type(4)));
typedef float f32x16 __attribute__((ext_vector_type(16)));
typedef float f32x4 __attribute__((ext_vector_type(4)));
typedef float f32x2 __attribute__((ext_vector_type(2)));
typedef unsigned u32x16 __attribute__((ext_vector_type(16)));
typedef unsigned u32x8 __attribute__((ext_vector_type(8)));
typedef unsigned u32x4 __attribute__((ext_vector_type(4)));
typedef unsigned u32x2 __attribute__((ext_vector_type(2)));
typedef __bf16 bfv8 __attribute__((ext_vector_type(8)));
typedef __bf16 bfv16 __attribute__((ext_vector_type(16)));
#define LAS __attribute__((address_space(3)))

constexpr int SEQ = 2048, DM = 512, NH = 32, DH = 128, HD = NH * DH;
constexpr int NSEG = 64, SEGL = 32;
constexpr float ATT_SCALE = 0.08838834764831845f;

#define ANCH 1
#define MDT 3000
#define VDT 3300
#define PDT 4200
#define XDT 0
#define PDL 300
#define VXP 0
#define WTD 16000
#define ADD 6
#define WAUX 2
#define WNT " nt"
#define EARLY 1
#define HDT 0
#define KVSC false
#define ADELAY 5000
#define MIX 1
#define ROT 1
#define LAG 2
#define PKM 0
#define PREV 0
#define CALA 0
#define CALN 8
#define JIT 3
#define RDT 0
#define PRIO 0
#define AM 167
#define AV 167
#define AX 104
#define AP 106
#define BDT 1500
#define APEN 40
#define SLACK 0
#define WARM 1
#define KPOIS 1
#define AA 62u
#define ADT 300
#define PCONT 1
#define GMAP 0x76543210u
#define WFLIP 1
#define PBAR 8
#define PDS 2
#define AH 160u
#define AFM 20u
#define PF1 1
#define PUBW 4
#define SANC 0
#define EF 1
#define PF 0
#define TCH 0
#define XT 6
#define ADS 2u
#define ADW 4u
#define ADM 12u
#define LOOK 1
#define ADS0 3u
#define FDT 200
#define VAD 900
#define AS0 6
#define VPD 2800
constexpr size_t SZ_ACT = (size_t)SEQ * HD * 2;
constexpr int NR = 4, SL_MEM = 0, SL_P = 1, SL_VM = 5, SLOTS = 37;
constexpr size_t SZ_RING = (size_t)NR * SLOTS * 16384;
constexpr size_t SZ_PREG = SZ_RING;
constexpr size_t SZ_POISON = SZ_PREG + 16384;
constexpr int NPAGE = (int)(SZ_RING >> 21) + 2;
static_assert(NPAGE <= 4, "parity bits");
constexpr size_t OFF_TMO = (SZ_POISON + 255) & ~(size_t)255;
constexpr size_t OFF_MAIL = OFF_TMO + 65536;
constexpr size_t OFF_QA = OFF_MAIL + 65536;
constexpr size_t OFF_KA = OFF_QA + SZ_ACT;
constexpr size_t OFF_VA = OFF_KA + SZ_ACT;
constexpr size_t OFF_OA = OFF_VA + SZ_ACT;
constexpr size_t OFF_QB = OFF_OA + SZ_ACT;
constexpr size_t OFF_KB = OFF_QB + SZ_ACT;
constexpr size_t OFF_VOX = OFF_KB + SZ_ACT;
constexpr size_t OFF_PR = OFF_VOX + 4 * SZ_ACT;
constexpr size_t OFF_PART = OFF_PR + (size_t)NSEG * NH * 2048;
constexpr size_t SZ_PART = (size_t)5 * SEQ * DM * 4;
constexpr size_t OFF_BB = OFF_PART + SZ_PART;
constexpr size_t OFF_G = OFF_BB + (size_t)SEQ * DM * 2;
constexpr size_t OFF_WT = OFF_G + 8192;
constexpr size_t WS_END = OFF_WT + (size_t)HD * DM * 2;
constexpr int W_CTRA = 288, W_AQ = 128, W_CTRP = 320, W_PROG = 1024, W_QCNT = 2048, W_OQ = 3584, W_LIST = 4352, W_PG = 4736, W_CAL = 6656, W_VO = 8192, W_RDY = 10240, W_FA = 12288;
constexpr int MASTER = 159;
constexpr int FSP = 56;
constexpr int SPLIT = 60;

typedef __bf16 bf16x2_t __attribute__((ext_vector_type(2)));
__device__ __forceinline__ unsigned cvtpk(float lo, float hi) {
    const f32x2 f = {lo, hi}; return __builtin_bit_cast(unsigned, __builtin_convertvector(f, bf16x2_t));
}
__device__ __forceinline__ float bf2f(unsigned short b) { return __uint_as_float((unsigned)b << 16); }
__device__ __forceinline__ float rbf(float x) { return bf2f((unsigned short)(cvtpk(x, x) & 0xFFFF)); }

__device__ __forceinline__ unsigned off_b(unsigned row, unsigned ch) { return 256u * row + 16u * (ch ^ (((row & 3) << 2) | ((row >> 2) & 3))); }
__device__ __forceinline__ unsigned tr_addr16(unsigned lane, unsigned c, unsigned t) {
    const unsigned g = lane >> 4, q = (lane & 15) >> 2, p = lane & 3;
    return off_b(8 * g + 4 * t + q, 2 * c + (p >> 1)) + 8 * (p & 1);
}
__device__ __forceinline__ s16x4 tr_rd(const char* lds) { return __builtin_amdgcn_ds_read_tr16_b64_v4i16((LAS s16x4*)lds); }
__device__ __forceinline__ u32x2 tr_rdu(const char* lds) { return __builtin_bit_cast(u32x2, tr_rd(lds)); }
__device__ __forceinline__ bf16x8 cat8(s16x4 a, s16x4 b) {
    const u32x2 x = __builtin_bit_cast(u32x2, a), y = __builtin_bit_cast(u32x2, b);
    const u32x4 w = {x[0], x[1], y[0], y[1]}; return __builtin_bit_cast(bf16x8, w); }
__device__ __forceinline__ f32x4 smf(u32x4 a, u32x8 b, f32x4 c, int idx) {
    return __builtin_amdgcn_smfmac_f32_16x16x64_bf16(__builtin_bit_cast(bfv8, a), __builtin_bit_cast(bfv16, b), c, idx, 0, 0); }
__device__ __forceinline__ u32x8 ilv(u32x2 a0, u32x2 a1, u32x2 b0, u32x2 b1) {
    return u32x8{a0[0], a1[0], a0[1], a1[1], b0[0], b1[0], b0[1], b1[1]}; }
__device__ __forceinline__ float swadd(float a, float b) {
    auto r = __builtin_amdgcn_permlane32_swap(__float_as_uint(a), __float_as_uint(b), false, false);
    return __uint_as_float(r[0]) + __uint_as_float(r[1]); }
__device__ __forceinline__ u32x4 mk_rs(const void* p, unsigned bytes) {
    return u32x4{(unsigned)(uintptr_t)p, (unsigned)((uintptr_t)p >> 32) & 0xFFFFu, bytes, 0x00020000u}; }
__device__ __forceinline__ u32x4 ldw(const bf16* p, unsigned v, unsigned so) {
    return __builtin_amdgcn_raw_buffer_load_b128(__builtin_amdgcn_make_buffer_rsrc((void*)p, 0, DM * HD * 2, 0x00020000), v, so, WAUX); }
template <bool SC1>
__device__ __forceinline__ void dma16(u32x4 rs, unsigned voff, unsigned soff, unsigned ldsaddr) {
    unsigned keep;
    soff = __builtin_amdgcn_readfirstlane(soff); ldsaddr = __builtin_amdgcn_readfirstlane(ldsaddr);
    if (SC1) asm volatile("s_mov_b32 %0, m0\n\ts_mov_b32 m0, %4\n\ts_nop 0\n\tbuffer_load_dwordx4 %1, %2, %3 offen sc1 lds\n\ts_mov_b32 m0, %0"
                          : "=&s"(keep) : "v"(voff), "s"(rs), "s"(soff), "s"(ldsaddr) : "memory");
    else asm volatile("s_mov_b32 %0, m0\n\ts_mov_b32 m0, %4\n\ts_nop 0\n\tbuffer_load_dwordx4 %1, %2, %3 offen lds\n\ts_mov_b32 m0, %0"
                      : "=&s"(keep) : "v"(voff), "s"(rs), "s"(soff), "s"(ldsaddr) : "memory");
}
#define DMA4K "buffer_load_dwordx4 %2, %3, %1 offen sc1 lds\n\tbuffer_load_dwordx4 %2, %3, %1 offen offset:1024 sc1 lds\n\t" \
              "buffer_load_dwordx4 %2, %3, %1 offen offset:2048 sc1 lds\n\tbuffer_load_dwordx4 %2, %3, %1 offen offset:3072 sc1 lds\n\t"
#define DMANX "s_add_u32 m0, m0, 0x1000\n\ts_add_u32 %1, %1, 0x1000\n\t"
__device__ __forceinline__ void dma16k(u32x4 rs, unsigned voff, unsigned soff, unsigned ldsaddr) {
    unsigned keep;
    soff = __builtin_amdgcn_readfirstlane(soff); ldsaddr = __builtin_amdgcn_readfirstlane(ldsaddr);
    asm volatile("s_mov_b32 %0, m0\n\ts_mov_b32 m0, %4\n\ts_nop 0\n\t" DMA4K DMANX DMA4K DMANX DMA4K DMANX DMA4K "s_mov_b32 m0, %0"
                 : "=&s"(keep), "+s"(soff) : "v"(voff), "s"(rs), "s"(ldsaddr) : "memory", "scc");
}
__device__ __forceinline__ void dma4(u32x4 rs, unsigned voff, unsigned ldsaddr) {
    unsigned keep;
    ldsaddr = __builtin_amdgcn_readfirstlane(ldsaddr);
    asm volatile("s_mov_b32 %0, m0\n\ts_mov_b32 m0, %3\n\ts_nop 0\n\tbuffer_load_dword %1, %2, 0 offen sc1 lds\n\ts_mov_b32 m0, %0"
                 : "=&s"(keep) : "v"(voff), "s"(rs), "s"(ldsaddr) : "memory");
}
template <class SO, class USE>
__device__ __forceinline__ void stream_w(const bf16* W, unsigned str, SO so, USE use, char* lds, int wid, int lane) {
    const u32x4 rs = mk_rs(W, DM * HD * 2);
    const unsigned vo = (4 * (wid & 3) + (lane >> 4)) * str + ((lane & 15) ^ (((lane >> 4) << 2) | (wid & 3))) * 16;
    const unsigned la = __builtin_amdgcn_readfirstlane((unsigned)(uintptr_t)lds + (wid >> 2) * 16384 + (wid & 3) * 1024);
    auto ask = [&](int i) {
#pragma unroll
        for (int p = 0; p < 4; ++p) { unsigned keep;
            asm volatile("s_mov_b32 %0, m0\n\ts_mov_b32 m0, %4\n\ts_nop 0\n\tbuffer_load_dwordx4 %1, %2, %3 offen" WNT " lds\n\ts_mov_b32 m0, %0"
                         : "=&s"(keep) : "v"(vo), "s"(rs), "s"(__builtin_amdgcn_readfirstlane(so(i, p))), "s"(la + (i & 3) * 32768 + p * 4096) : "memory"); } };
#pragma unroll
    for (int i = 0; i < 4; ++i) { ask(i); if (i < 3) __builtin_amdgcn_s_barrier(); }
#pragma unroll
    for (int i = 0; i < 8; ++i) {
        asm volatile("s_waitcnt vmcnt(%0)\n\ts_barrier" :: "i"(i < 5 ? 12 : 28 - 4 * i) : "memory");
        use(i, lds + (wid >> 2) * 16384 + (i & 3) * 32768);
        if (i < 4) { asm volatile("s_waitcnt lgkmcnt(0)\n\ts_barrier" ::: "memory"); ask(i + 4); } }
}
#define LDS_BARRIER() do { asm volatile("s_waitcnt lgkmcnt(0)" ::: "memory"); __builtin_amdgcn_s_barrier(); asm volatile("" ::: "memory"); } while (0)
#define LRD(d, a, o) asm volatile("ds_read_b128 %0, %1 offset:%2" : "=v"(d) : "v"(a), "i"(o))
#define LWAIT1(n, a) asm volatile("s_waitcnt lgkmcnt(%1)" : "+v"(a) : "i"(n))
#define LWAIT2(n, a, b) asm volatile("s_waitcnt lgkmcnt(" #n ")" : "+v"(a), "+v"(b))
#define VMWAIT(n) asm volatile("s_waitcnt vmcnt(%0)" :: "i"(n) : "memory")

constexpr unsigned SPIN_CAP = 1u << 17;
__device__ __forceinline__ bool bail(unsigned spins, unsigned cap, unsigned* tmo, unsigned code) {
    if ((spins & 63u) == 63u && __hip_atomic_load(tmo, __ATOMIC_RELAXED, __HIP_MEMORY_SCOPE_AGENT) != 0u) return true;
    if (spins > cap) { __hip_atomic_store(tmo, code, __ATOMIC_RELAXED, __HIP_MEMORY_SCOPE_AGENT); return true; }
    return false; }
template <int SLP>
__device__ __forceinline__ unsigned wave_wait(unsigned* w, unsigned target, unsigned* tmo, unsigned code, unsigned mask = ~0u) {
    unsigned v;
    for (unsigned spins = 0;; ++spins) {
        v = __builtin_amdgcn_readfirstlane(__hip_atomic_load(w, __ATOMIC_RELAXED, __HIP_MEMORY_SCOPE_AGENT));
        if ((v & mask) >= target) break;
        if (bail(spins, SPIN_CAP << 1, tmo, code)) break;
        __builtin_amdgcn_s_sleep(SLP); asm volatile("" ::: "memory");
    }
    return v;
}
__device__ __forceinline__ unsigned pin_s(unsigned x) { x = __builtin_amdgcn_readfirstlane(x); asm volatile("" : "+s"(x)); return x; }
constexpr int NPA = 96;
struct Hello {
    unsigned long long* rep; unsigned long long num;
#define PRB(k) do {} while (0)
    __device__ __forceinline__ unsigned long long peek() const { return __hip_atomic_load(rep, __ATOMIC_RELAXED, __HIP_MEMORY_SCOPE_AGENT); }
    __device__ __forceinline__ void wait(unsigned long long v) const {
        for (unsigned i = 0; v != num; ++i) {
            if (i > SPIN_CAP) { __hip_atomic_store((unsigned*)((char*)(rep - blockIdx.x) - 98304), 0xC0000u, __ATOMIC_RELAXED, __HIP_MEMORY_SCOPE_AGENT); break; }
            __builtin_amdgcn_s_sleep(4); v = peek(); } }
};

#define ASM_PROJ_PUB \
        "s_mov_b32 %[keep], m0\n\ts_mov_b32 %[n], 0\n\tv_add_u32 v180, 0x18000, %[xb]\n\tv_xor_b32 v182, 64, v180\n\tds_read_b128 v[128:131], v180 offset:0\n\t" \
        "ds_read_b128 v[132:135], v182 offset:0\n\tds_read_b128 v[136:139], v180 offset:1024\n\tds_read_b128 v[140:143], v182 offset:1024\n\t1:\ns_waitcnt lgkmcnt(3)\n\t" \
        "v_mfma_f32_16x16x32_bf16 v[160:163], v[0:3], v[128:131], 0\n\tds_read_b128 v[144:147], v180 offset:2048\n\tv_mfma_f32_16x16x32_bf16 v[164:167], v[64:67], v[128:131], 0\n\t" \
        "s_lshl_b32 %[im], %[n], 15\n\ts_and_b32 %[im], %[im], 0x18000\n\ts_waitcnt lgkmcnt(3)\n\tv_mfma_f32_16x16x32_bf16 v[160:163], v[4:7], v[132:135], v[160:163]\n\t" \
        "ds_read_b128 v[148:151], v182 offset:2048\n\tv_mfma_f32_16x16x32_bf16 v[164:167], v[68:71], v[132:135], v[164:167]\n\ts_xor_b32 %[t0], %[im], 0x10000\n\ts_add_u32 %[t0], %[t0], %[lb]\n\t" \
        "s_cmp_eq_u32 %[n], 0\n\ts_waitcnt lgkmcnt(3)\n\tv_mfma_f32_16x16x32_bf16 v[160:163], v[8:11], v[136:139], v[160:163]\n\tds_read_b128 v[152:155], v180 offset:3072\n\t" \
        "v_mfma_f32_16x16x32_bf16 v[164:167], v[72:75], v[136:139], v[164:167]\n\tv_mul_f32 v168, %[sc], v168\n\tv_mul_f32 v169, %[sc], v169\n\tv_mul_f32 v170, %[sc], v170\n\t" \
        "v_mul_f32 v171, %[sc], v171\n\ts_waitcnt lgkmcnt(3)\n\tv_mfma_f32_16x16x32_bf16 v[160:163], v[12:15], v[140:143], v[160:163]\n\tds_read_b128 v[156:159], v182 offset:3072\n\t" \
        "v_mfma_f32_16x16x32_bf16 v[164:167], v[76:79], v[140:143], v[164:167]\n\tv_mul_f32 v172, %[sc], v172\n\tv_mul_f32 v173, %[sc], v173\n\tv_mul_f32 v174, %[sc], v174\n\t" \
        "v_mul_f32 v175, %[sc], v175\n\ts_waitcnt lgkmcnt(3)\n\tv_mfma_f32_16x16x32_bf16 v[160:163], v[16:19], v[144:147], v[160:163]\n\tds_read_b128 v[128:131], v180 offset:4096\n\t" \
        "v_mfma_f32_16x16x32_bf16 v[164:167], v[80:83], v[144:147], v[164:167]\n\ts_cbranch_scc1 2f\n\tv_cvt_pk_bf16_f32 v176, v168, v169\n\tv_cvt_pk_bf16_f32 v177, v170, v171\n\t" \
        "v_cvt_pk_bf16_f32 v178, v172, v173\n\tv_cvt_pk_bf16_f32 v179, v174, v175\n\tbuffer_store_dwordx4 v[176:179], %[co], %[rc], %[sp] offen offset:1024 sc1\n\t2:\ns_waitcnt lgkmcnt(3)\n\t" \
        "v_mfma_f32_16x16x32_bf16 v[160:163], v[20:23], v[148:151], v[160:163]\n\tds_read_b128 v[132:135], v182 offset:4096\n\t" \
        "v_mfma_f32_16x16x32_bf16 v[164:167], v[84:87], v[148:151], v[164:167]\n\ts_add_u32 %[sp], %[sp], 262144\n\ts_waitcnt lgkmcnt(3)\n\t" \
        "v_mfma_f32_16x16x32_bf16 v[160:163], v[24:27], v[152:155], v[160:163]\n\tds_read_b128 v[136:139], v180 offset:5120\n\t" \
        "v_mfma_f32_16x16x32_bf16 v[164:167], v[88:91], v[152:155], v[164:167]\n\ts_waitcnt lgkmcnt(3)\n\tv_mfma_f32_16x16x32_bf16 v[160:163], v[28:31], v[156:159], v[160:163]\n\t" \
        "ds_read_b128 v[140:143], v182 offset:5120\n\tv_mfma_f32_16x16x32_bf16 v[164:167], v[92:95], v[156:159], v[164:167]\n\ts_sub_u32 %[t1], %[n], 1\n\ts_cmp_lt_u32 %[n], 3\n\t" \
        "s_waitcnt lgkmcnt(3)\n\tv_mfma_f32_16x16x32_bf16 v[160:163], v[32:35], v[128:131], v[160:163]\n\tds_read_b128 v[144:147], v180 offset:6144\n\t" \
        "v_mfma_f32_16x16x32_bf16 v[164:167], v[96:99], v[128:131], v[164:167]\n\ts_cbranch_scc1 4f\n\ts_waitcnt vmcnt(6)\n\t3:\ns_barrier\n\ts_mov_b32 m0, %[t0]\n\tv_mov_b32 v181, %[t1]\n\t" \
        "s_mov_b64 exec, %[em]\n\tglobal_store_dword %[co], v181, %[pg] sc1\n\ts_mov_b64 exec, -1\n\ts_waitcnt lgkmcnt(3)\n\tv_mfma_f32_16x16x32_bf16 v[160:163], v[36:39], v[132:135], v[160:163]\n\t" \
        "ds_read_b128 v[148:151], v182 offset:6144\n\tv_mfma_f32_16x16x32_bf16 v[164:167], v[100:103], v[132:135], v[164:167]\n\tbuffer_load_dwordx4 %[dv], %[ra], %[so] offen lds\n\t" \
        "s_add_u32 m0, m0, 896\n\ts_waitcnt lgkmcnt(3)\n\tv_mfma_f32_16x16x32_bf16 v[160:163], v[40:43], v[136:139], v[160:163]\n\tds_read_b128 v[152:155], v180 offset:7168\n\t" \
        "v_mfma_f32_16x16x32_bf16 v[164:167], v[104:107], v[136:139], v[164:167]\n\ts_waitcnt lgkmcnt(3)\n\tv_mfma_f32_16x16x32_bf16 v[160:163], v[44:47], v[140:143], v[160:163]\n\t" \
        "ds_read_b128 v[156:159], v182 offset:7168\n\tv_mfma_f32_16x16x32_bf16 v[164:167], v[108:111], v[140:143], v[164:167]\n\tbuffer_load_dwordx4 %[dv], %[ra], %[so] offen offset:128 lds\n\t" \
        "s_add_u32 m0, m0, 896\n\ts_waitcnt lgkmcnt(3)\n\tv_mfma_f32_16x16x32_bf16 v[160:163], v[48:51], v[144:147], v[160:163]\n\tds_read_b128 v[128:131], v180 offset:16384\n\t" \
        "v_mfma_f32_16x16x32_bf16 v[164:167], v[112:115], v[144:147], v[164:167]\n\ts_waitcnt lgkmcnt(3)\n\tv_mfma_f32_16x16x32_bf16 v[160:163], v[52:55], v[148:151], v[160:163]\n\t" \
        "ds_read_b128 v[132:135], v182 offset:16384\n\tv_mfma_f32_16x16x32_bf16 v[164:167], v[116:119], v[148:151], v[164:167]\n\tbuffer_load_dwordx4 %[dv], %[ra], %[so] offen offset:256 lds\n\t" \
        "s_add_u32 m0, m0, 896\n\ts_waitcnt lgkmcnt(3)\n\tv_mfma_f32_16x16x32_bf16 v[160:163], v[56:59], v[152:155], v[160:163]\n\tds_read_b128 v[136:139], v180 offset:17408\n\t" \
        "v_mfma_f32_16x16x32_bf16 v[164:167], v[120:123], v[152:155], v[164:167]\n\ts_waitcnt lgkmcnt(3)\n\tv_mfma_f32_16x16x32_bf16 v[160:163], v[60:63], v[156:159], v[160:163]\n\t" \
        "ds_read_b128 v[140:143], v182 offset:17408\n\tv_mfma_f32_16x16x32_bf16 v[164:167], v[124:127], v[156:159], v[164:167]\n\tbuffer_load_dwordx4 %[dv], %[ra], %[so] offen offset:384 lds\n\t" \
        "s_add_u32 %[so], %[so], 32768\n\ts_min_u32 %[so], %[so], 0x1f8000\n\ts_waitcnt lgkmcnt(3)\n\tv_mfma_f32_16x16x32_bf16 v[168:171], v[0:3], v[128:131], 0\n\t" \
        "ds_read_b128 v[144:147], v180 offset:18432\n\tv_mfma_f32_16x16x32_bf16 v[172:175], v[64:67], v[128:131], 0\n\ts_waitcnt lgkmcnt(3)\n\t" \
        "v_mfma_f32_16x16x32_bf16 v[168:171], v[4:7], v[132:135], v[168:171]\n\tds_read_b128 v[148:151], v182 offset:18432\n\tv_mfma_f32_16x16x32_bf16 v[172:175], v[68:71], v[132:135], v[172:175]\n\t" \
        "s_waitcnt lgkmcnt(3)\n\tv_mfma_f32_16x16x32_bf16 v[168:171], v[8:11], v[136:139], v[168:171]\n\tds_read_b128 v[152:155], v180 offset:19456\n\t" \
        "v_mfma_f32_16x16x32_bf16 v[172:175], v[72:75], v[136:139], v[172:175]\n\tv_mul_f32 v160, %[sc], v160\n\tv_mul_f32 v161, %[sc], v161\n\tv_mul_f32 v162, %[sc], v162\n\t" \
        "v_mul_f32 v163, %[sc], v163\n\ts_waitcnt lgkmcnt(3)\n\tv_mfma_f32_16x16x32_bf16 v[168:171], v[12:15], v[140:143], v[168:171]\n\tds_read_b128 v[156:159], v182 offset:19456\n\t" \
        "v_mfma_f32_16x16x32_bf16 v[172:175], v[76:79], v[140:143], v[172:175]\n\tv_mul_f32 v164, %[sc], v164\n\tv_mul_f32 v165, %[sc], v165\n\tv_mul_f32 v166, %[sc], v166\n\t" \
        "v_mul_f32 v167, %[sc], v167\n\ts_waitcnt lgkmcnt(3)\n\tv_mfma_f32_16x16x32_bf16 v[168:171], v[16:19], v[144:147], v[168:171]\n\tds_read_b128 v[128:131], v180 offset:20480\n\t" \
        "v_mfma_f32_16x16x32_bf16 v[172:175], v[80:83], v[144:147], v[172:175]\n\tv_cvt_pk_bf16_f32 v176, v160, v161\n\tv_cvt_pk_bf16_f32 v177, v162, v163\n\tv_cvt_pk_bf16_f32 v178, v164, v165\n\t" \
        "v_cvt_pk_bf16_f32 v179, v166, v167\n\tbuffer_store_dwordx4 v[176:179], %[co], %[rc], %[sp] offen sc1\n\ts_waitcnt lgkmcnt(3)\n\t" \
        "v_mfma_f32_16x16x32_bf16 v[168:171], v[20:23], v[148:151], v[168:171]\n\tds_read_b128 v[132:135], v182 offset:20480\n\t" \
        "v_mfma_f32_16x16x32_bf16 v[172:175], v[84:87], v[148:151], v[172:175]\n\ts_waitcnt lgkmcnt(3)\n\tv_mfma_f32_16x16x32_bf16 v[168:171], v[24:27], v[152:155], v[168:171]\n\t" \
        "ds_read_b128 v[136:139], v180 offset:21504\n\tv_mfma_f32_16x16x32_bf16 v[172:175], v[88:91], v[152:155], v[172:175]\n\ts_waitcnt lgkmcnt(3)\n\t" \
        "v_mfma_f32_16x16x32_bf16 v[168:171], v[28:31], v[156:159], v[168:171]\n\tds_read_b128 v[140:143], v182 offset:21504\n\t" \
        "v_mfma_f32_16x16x32_bf16 v[172:175], v[92:95], v[156:159], v[172:175]\n\ts_waitcnt lgkmcnt(3)\n\tv_mfma_f32_16x16x32_bf16 v[168:171], v[32:35], v[128:131], v[168:171]\n\t" \
        "ds_read_b128 v[144:147], v180 offset:22528\n\tv_mfma_f32_16x16x32_bf16 v[172:175], v[96:99], v[128:131], v[172:175]\n\ts_sub_u32 %[t1], %[n], 40\n\ts_and_b32 %[t0], %[t1], 0xfffffff1\n\t" \
        "s_cbranch_scc0 6f\n\t7:\ns_waitcnt lgkmcnt(3)\n\tv_mfma_f32_16x16x32_bf16 v[168:171], v[36:39], v[132:135], v[168:171]\n\tds_read_b128 v[148:151], v182 offset:22528\n\t" \
        "v_mfma_f32_16x16x32_bf16 v[172:175], v[100:103], v[132:135], v[172:175]\n\ts_waitcnt lgkmcnt(3)\n\tv_mfma_f32_16x16x32_bf16 v[168:171], v[40:43], v[136:139], v[168:171]\n\t" \
        "ds_read_b128 v[152:155], v180 offset:23552\n\tv_mfma_f32_16x16x32_bf16 v[172:175], v[104:107], v[136:139], v[172:175]\n\ts_waitcnt lgkmcnt(3)\n\t" \
        "v_mfma_f32_16x16x32_bf16 v[168:171], v[44:47], v[140:143], v[168:171]\n\tds_read_b128 v[156:159], v182 offset:23552\n\t" \
        "v_mfma_f32_16x16x32_bf16 v[172:175], v[108:111], v[140:143], v[172:175]\n\tv_add_u32 v180, %[im], %[xb]\n\tv_xor_b32 v182, 64, v180\n\ts_waitcnt lgkmcnt(3)\n\t" \
        "v_mfma_f32_16x16x32_bf16 v[168:171], v[48:51], v[144:147], v[168:171]\n\tds_read_b128 v[128:131], v180 offset:0\n\tv_mfma_f32_16x16x32_bf16 v[172:175], v[112:115], v[144:147], v[172:175]\n\t" \
        "s_waitcnt lgkmcnt(3)\n\tv_mfma_f32_16x16x32_bf16 v[168:171], v[52:55], v[148:151], v[168:171]\n\tds_read_b128 v[132:135], v182 offset:0\n\t" \
        "v_mfma_f32_16x16x32_bf16 v[172:175], v[116:119], v[148:151], v[172:175]\n\ts_waitcnt lgkmcnt(3)\n\tv_mfma_f32_16x16x32_bf16 v[168:171], v[56:59], v[152:155], v[168:171]\n\t" \
        "ds_read_b128 v[136:139], v180 offset:1024\n\tv_mfma_f32_16x16x32_bf16 v[172:175], v[120:123], v[152:155], v[172:175]\n\ts_add_u32 %[n], %[n], 1\n\ts_cmp_lt_u32 %[n], 64\n\t" \
        "s_waitcnt lgkmcnt(3)\n\tv_mfma_f32_16x16x32_bf16 v[168:171], v[60:63], v[156:159], v[168:171]\n\tds_read_b128 v[140:143], v182 offset:1024\n\t" \
        "v_mfma_f32_16x16x32_bf16 v[172:175], v[124:127], v[156:159], v[172:175]\n\ts_cbranch_scc1 1b\n\ts_mov_b32 m0, %[keep]\n\ts_waitcnt lgkmcnt(0)\n\ts_nop 7\n\tv_mul_f32 v168, %[sc], v168\n\t" \
        "v_mul_f32 v169, %[sc], v169\n\tv_mul_f32 v170, %[sc], v170\n\tv_mul_f32 v171, %[sc], v171\n\tv_mul_f32 v172, %[sc], v172\n\tv_mul_f32 v173, %[sc], v173\n\tv_mul_f32 v174, %[sc], v174\n\t" \
        "v_mul_f32 v175, %[sc], v175\n\tv_cvt_pk_bf16_f32 v176, v168, v169\n\tv_cvt_pk_bf16_f32 v177, v170, v171\n\tv_cvt_pk_bf16_f32 v178, v172, v173\n\tv_cvt_pk_bf16_f32 v179, v174, v175\n\t" \
        "buffer_store_dwordx4 v[176:179], %[co], %[rc], %[sp] offen offset:1024 sc1\n\ts_branch 5f\n\t4:\ns_waitcnt vmcnt(0)\n\ts_mov_b32 %[t1], %[n]\n\ts_branch 3b\n\t6:\n" \
        "s_lshl_b32 %[t0], %[t1], 13\n\ts_cmp_lg_u32 %[t1], 0\n\ts_cbranch_scc1 10f\n\tbuffer_load_dwordx4 v[184:187], %[wv], %[rw], %[t0] offen nt\n\ts_add_u32 %[t0], %[t0], 0x2000\n\t" \
        "buffer_load_dwordx4 v[188:191], %[wv], %[rw], %[t0] offen nt\n\ts_branch 7b\n\t10:\ns_cmp_lg_u32 %[t1], 2\n\ts_cbranch_scc1 11f\n\t" \
        "buffer_load_dwordx4 v[192:195], %[wv], %[rw], %[t0] offen nt\n\ts_add_u32 %[t0], %[t0], 0x2000\n\tbuffer_load_dwordx4 v[196:199], %[wv], %[rw], %[t0] offen nt\n\ts_branch 7b\n\t11:\n" \
        "s_cmp_lg_u32 %[t1], 4\n\ts_cbranch_scc1 12f\n\tbuffer_load_dwordx4 v[200:203], %[wv], %[rw], %[t0] offen nt\n\ts_add_u32 %[t0], %[t0], 0x2000\n\t" \
        "buffer_load_dwordx4 v[204:207], %[wv], %[rw], %[t0] offen nt\n\ts_branch 7b\n\t12:\ns_cmp_lg_u32 %[t1], 6\n\ts_cbranch_scc1 13f\n\t" \
        "buffer_load_dwordx4 v[208:211], %[wv], %[rw], %[t0] offen nt\n\ts_add_u32 %[t0], %[t0], 0x2000\n\tbuffer_load_dwordx4 v[212:215], %[wv], %[rw], %[t0] offen nt\n\ts_branch 7b\n\t13:\n" \
        "s_cmp_lg_u32 %[t1], 8\n\ts_cbranch_scc1 14f\n\tbuffer_load_dwordx4 v[216:219], %[wv], %[rw], %[t0] offen nt\n\ts_add_u32 %[t0], %[t0], 0x2000\n\t" \
        "buffer_load_dwordx4 v[220:223], %[wv], %[rw], %[t0] offen nt\n\ts_branch 7b\n\t14:\ns_cmp_lg_u32 %[t1], 10\n\ts_cbranch_scc1 15f\n\t" \
        "buffer_load_dwordx4 v[224:227], %[wv], %[rw], %[t0] offen nt\n\ts_add_u32 %[t0], %[t0], 0x2000\n\tbuffer_load_dwordx4 v[228:231], %[wv], %[rw], %[t0] offen nt\n\ts_branch 7b\n\t15:\n" \
        "s_cmp_lg_u32 %[t1], 12\n\ts_cbranch_scc1 16f\n\tbuffer_load_dwordx4 v[232:235], %[wv], %[rw], %[t0] offen nt\n\ts_add_u32 %[t0], %[t0], 0x2000\n\t" \
        "buffer_load_dwordx4 v[236:239], %[wv], %[rw], %[t0] offen nt\n\ts_branch 7b\n\t16:\nbuffer_load_dwordx4 v[240:243], %[wv], %[rw], %[t0] offen nt\n\ts_add_u32 %[t0], %[t0], 0x2000\n\t" \
        "buffer_load_dwordx4 v[244:247], %[wv], %[rw], %[t0] offen nt\n\ts_branch 7b\n\t5:\ns_waitcnt vmcnt(0)\n\t"
#define ASM_PROJ_A \
        "s_mov_b32 %[keep], m0\n\ts_mov_b32 %[n], 0\n\tv_add_u32 v180, 0x18000, %[xb]\n\tv_xor_b32 v182, 64, v180\n\tds_read_b128 v[128:131], v180 offset:0\n\t" \
        "ds_read_b128 v[132:135], v182 offset:0\n\tds_read_b128 v[136:139], v180 offset:1024\n\tds_read_b128 v[140:143], v182 offset:1024\n\t1:\ns_waitcnt lgkmcnt(3)\n\t" \
        "v_mfma_f32_16x16x32_bf16 v[160:163], v[0:3], v[128:131], 0\n\tds_read_b128 v[144:147], v180 offset:2048\n\tv_mfma_f32_16x16x32_bf16 v[164:167], v[64:67], v[128:131], 0\n\t" \
        "s_lshl_b32 %[im], %[n], 15\n\ts_and_b32 %[im], %[im], 0x18000\n\ts_waitcnt lgkmcnt(3)\n\tv_mfma_f32_16x16x32_bf16 v[160:163], v[4:7], v[132:135], v[160:163]\n\t" \
        "ds_read_b128 v[148:151], v182 offset:2048\n\tv_mfma_f32_16x16x32_bf16 v[164:167], v[68:71], v[132:135], v[164:167]\n\ts_xor_b32 %[t0], %[im], 0x10000\n\ts_add_u32 %[t0], %[t0], %[lb]\n\t" \
        "s_cmp_eq_u32 %[n], 0\n\ts_waitcnt lgkmcnt(3)\n\tv_mfma_f32_16x16x32_bf16 v[160:163], v[8:11], v[136:139], v[160:163]\n\tds_read_b128 v[152:155], v180 offset:3072\n\t" \
        "v_mfma_f32_16x16x32_bf16 v[164:167], v[72:75], v[136:139], v[164:167]\n\tv_mul_f32 v168, %[sc], v168\n\tv_mul_f32 v169, %[sc], v169\n\tv_mul_f32 v170, %[sc], v170\n\t" \
        "v_mul_f32 v171, %[sc], v171\n\ts_waitcnt lgkmcnt(3)\n\tv_mfma_f32_16x16x32_bf16 v[160:163], v[12:15], v[140:143], v[160:163]\n\tds_read_b128 v[156:159], v182 offset:3072\n\t" \
        "v_mfma_f32_16x16x32_bf16 v[164:167], v[76:79], v[140:143], v[164:167]\n\tv_mul_f32 v172, %[sc], v172\n\tv_mul_f32 v173, %[sc], v173\n\tv_mul_f32 v174, %[sc], v174\n\t" \
        "v_mul_f32 v175, %[sc], v175\n\ts_waitcnt lgkmcnt(3)\n\tv_mfma_f32_16x16x32_bf16 v[160:163], v[16:19], v[144:147], v[160:163]\n\tds_read_b128 v[128:131], v180 offset:4096\n\t" \
        "v_mfma_f32_16x16x32_bf16 v[164:167], v[80:83], v[144:147], v[164:167]\n\ts_cbranch_scc1 2f\n\tv_cvt_pk_bf16_f32 v176, v168, v169\n\tv_cvt_pk_bf16_f32 v177, v170, v171\n\t" \
        "v_cvt_pk_bf16_f32 v178, v172, v173\n\tv_cvt_pk_bf16_f32 v179, v174, v175\n\tbuffer_store_dwordx4 v[176:179], %[co], %[rc], %[sp] offen offset:1024 sc1\n\t2:\ns_waitcnt lgkmcnt(3)\n\t" \
        "v_mfma_f32_16x16x32_bf16 v[160:163], v[20:23], v[148:151], v[160:163]\n\tds_read_b128 v[132:135], v182 offset:4096\n\t" \
        "v_mfma_f32_16x16x32_bf16 v[164:167], v[84:87], v[148:151], v[164:167]\n\ts_add_u32 %[sp], %[sp], 524288\n\ts_waitcnt lgkmcnt(3)\n\t" \
        "v_mfma_f32_16x16x32_bf16 v[160:163], v[24:27], v[152:155], v[160:163]\n\tds_read_b128 v[136:139], v180 offset:5120\n\t" \
        "v_mfma_f32_16x16x32_bf16 v[164:167], v[88:91], v[152:155], v[164:167]\n\ts_waitcnt lgkmcnt(3)\n\tv_mfma_f32_16x16x32_bf16 v[160:163], v[28:31], v[156:159], v[160:163]\n\t" \
        "ds_read_b128 v[140:143], v182 offset:5120\n\tv_mfma_f32_16x16x32_bf16 v[164:167], v[92:95], v[156:159], v[164:167]\n\ts_cmp_lt_u32 %[n], 2\n\ts_waitcnt lgkmcnt(3)\n\t" \
        "v_mfma_f32_16x16x32_bf16 v[160:163], v[32:35], v[128:131], v[160:163]\n\tds_read_b128 v[144:147], v180 offset:6144\n\t" \
        "v_mfma_f32_16x16x32_bf16 v[164:167], v[96:99], v[128:131], v[164:167]\n\ts_cbranch_scc1 4f\n\ts_waitcnt vmcnt(8)\n\t3:\ns_barrier\n\ts_mov_b32 m0, %[t0]\n\ts_waitcnt lgkmcnt(3)\n\t" \
        "v_mfma_f32_16x16x32_bf16 v[160:163], v[36:39], v[132:135], v[160:163]\n\tds_read_b128 v[148:151], v182 offset:6144\n\t" \
        "v_mfma_f32_16x16x32_bf16 v[164:167], v[100:103], v[132:135], v[164:167]\n\tbuffer_load_dwordx4 %[dv], %[ra], %[so] offen lds\n\ts_add_u32 m0, m0, 896\n\ts_waitcnt lgkmcnt(3)\n\t" \
        "v_mfma_f32_16x16x32_bf16 v[160:163], v[40:43], v[136:139], v[160:163]\n\tds_read_b128 v[152:155], v180 offset:7168\n\t" \
        "v_mfma_f32_16x16x32_bf16 v[164:167], v[104:107], v[136:139], v[164:167]\n\ts_waitcnt lgkmcnt(3)\n\tv_mfma_f32_16x16x32_bf16 v[160:163], v[44:47], v[140:143], v[160:163]\n\t" \
        "ds_read_b128 v[156:159], v182 offset:7168\n\tv_mfma_f32_16x16x32_bf16 v[164:167], v[108:111], v[140:143], v[164:167]\n\tbuffer_load_dwordx4 %[dv], %[ra], %[so] offen offset:128 lds\n\t" \
        "s_add_u32 m0, m0, 896\n\ts_waitcnt lgkmcnt(3)\n\tv_mfma_f32_16x16x32_bf16 v[160:163], v[48:51], v[144:147], v[160:163]\n\tds_read_b128 v[128:131], v180 offset:16384\n\t" \
        "v_mfma_f32_16x16x32_bf16 v[164:167], v[112:115], v[144:147], v[164:167]\n\ts_waitcnt lgkmcnt(3)\n\tv_mfma_f32_16x16x32_bf16 v[160:163], v[52:55], v[148:151], v[160:163]\n\t" \
        "ds_read_b128 v[132:135], v182 offset:16384\n\tv_mfma_f32_16x16x32_bf16 v[164:167], v[116:119], v[148:151], v[164:167]\n\tbuffer_load_dwordx4 %[dv], %[ra], %[so] offen offset:256 lds\n\t" \
        "s_add_u32 m0, m0, 896\n\ts_waitcnt lgkmcnt(3)\n\tv_mfma_f32_16x16x32_bf16 v[160:163], v[56:59], v[152:155], v[160:163]\n\tds_read_b128 v[136:139], v180 offset:17408\n\t" \
        "v_mfma_f32_16x16x32_bf16 v[164:167], v[120:123], v[152:155], v[164:167]\n\ts_waitcnt lgkmcnt(3)\n\tv_mfma_f32_16x16x32_bf16 v[160:163], v[60:63], v[156:159], v[160:163]\n\t" \
        "ds_read_b128 v[140:143], v182 offset:17408\n\tv_mfma_f32_16x16x32_bf16 v[164:167], v[124:127], v[156:159], v[164:167]\n\tbuffer_load_dwordx4 %[dv], %[ra], %[so] offen offset:384 lds\n\t" \
        "s_add_u32 %[so], %[so], 65536\n\ts_min_u32 %[so], %[so], 0x1f8000\n\ts_waitcnt lgkmcnt(3)\n\tv_mfma_f32_16x16x32_bf16 v[168:171], v[0:3], v[128:131], 0\n\t" \
        "ds_read_b128 v[144:147], v180 offset:18432\n\tv_mfma_f32_16x16x32_bf16 v[172:175], v[64:67], v[128:131], 0\n\ts_waitcnt lgkmcnt(3)\n\t" \
        "v_mfma_f32_16x16x32_bf16 v[168:171], v[4:7], v[132:135], v[168:171]\n\tds_read_b128 v[148:151], v182 offset:18432\n\tv_mfma_f32_16x16x32_bf16 v[172:175], v[68:71], v[132:135], v[172:175]\n\t" \
        "s_waitcnt lgkmcnt(3)\n\tv_mfma_f32_16x16x32_bf16 v[168:171], v[8:11], v[136:139], v[168:171]\n\tds_read_b128 v[152:155], v180 offset:19456\n\t" \
        "v_mfma_f32_16x16x32_bf16 v[172:175], v[72:75], v[136:139], v[172:175]\n\tv_mul_f32 v160, %[sc], v160\n\tv_mul_f32 v161, %[sc], v161\n\tv_mul_f32 v162, %[sc], v162\n\t" \
        "v_mul_f32 v163, %[sc], v163\n\ts_waitcnt lgkmcnt(3)\n\tv_mfma_f32_16x16x32_bf16 v[168:171], v[12:15], v[140:143], v[168:171]\n\tds_read_b128 v[156:159], v182 offset:19456\n\t" \
        "v_mfma_f32_16x16x32_bf16 v[172:175], v[76:79], v[140:143], v[172:175]\n\tv_mul_f32 v164, %[sc], v164\n\tv_mul_f32 v165, %[sc], v165\n\tv_mul_f32 v166, %[sc], v166\n\t" \
        "v_mul_f32 v167, %[sc], v167\n\ts_waitcnt lgkmcnt(3)\n\tv_mfma_f32_16x16x32_bf16 v[168:171], v[16:19], v[144:147], v[168:171]\n\tds_read_b128 v[128:131], v180 offset:20480\n\t" \
        "v_mfma_f32_16x16x32_bf16 v[172:175], v[80:83], v[144:147], v[172:175]\n\tv_cvt_pk_bf16_f32 v176, v160, v161\n\tv_cvt_pk_bf16_f32 v177, v162, v163\n\tv_cvt_pk_bf16_f32 v178, v164, v165\n\t" \
        "v_cvt_pk_bf16_f32 v179, v166, v167\n\tbuffer_store_dwordx4 v[176:179], %[co], %[rc], %[sp] offen sc1\n\ts_waitcnt lgkmcnt(3)\n\t" \
        "v_mfma_f32_16x16x32_bf16 v[168:171], v[20:23], v[148:151], v[168:171]\n\tds_read_b128 v[132:135], v182 offset:20480\n\t" \
        "v_mfma_f32_16x16x32_bf16 v[172:175], v[84:87], v[148:151], v[172:175]\n\ts_waitcnt lgkmcnt(3)\n\tv_mfma_f32_16x16x32_bf16 v[168:171], v[24:27], v[152:155], v[168:171]\n\t" \
        "ds_read_b128 v[136:139], v180 offset:21504\n\tv_mfma_f32_16x16x32_bf16 v[172:175], v[88:91], v[152:155], v[172:175]\n\ts_waitcnt lgkmcnt(3)\n\t" \
        "v_mfma_f32_16x16x32_bf16 v[168:171], v[28:31], v[156:159], v[168:171]\n\tds_read_b128 v[140:143], v182 offset:21504\n\t" \
        "v_mfma_f32_16x16x32_bf16 v[172:175], v[92:95], v[156:159], v[172:175]\n\ts_waitcnt lgkmcnt(3)\n\tv_mfma_f32_16x16x32_bf16 v[168:171], v[32:35], v[128:131], v[168:171]\n\t" \
        "ds_read_b128 v[144:147], v180 offset:22528\n\tv_mfma_f32_16x16x32_bf16 v[172:175], v[96:99], v[128:131], v[172:175]\n\ts_waitcnt lgkmcnt(3)\n\t" \
        "v_mfma_f32_16x16x32_bf16 v[168:171], v[36:39], v[132:135], v[168:171]\n\tds_read_b128 v[148:151], v182 offset:22528\n\t" \
        "v_mfma_f32_16x16x32_bf16 v[172:175], v[100:103], v[132:135], v[172:175]\n\ts_waitcnt lgkmcnt(3)\n\tv_mfma_f32_16x16x32_bf16 v[168:171], v[40:43], v[136:139], v[168:171]\n\t" \
        "ds_read_b128 v[152:155], v180 offset:23552\n\tv_mfma_f32_16x16x32_bf16 v[172:175], v[104:107], v[136:139], v[172:175]\n\ts_waitcnt lgkmcnt(3)\n\t" \
        "v_mfma_f32_16x16x32_bf16 v[168:171], v[44:47], v[140:143], v[168:171]\n\tds_read_b128 v[156:159], v182 offset:23552\n\t" \
        "v_mfma_f32_16x16x32_bf16 v[172:175], v[108:111], v[140:143], v[172:175]\n\tv_add_u32 v180, %[im], %[xb]\n\tv_xor_b32 v182, 64, v180\n\ts_waitcnt lgkmcnt(3)\n\t" \
        "v_mfma_f32_16x16x32_bf16 v[168:171], v[48:51], v[144:147], v[168:171]\n\tds_read_b128 v[128:131], v180 offset:0\n\tv_mfma_f32_16x16x32_bf16 v[172:175], v[112:115], v[144:147], v[172:175]\n\t" \
        "s_waitcnt lgkmcnt(3)\n\tv_mfma_f32_16x16x32_bf16 v[168:171], v[52:55], v[148:151], v[168:171]\n\tds_read_b128 v[132:135], v182 offset:0\n\t" \
        "v_mfma_f32_16x16x32_bf16 v[172:175], v[116:119], v[148:151], v[172:175]\n\ts_waitcnt lgkmcnt(3)\n\tv_mfma_f32_16x16x32_bf16 v[168:171], v[56:59], v[152:155], v[168:171]\n\t" \
        "ds_read_b128 v[136:139], v180 offset:1024\n\tv_mfma_f32_16x16x32_bf16 v[172:175], v[120:123], v[152:155], v[172:175]\n\ts_add_u32 %[n], %[n], 1\n\ts_cmp_lt_u32 %[n], 32\n\t" \
        "s_waitcnt lgkmcnt(3)\n\tv_mfma_f32_16x16x32_bf16 v[168:171], v[60:63], v[156:159], v[168:171]\n\tds_read_b128 v[140:143], v182 offset:1024\n\t" \
        "v_mfma_f32_16x16x32_bf16 v[172:175], v[124:127], v[156:159], v[172:175]\n\ts_cbranch_scc1 1b\n\ts_mov_b32 m0, %[keep]\n\ts_waitcnt lgkmcnt(0)\n\ts_nop 7\n\tv_mul_f32 v168, %[sc], v168\n\t" \
        "v_mul_f32 v169, %[sc], v169\n\tv_mul_f32 v170, %[sc], v170\n\tv_mul_f32 v171, %[sc], v171\n\tv_mul_f32 v172, %[sc], v172\n\tv_mul_f32 v173, %[sc], v173\n\tv_mul_f32 v174, %[sc], v174\n\t" \
        "v_mul_f32 v175, %[sc], v175\n\tv_cvt_pk_bf16_f32 v176, v168, v169\n\tv_cvt_pk_bf16_f32 v177, v170, v171\n\tv_cvt_pk_bf16_f32 v178, v172, v173\n\tv_cvt_pk_bf16_f32 v179, v174, v175\n\t" \
        "buffer_store_dwordx4 v[176:179], %[co], %[rc], %[sp] offen offset:1024 sc1\n\ts_branch 5f\n\t4:\ns_waitcnt vmcnt(4)\n\ts_branch 3b\n\t5:\n"
#define ASM_VOXA \
        "s_mov_b32 %[keep], m0\n\ts_mov_b32 %[n], 0\n\tv_add_u32 v180, 0x18000, %[xb]\n\tv_xor_b32 v182, 64, v180\n\tds_read_b128 v[128:131], v180 offset:0\n\t" \
        "ds_read_b128 v[132:135], v182 offset:0\n\tds_read_b128 v[136:139], v180 offset:1024\n\tds_read_b128 v[140:143], v182 offset:1024\n\t1:\ns_waitcnt lgkmcnt(3)\n\t" \
        "v_mfma_f32_16x16x32_bf16 v[160:163], v[0:3], v[128:131], 0\n\tds_read_b128 v[144:147], v180 offset:2048\n\tv_mfma_f32_16x16x32_bf16 v[164:167], v[64:67], v[128:131], 0\n\t" \
        "s_lshl_b32 %[im], %[n], 15\n\ts_and_b32 %[im], %[im], 0x18000\n\ts_waitcnt lgkmcnt(3)\n\tv_mfma_f32_16x16x32_bf16 v[160:163], v[4:7], v[132:135], v[160:163]\n\t" \
        "ds_read_b128 v[148:151], v182 offset:2048\n\tv_mfma_f32_16x16x32_bf16 v[164:167], v[68:71], v[132:135], v[164:167]\n\ts_xor_b32 %[t0], %[im], 0x10000\n\ts_add_u32 m0, %[t0], %[lb]\n\t" \
        "s_waitcnt lgkmcnt(3)\n\tv_mfma_f32_16x16x32_bf16 v[160:163], v[8:11], v[136:139], v[160:163]\n\tds_read_b128 v[152:155], v180 offset:3072\n\t" \
        "v_mfma_f32_16x16x32_bf16 v[164:167], v[72:75], v[136:139], v[164:167]\n\tv_cvt_pk_bf16_f32 v176, v168, v169\n\tv_cvt_pk_bf16_f32 v177, v170, v171\n\tv_cvt_pk_bf16_f32 v178, v172, v173\n\t" \
        "v_cvt_pk_bf16_f32 v179, v174, v175\n\ts_waitcnt lgkmcnt(3)\n\tv_mfma_f32_16x16x32_bf16 v[160:163], v[12:15], v[140:143], v[160:163]\n\tds_read_b128 v[156:159], v182 offset:3072\n\t" \
        "v_mfma_f32_16x16x32_bf16 v[164:167], v[76:79], v[140:143], v[164:167]\n\tds_write_b128 %[vw], v[176:179] offset:1024\n\tv_xor_b32 %[vw], 0x2000, %[vw]\n\ts_waitcnt lgkmcnt(4)\n\t" \
        "v_mfma_f32_16x16x32_bf16 v[160:163], v[16:19], v[144:147], v[160:163]\n\tds_read_b128 v[128:131], v180 offset:4096\n\t" \
        "v_mfma_f32_16x16x32_bf16 v[164:167], v[80:83], v[144:147], v[164:167]\n\ts_waitcnt lgkmcnt(4)\n\tv_mfma_f32_16x16x32_bf16 v[160:163], v[20:23], v[148:151], v[160:163]\n\t" \
        "ds_read_b128 v[132:135], v182 offset:4096\n\tv_mfma_f32_16x16x32_bf16 v[164:167], v[84:87], v[148:151], v[164:167]\n\ts_waitcnt lgkmcnt(2)\n\ts_waitcnt vmcnt(8)\n\ts_barrier\n\t" \
        "v_mfma_f32_16x16x32_bf16 v[160:163], v[24:27], v[152:155], v[160:163]\n\tds_read_b128 v[136:139], v180 offset:5120\n\t" \
        "v_mfma_f32_16x16x32_bf16 v[164:167], v[88:91], v[152:155], v[164:167]\n\tbuffer_load_dwordx4 %[dv], %[ra], %[so] offen lds\n\ts_add_u32 m0, m0, 896\n\t" \
        "v_mfma_f32_16x16x32_bf16 v[160:163], v[28:31], v[156:159], v[160:163]\n\tds_read_b128 v[140:143], v182 offset:5120\n\t" \
        "v_mfma_f32_16x16x32_bf16 v[164:167], v[92:95], v[156:159], v[164:167]\n\ts_waitcnt lgkmcnt(3)\n\tv_mfma_f32_16x16x32_bf16 v[160:163], v[32:35], v[128:131], v[160:163]\n\t" \
        "ds_read_b128 v[144:147], v180 offset:6144\n\tv_mfma_f32_16x16x32_bf16 v[164:167], v[96:99], v[128:131], v[164:167]\n\ts_waitcnt lgkmcnt(3)\n\t" \
        "v_mfma_f32_16x16x32_bf16 v[160:163], v[36:39], v[132:135], v[160:163]\n\tds_read_b128 v[148:151], v182 offset:6144\n\t" \
        "v_mfma_f32_16x16x32_bf16 v[164:167], v[100:103], v[132:135], v[164:167]\n\tbuffer_load_dwordx4 %[dv], %[ra], %[so] offen offset:128 lds\n\ts_add_u32 m0, m0, 896\n\ts_waitcnt lgkmcnt(3)\n\t" \
        "v_mfma_f32_16x16x32_bf16 v[160:163], v[40:43], v[136:139], v[160:163]\n\tds_read_b128 v[152:155], v180 offset:7168\n\t" \
        "v_mfma_f32_16x16x32_bf16 v[164:167], v[104:107], v[136:139], v[164:167]\n\ts_waitcnt lgkmcnt(3)\n\tv_mfma_f32_16x16x32_bf16 v[160:163], v[44:47], v[140:143], v[160:163]\n\t" \
        "ds_read_b128 v[156:159], v182 offset:7168\n\tv_mfma_f32_16x16x32_bf16 v[164:167], v[108:111], v[140:143], v[164:167]\n\ts_waitcnt lgkmcnt(3)\n\t" \
        "v_mfma_f32_16x16x32_bf16 v[160:163], v[48:51], v[144:147], v[160:163]\n\tds_read_b128 v[128:131], v180 offset:8192\n\t" \
        "v_mfma_f32_16x16x32_bf16 v[164:167], v[112:115], v[144:147], v[164:167]\n\tbuffer_load_dwordx4 %[dv], %[ra], %[so] offen offset:256 lds\n\ts_add_u32 m0, m0, 896\n\ts_waitcnt lgkmcnt(3)\n\t" \
        "v_mfma_f32_16x16x32_bf16 v[160:163], v[52:55], v[148:151], v[160:163]\n\tds_read_b128 v[132:135], v182 offset:8192\n\t" \
        "v_mfma_f32_16x16x32_bf16 v[164:167], v[116:119], v[148:151], v[164:167]\n\ts_waitcnt lgkmcnt(3)\n\tv_mfma_f32_16x16x32_bf16 v[160:163], v[56:59], v[152:155], v[160:163]\n\t" \
        "ds_read_b128 v[136:139], v180 offset:9216\n\tv_mfma_f32_16x16x32_bf16 v[164:167], v[120:123], v[152:155], v[164:167]\n\ts_waitcnt lgkmcnt(3)\n\t" \
        "v_mfma_f32_16x16x32_bf16 v[160:163], v[60:63], v[156:159], v[160:163]\n\tds_read_b128 v[140:143], v182 offset:9216\n\t" \
        "v_mfma_f32_16x16x32_bf16 v[164:167], v[124:127], v[156:159], v[164:167]\n\tbuffer_load_dwordx4 %[dv], %[ra], %[so] offen offset:384 lds\n\ts_add_u32 m0, m0, 896\n\ts_waitcnt lgkmcnt(3)\n\t" \
        "v_mfma_f32_16x16x32_bf16 v[168:171], v[0:3], v[128:131], 0\n\tds_read_b128 v[144:147], v180 offset:10240\n\tv_mfma_f32_16x16x32_bf16 v[172:175], v[64:67], v[128:131], 0\n\t" \
        "s_waitcnt lgkmcnt(3)\n\tv_mfma_f32_16x16x32_bf16 v[168:171], v[4:7], v[132:135], v[168:171]\n\tds_read_b128 v[148:151], v182 offset:10240\n\t" \
        "v_mfma_f32_16x16x32_bf16 v[172:175], v[68:71], v[132:135], v[172:175]\n\ts_waitcnt lgkmcnt(3)\n\tv_mfma_f32_16x16x32_bf16 v[168:171], v[8:11], v[136:139], v[168:171]\n\t" \
        "ds_read_b128 v[152:155], v180 offset:11264\n\tv_mfma_f32_16x16x32_bf16 v[172:175], v[72:75], v[136:139], v[172:175]\n\tbuffer_load_dwordx4 %[dv], %[ra], %[so] offen offset:512 lds\n\t" \
        "s_add_u32 m0, m0, 896\n\tv_cvt_pk_bf16_f32 v176, v160, v161\n\tv_cvt_pk_bf16_f32 v177, v162, v163\n\tv_cvt_pk_bf16_f32 v178, v164, v165\n\tv_cvt_pk_bf16_f32 v179, v166, v167\n\t" \
        "s_waitcnt lgkmcnt(3)\n\tv_mfma_f32_16x16x32_bf16 v[168:171], v[12:15], v[140:143], v[168:171]\n\tds_read_b128 v[156:159], v182 offset:11264\n\t" \
        "v_mfma_f32_16x16x32_bf16 v[172:175], v[76:79], v[140:143], v[172:175]\n\tds_write_b128 %[vw], v[176:179]\n\ts_waitcnt lgkmcnt(4)\n\t" \
        "v_mfma_f32_16x16x32_bf16 v[168:171], v[16:19], v[144:147], v[168:171]\n\tds_read_b128 v[128:131], v180 offset:12288\n\t" \
        "v_mfma_f32_16x16x32_bf16 v[172:175], v[80:83], v[144:147], v[172:175]\n\ts_waitcnt lgkmcnt(4)\n\tv_mfma_f32_16x16x32_bf16 v[168:171], v[20:23], v[148:151], v[168:171]\n\t" \
        "ds_read_b128 v[132:135], v182 offset:12288\n\tv_mfma_f32_16x16x32_bf16 v[172:175], v[84:87], v[148:151], v[172:175]\n\tbuffer_load_dwordx4 %[dv], %[ra], %[so] offen offset:640 lds\n\t" \
        "s_add_u32 m0, m0, 896\n\ts_waitcnt lgkmcnt(4)\n\tv_mfma_f32_16x16x32_bf16 v[168:171], v[24:27], v[152:155], v[168:171]\n\tds_read_b128 v[136:139], v180 offset:13312\n\t" \
        "v_mfma_f32_16x16x32_bf16 v[172:175], v[88:91], v[152:155], v[172:175]\n\ts_waitcnt lgkmcnt(4)\n\tv_mfma_f32_16x16x32_bf16 v[168:171], v[28:31], v[156:159], v[168:171]\n\t" \
        "ds_read_b128 v[140:143], v182 offset:13312\n\tv_mfma_f32_16x16x32_bf16 v[172:175], v[92:95], v[156:159], v[172:175]\n\ts_waitcnt lgkmcnt(3)\n\t" \
        "v_mfma_f32_16x16x32_bf16 v[168:171], v[32:35], v[128:131], v[168:171]\n\tds_read_b128 v[144:147], v180 offset:14336\n\t" \
        "v_mfma_f32_16x16x32_bf16 v[172:175], v[96:99], v[128:131], v[172:175]\n\tbuffer_load_dwordx4 %[dv], %[ra], %[so] offen offset:768 lds\n\ts_add_u32 m0, m0, 896\n\ts_waitcnt lgkmcnt(3)\n\t" \
        "v_mfma_f32_16x16x32_bf16 v[168:171], v[36:39], v[132:135], v[168:171]\n\tds_read_b128 v[148:151], v182 offset:14336\n\t" \
        "v_mfma_f32_16x16x32_bf16 v[172:175], v[100:103], v[132:135], v[172:175]\n\ts_waitcnt lgkmcnt(3)\n\tv_mfma_f32_16x16x32_bf16 v[168:171], v[40:43], v[136:139], v[168:171]\n\t" \
        "ds_read_b128 v[152:155], v180 offset:15360\n\tv_mfma_f32_16x16x32_bf16 v[172:175], v[104:107], v[136:139], v[172:175]\n\ts_waitcnt lgkmcnt(3)\n\t" \
        "v_mfma_f32_16x16x32_bf16 v[168:171], v[44:47], v[140:143], v[168:171]\n\tds_read_b128 v[156:159], v182 offset:15360\n\t" \
        "v_mfma_f32_16x16x32_bf16 v[172:175], v[108:111], v[140:143], v[172:175]\n\tbuffer_load_dwordx4 %[dv], %[ra], %[so] offen offset:896 lds\n\ts_add_u32 %[so], %[so], 0x8000\n\t" \
        "s_min_u32 %[so], %[so], 0x1f8000\n\tv_add_u32 v180, %[im], %[xb]\n\tv_xor_b32 v182, 64, v180\n\ts_waitcnt lgkmcnt(3)\n\t" \
        "v_mfma_f32_16x16x32_bf16 v[168:171], v[48:51], v[144:147], v[168:171]\n\tds_read_b128 v[128:131], v180 offset:0\n\tv_mfma_f32_16x16x32_bf16 v[172:175], v[112:115], v[144:147], v[172:175]\n\t" \
        "s_waitcnt lgkmcnt(3)\n\tv_mfma_f32_16x16x32_bf16 v[168:171], v[52:55], v[148:151], v[168:171]\n\tds_read_b128 v[132:135], v182 offset:0\n\t" \
        "v_mfma_f32_16x16x32_bf16 v[172:175], v[116:119], v[148:151], v[172:175]\n\ts_waitcnt lgkmcnt(3)\n\tv_mfma_f32_16x16x32_bf16 v[168:171], v[56:59], v[152:155], v[168:171]\n\t" \
        "ds_read_b128 v[136:139], v180 offset:1024\n\tv_mfma_f32_16x16x32_bf16 v[172:175], v[120:123], v[152:155], v[172:175]\n\ts_add_u32 %[n], %[n], 1\n\ts_cmp_lt_u32 %[n], 64\n\t" \
        "s_waitcnt lgkmcnt(3)\n\tv_mfma_f32_16x16x32_bf16 v[168:171], v[60:63], v[156:159], v[168:171]\n\tds_read_b128 v[140:143], v182 offset:1024\n\t" \
        "v_mfma_f32_16x16x32_bf16 v[172:175], v[124:127], v[156:159], v[172:175]\n\ts_cbranch_scc1 1b\n\ts_mov_b32 m0, %[keep]\n\ts_nop 7\n\tv_cvt_pk_bf16_f32 v176, v168, v169\n\t" \
        "v_cvt_pk_bf16_f32 v177, v170, v171\n\tv_cvt_pk_bf16_f32 v178, v172, v173\n\tv_cvt_pk_bf16_f32 v179, v174, v175\n\tds_write_b128 %[vw], v[176:179] offset:1024\n\ts_waitcnt lgkmcnt(0)\n\t" \
        "s_barrier\n\t"
#define ASM_VOXB \
        "s_mov_b32 %[n], 1\n\ts_mov_b32 %[t1], 0\n\tv_mov_b32 v185, 1\n\ts_barrier\n\t1:\ns_barrier\n\tds_read_b128 v[128:131], %[vi] offset:0\n\tds_read_b128 v[132:135], %[vi] offset:1024\n\t" \
        "ds_read_b128 v[136:139], %[vi] offset:2048\n\tds_read_b128 v[140:143], %[vi] offset:3072\n\tds_read_b128 v[144:147], %[vi] offset:4096\n\tds_read_b128 v[148:151], %[vi] offset:5120\n\t" \
        "ds_read_b128 v[152:155], %[vi] offset:6144\n\tds_read_b128 v[156:159], %[vi] offset:7168\n\ts_lshl_b32 %[t2], %[cnt], 7\n\tv_mov_b32 v184, %[t2]\n\ts_cmp_lt_u32 %[cnt], %[t1]\n\t" \
        "s_cselect_b64 exec, %[em], 0\n\tglobal_atomic_add v184, v185, %[dn]\n\ts_mov_b64 exec, -1\n\ts_addc_u32 %[cnt], %[cnt], 0\n\tv_xor_b32 %[vi], 0x2000, %[vi]\n\ts_waitcnt lgkmcnt(7)\n\t" \
        "v_mfma_f32_16x16x32_bf16 v[160:163], v[128:131], v[0:3], 0\n\ts_waitcnt lgkmcnt(6)\n\tv_mfma_f32_16x16x32_bf16 v[164:167], v[132:135], v[0:3], 0\n\ts_waitcnt lgkmcnt(5)\n\t" \
        "v_mfma_f32_16x16x32_bf16 v[160:163], v[136:139], v[4:7], v[160:163]\n\ts_waitcnt lgkmcnt(4)\n\tv_mfma_f32_16x16x32_bf16 v[164:167], v[140:143], v[4:7], v[164:167]\n\ts_waitcnt lgkmcnt(3)\n\t" \
        "v_mfma_f32_16x16x32_bf16 v[160:163], v[144:147], v[8:11], v[160:163]\n\ts_waitcnt lgkmcnt(2)\n\tv_mfma_f32_16x16x32_bf16 v[164:167], v[148:151], v[8:11], v[164:167]\n\t" \
        "s_waitcnt lgkmcnt(1)\n\tv_mfma_f32_16x16x32_bf16 v[160:163], v[152:155], v[12:15], v[160:163]\n\ts_waitcnt lgkmcnt(0)\n\t" \
        "v_mfma_f32_16x16x32_bf16 v[164:167], v[156:159], v[12:15], v[164:167]\n\tv_mfma_f32_16x16x32_bf16 v[168:171], v[128:131], v[16:19], 0\n\ts_nop 1\n\t" \
        "v_mfma_f32_16x16x32_bf16 v[172:175], v[132:135], v[16:19], 0\n\ts_nop 1\n\tv_mfma_f32_16x16x32_bf16 v[168:171], v[136:139], v[20:23], v[168:171]\n\ts_nop 1\n\t" \
        "v_mfma_f32_16x16x32_bf16 v[172:175], v[140:143], v[20:23], v[172:175]\n\tv_cvt_pk_bf16_f32 v176, v160, v161\n\tv_cvt_pk_bf16_f32 v177, v162, v163\n\t" \
        "v_mfma_f32_16x16x32_bf16 v[168:171], v[144:147], v[24:27], v[168:171]\n\tv_cvt_pk_bf16_f32 v178, v164, v165\n\tv_cvt_pk_bf16_f32 v179, v166, v167\n\t" \
        "v_mfma_f32_16x16x32_bf16 v[172:175], v[148:151], v[24:27], v[172:175]\n\tbuffer_store_dwordx4 v[176:179], %[st], %[rw], %[sp] offen sc1\n\ts_add_u32 %[sp], %[sp], 32768\n\t" \
        "v_mfma_f32_16x16x32_bf16 v[168:171], v[152:155], v[28:31], v[168:171]\n\tv_mfma_f32_16x16x32_bf16 v[172:175], v[156:159], v[28:31], v[172:175]\n\t" \
        "v_mfma_f32_16x16x32_bf16 v[160:163], v[128:131], v[32:35], 0\n\ts_nop 1\n\tv_mfma_f32_16x16x32_bf16 v[164:167], v[132:135], v[32:35], 0\n\ts_nop 1\n\t" \
        "v_mfma_f32_16x16x32_bf16 v[160:163], v[136:139], v[36:39], v[160:163]\n\ts_nop 1\n\tv_mfma_f32_16x16x32_bf16 v[164:167], v[140:143], v[36:39], v[164:167]\n\t" \
        "v_cvt_pk_bf16_f32 v180, v168, v169\n\tv_cvt_pk_bf16_f32 v181, v170, v171\n\tv_mfma_f32_16x16x32_bf16 v[160:163], v[144:147], v[40:43], v[160:163]\n\tv_cvt_pk_bf16_f32 v182, v172, v173\n\t" \
        "v_cvt_pk_bf16_f32 v183, v174, v175\n\tv_mfma_f32_16x16x32_bf16 v[164:167], v[148:151], v[40:43], v[164:167]\n\tbuffer_store_dwordx4 v[180:183], %[st], %[rw], %[sp] offen sc1\n\t" \
        "s_add_u32 %[sp], %[sp], 32768\n\tv_mfma_f32_16x16x32_bf16 v[160:163], v[152:155], v[44:47], v[160:163]\n\tv_mfma_f32_16x16x32_bf16 v[164:167], v[156:159], v[44:47], v[164:167]\n\t" \
        "v_mfma_f32_16x16x32_bf16 v[168:171], v[128:131], v[48:51], 0\n\ts_nop 1\n\tv_mfma_f32_16x16x32_bf16 v[172:175], v[132:135], v[48:51], 0\n\ts_nop 1\n\t" \
        "v_mfma_f32_16x16x32_bf16 v[168:171], v[136:139], v[52:55], v[168:171]\n\ts_nop 1\n\tv_mfma_f32_16x16x32_bf16 v[172:175], v[140:143], v[52:55], v[172:175]\n\t" \
        "v_cvt_pk_bf16_f32 v176, v160, v161\n\tv_cvt_pk_bf16_f32 v177, v162, v163\n\tv_mfma_f32_16x16x32_bf16 v[168:171], v[144:147], v[56:59], v[168:171]\n\tv_cvt_pk_bf16_f32 v178, v164, v165\n\t" \
        "v_cvt_pk_bf16_f32 v179, v166, v167\n\tv_mfma_f32_16x16x32_bf16 v[172:175], v[148:151], v[56:59], v[172:175]\n\tbuffer_store_dwordx4 v[176:179], %[st], %[rw], %[sp] offen sc1\n\t" \
        "s_add_u32 %[sp], %[sp], 32768\n\tv_mfma_f32_16x16x32_bf16 v[168:171], v[152:155], v[60:63], v[168:171]\n\tv_mfma_f32_16x16x32_bf16 v[172:175], v[156:159], v[60:63], v[172:175]\n\t" \
        "v_mfma_f32_16x16x32_bf16 v[160:163], v[128:131], v[64:67], 0\n\ts_nop 1\n\tv_mfma_f32_16x16x32_bf16 v[164:167], v[132:135], v[64:67], 0\n\ts_nop 1\n\t" \
        "v_mfma_f32_16x16x32_bf16 v[160:163], v[136:139], v[68:71], v[160:163]\n\ts_nop 1\n\tv_mfma_f32_16x16x32_bf16 v[164:167], v[140:143], v[68:71], v[164:167]\n\t" \
        "v_cvt_pk_bf16_f32 v180, v168, v169\n\tv_cvt_pk_bf16_f32 v181, v170, v171\n\tv_mfma_f32_16x16x32_bf16 v[160:163], v[144:147], v[72:75], v[160:163]\n\tv_cvt_pk_bf16_f32 v182, v172, v173\n\t" \
        "v_cvt_pk_bf16_f32 v183, v174, v175\n\tv_mfma_f32_16x16x32_bf16 v[164:167], v[148:151], v[72:75], v[164:167]\n\tbuffer_store_dwordx4 v[180:183], %[st], %[rw], %[sp] offen sc1\n\t" \
        "s_add_u32 %[sp], %[sp], 32768\n\tv_mfma_f32_16x16x32_bf16 v[160:163], v[152:155], v[76:79], v[160:163]\n\tv_mfma_f32_16x16x32_bf16 v[164:167], v[156:159], v[76:79], v[164:167]\n\t" \
        "v_mfma_f32_16x16x32_bf16 v[168:171], v[128:131], v[80:83], 0\n\ts_nop 1\n\tv_mfma_f32_16x16x32_bf16 v[172:175], v[132:135], v[80:83], 0\n\ts_nop 1\n\t" \
        "v_mfma_f32_16x16x32_bf16 v[168:171], v[136:139], v[84:87], v[168:171]\n\ts_nop 1\n\tv_mfma_f32_16x16x32_bf16 v[172:175], v[140:143], v[84:87], v[172:175]\n\t" \
        "v_cvt_pk_bf16_f32 v176, v160, v161\n\tv_cvt_pk_bf16_f32 v177, v162, v163\n\tv_mfma_f32_16x16x32_bf16 v[168:171], v[144:147], v[88:91], v[168:171]\n\tv_cvt_pk_bf16_f32 v178, v164, v165\n\t" \
        "v_cvt_pk_bf16_f32 v179, v166, v167\n\tv_mfma_f32_16x16x32_bf16 v[172:175], v[148:151], v[88:91], v[172:175]\n\tbuffer_store_dwordx4 v[176:179], %[st], %[rw], %[sp] offen sc1\n\t" \
        "s_add_u32 %[sp], %[sp], 32768\n\tv_mfma_f32_16x16x32_bf16 v[168:171], v[152:155], v[92:95], v[168:171]\n\tv_mfma_f32_16x16x32_bf16 v[172:175], v[156:159], v[92:95], v[172:175]\n\t" \
        "v_mfma_f32_16x16x32_bf16 v[160:163], v[128:131], v[96:99], 0\n\ts_nop 1\n\tv_mfma_f32_16x16x32_bf16 v[164:167], v[132:135], v[96:99], 0\n\ts_nop 1\n\t" \
        "v_mfma_f32_16x16x32_bf16 v[160:163], v[136:139], v[100:103], v[160:163]\n\ts_nop 1\n\tv_mfma_f32_16x16x32_bf16 v[164:167], v[140:143], v[100:103], v[164:167]\n\t" \
        "v_cvt_pk_bf16_f32 v180, v168, v169\n\tv_cvt_pk_bf16_f32 v181, v170, v171\n\tv_mfma_f32_16x16x32_bf16 v[160:163], v[144:147], v[104:107], v[160:163]\n\tv_cvt_pk_bf16_f32 v182, v172, v173\n\t" \
        "v_cvt_pk_bf16_f32 v183, v174, v175\n\tv_mfma_f32_16x16x32_bf16 v[164:167], v[148:151], v[104:107], v[164:167]\n\tbuffer_store_dwordx4 v[180:183], %[st], %[rw], %[sp] offen sc1\n\t" \
        "s_add_u32 %[sp], %[sp], 32768\n\tv_mfma_f32_16x16x32_bf16 v[160:163], v[152:155], v[108:111], v[160:163]\n\tv_mfma_f32_16x16x32_bf16 v[164:167], v[156:159], v[108:111], v[164:167]\n\t" \
        "v_mfma_f32_16x16x32_bf16 v[168:171], v[128:131], v[112:115], 0\n\ts_nop 1\n\tv_mfma_f32_16x16x32_bf16 v[172:175], v[132:135], v[112:115], 0\n\ts_nop 1\n\t" \
        "v_mfma_f32_16x16x32_bf16 v[168:171], v[136:139], v[116:119], v[168:171]\n\ts_nop 1\n\tv_mfma_f32_16x16x32_bf16 v[172:175], v[140:143], v[116:119], v[172:175]\n\t" \
        "v_cvt_pk_bf16_f32 v176, v160, v161\n\tv_cvt_pk_bf16_f32 v177, v162, v163\n\tv_mfma_f32_16x16x32_bf16 v[168:171], v[144:147], v[120:123], v[168:171]\n\tv_cvt_pk_bf16_f32 v178, v164, v165\n\t" \
        "v_cvt_pk_bf16_f32 v179, v166, v167\n\tv_mfma_f32_16x16x32_bf16 v[172:175], v[148:151], v[120:123], v[172:175]\n\tbuffer_store_dwordx4 v[176:179], %[st], %[rw], %[sp] offen sc1\n\t" \
        "s_add_u32 %[sp], %[sp], 32768\n\tv_mfma_f32_16x16x32_bf16 v[168:171], v[152:155], v[124:127], v[168:171]\n\tv_mfma_f32_16x16x32_bf16 v[172:175], v[156:159], v[124:127], v[172:175]\n\t" \
        "s_mov_b32 %[t1], %[n]\n\ts_add_u32 %[n], %[n], 1\n\ts_nop 5\n\tv_cvt_pk_bf16_f32 v180, v168, v169\n\tv_cvt_pk_bf16_f32 v181, v170, v171\n\tv_cvt_pk_bf16_f32 v182, v172, v173\n\t" \
        "v_cvt_pk_bf16_f32 v183, v174, v175\n\tbuffer_store_dwordx4 v[180:183], %[st], %[rw], %[sp] offen sc1\n\ts_add_u32 %[sp], %[sp], 819200\n\ts_cmp_le_u32 %[n], 8\n\ts_cbranch_scc1 2f\n\t" \
        "s_sub_u32 %[t1], %[t1], 1\n\ts_waitcnt vmcnt(8)\n\ts_branch 3f\n\t2:\ns_waitcnt vmcnt(0)\n\t3:\ns_cmp_le_u32 %[n], 64\n\ts_cbranch_scc1 1b\n\t"
#define ASM_OPROJ \
        "ds_read_b128 v[128:131], %[xa] offset:0\n\tds_read_b128 v[132:135], %[xa] offset:1024\n\tds_read_b64 v[168:169], %[fr]\n\tds_read_b128 v[136:139], %[xa] offset:2048\n\t" \
        "ds_read_b128 v[140:143], %[xa] offset:3072\n\tds_read_b128 v[144:147], %[xa] offset:4096\n\tds_read_b128 v[148:151], %[xa] offset:5120\n\ts_mov_b32 %[keep], m0\n\ts_mov_b64 vcc, %[em]\n\t" \
        "s_cbranch_vccz 1f\n\tv_mov_b32 v172, 1\n\tv_mov_b32 v173, 0\n\tv_mov_b32 v174, %[dr]\n\ts_cmp_lt_i32 %[dr], 0\n\ts_cselect_b64 exec, 0, 1\n\tglobal_atomic_add v174, v172, %[fin]\n\t" \
        "s_mov_b64 exec, 1\n\tglobal_atomic_add v170, v173, v172, %[qu] sc0\n\tglobal_load_dword v171, v173, %[li] sc1\n\ts_mov_b64 exec, -1\n\t1:\ns_waitcnt lgkmcnt(6)\n\t" \
        "v_mfma_f32_16x16x32_bf16 v[160:163], v[0:3], v[128:131], 0\n\tds_read_b128 v[152:155], %[xa] offset:6144\n\tds_read_b128 v[156:159], %[xa] offset:7168\n\ts_waitcnt lgkmcnt(7)\n\t" \
        "v_mfma_f32_16x16x32_bf16 v[164:167], v[0:3], v[132:135], 0\n\ts_waitcnt lgkmcnt(5)\n\tv_mfma_f32_16x16x32_bf16 v[160:163], v[4:7], v[136:139], v[160:163]\n\t" \
        "ds_read_b128 v[128:131], %[xa] offset:8192\n\tds_read_b128 v[132:135], %[xa] offset:9216\n\ts_waitcnt lgkmcnt(6)\n\tv_mfma_f32_16x16x32_bf16 v[164:167], v[4:7], v[140:143], v[164:167]\n\t" \
        "v_readfirstlane_b32 %[pos], v168\n\tv_readfirstlane_b32 %[e], v169\n\ts_mov_b32 m0, %[lb]\n\ts_waitcnt lgkmcnt(5)\n\tv_mfma_f32_16x16x32_bf16 v[160:163], v[8:11], v[144:147], v[160:163]\n\t" \
        "ds_read_b128 v[136:139], %[xa] offset:10240\n\tds_read_b128 v[140:143], %[xa] offset:11264\n\ts_waitcnt lgkmcnt(6)\n\tv_mfma_f32_16x16x32_bf16 v[164:167], v[8:11], v[148:151], v[164:167]\n\t" \
        "s_lshr_b32 %[nx], %[pos], 3\n\ts_lshl_b32 %[nx], %[nx], 2\n\ts_lshr_b32 %[e], %[e], %[nx]\n\ts_and_b32 %[e], %[e], 15\n\ts_cmp_lt_u32 %[pos], 64\n\ts_cselect_b32 %[e], %[e], 0\n\t" \
        "s_lshl_b32 %[nx], %[e], 3\n\ts_and_b32 %[so], %[pos], 7\n\ts_add_u32 %[nx], %[nx], %[so]\n\ts_sub_u32 %[nx], %[nx], 8\n\ts_cselect_b32 %[nx], %[ch], %[nx]\n\ts_lshl_b32 %[so], %[nx], 18\n\t" \
        "s_add_u32 %[so], %[so], %[sb]\n\ts_waitcnt lgkmcnt(5)\n\tv_mfma_f32_16x16x32_bf16 v[160:163], v[12:15], v[152:155], v[160:163]\n\tds_read_b128 v[144:147], %[xa] offset:12288\n\t" \
        "ds_read_b128 v[148:151], %[xa] offset:13312\n\ts_waitcnt lgkmcnt(6)\n\tv_mfma_f32_16x16x32_bf16 v[164:167], v[12:15], v[156:159], v[164:167]\n\t" \
        "buffer_load_dwordx4 %[dv], %[ra], %[so] offen lds\n\ts_add_u32 m0, m0, 0x2000\n\ts_add_u32 %[so], %[so], 0x2000\n\ts_waitcnt lgkmcnt(5)\n\t" \
        "v_mfma_f32_16x16x32_bf16 v[160:163], v[16:19], v[128:131], v[160:163]\n\tds_read_b128 v[152:155], %[xa] offset:14336\n\tds_read_b128 v[156:159], %[xa] offset:15360\n\t" \
        "s_waitcnt lgkmcnt(6)\n\tv_mfma_f32_16x16x32_bf16 v[164:167], v[16:19], v[132:135], v[164:167]\n\ts_waitcnt lgkmcnt(5)\n\t" \
        "v_mfma_f32_16x16x32_bf16 v[160:163], v[20:23], v[136:139], v[160:163]\n\tds_read_b128 v[128:131], %[xa] offset:16384\n\tds_read_b128 v[132:135], %[xa] offset:17408\n\t" \
        "s_waitcnt lgkmcnt(6)\n\tv_mfma_f32_16x16x32_bf16 v[164:167], v[20:23], v[140:143], v[164:167]\n\ts_waitcnt lgkmcnt(5)\n\t" \
        "v_mfma_f32_16x16x32_bf16 v[160:163], v[24:27], v[144:147], v[160:163]\n\tds_read_b128 v[136:139], %[xa] offset:18432\n\tds_read_b128 v[140:143], %[xa] offset:19456\n\t" \
        "s_waitcnt lgkmcnt(6)\n\tv_mfma_f32_16x16x32_bf16 v[164:167], v[24:27], v[148:151], v[164:167]\n\tbuffer_load_dwordx4 %[dv], %[ra], %[so] offen lds\n\ts_add_u32 m0, m0, 0x2000\n\t" \
        "s_add_u32 %[so], %[so], 0x2000\n\ts_waitcnt lgkmcnt(5)\n\tv_mfma_f32_16x16x32_bf16 v[160:163], v[28:31], v[152:155], v[160:163]\n\tds_read_b128 v[144:147], %[xa] offset:20480\n\t" \
        "ds_read_b128 v[148:151], %[xa] offset:21504\n\ts_waitcnt lgkmcnt(6)\n\tv_mfma_f32_16x16x32_bf16 v[164:167], v[28:31], v[156:159], v[164:167]\n\ts_waitcnt lgkmcnt(5)\n\t" \
        "v_mfma_f32_16x16x32_bf16 v[160:163], v[32:35], v[128:131], v[160:163]\n\tds_read_b128 v[152:155], %[xa] offset:22528\n\tds_read_b128 v[156:159], %[xa] offset:23552\n\t" \
        "s_waitcnt lgkmcnt(6)\n\tv_mfma_f32_16x16x32_bf16 v[164:167], v[32:35], v[132:135], v[164:167]\n\ts_waitcnt lgkmcnt(5)\n\t" \
        "v_mfma_f32_16x16x32_bf16 v[160:163], v[36:39], v[136:139], v[160:163]\n\tds_read_b128 v[128:131], %[xa] offset:24576\n\tds_read_b128 v[132:135], %[xa] offset:25600\n\t" \
        "s_waitcnt lgkmcnt(6)\n\tv_mfma_f32_16x16x32_bf16 v[164:167], v[36:39], v[140:143], v[164:167]\n\tbuffer_load_dwordx4 %[dv], %[ra], %[so] offen lds\n\ts_add_u32 m0, m0, 0x2000\n\t" \
        "s_add_u32 %[so], %[so], 0x2000\n\ts_waitcnt lgkmcnt(5)\n\tv_mfma_f32_16x16x32_bf16 v[160:163], v[40:43], v[144:147], v[160:163]\n\tds_read_b128 v[136:139], %[xa] offset:26624\n\t" \
        "ds_read_b128 v[140:143], %[xa] offset:27648\n\ts_waitcnt lgkmcnt(6)\n\tv_mfma_f32_16x16x32_bf16 v[164:167], v[40:43], v[148:151], v[164:167]\n\ts_waitcnt lgkmcnt(5)\n\t" \
        "v_mfma_f32_16x16x32_bf16 v[160:163], v[44:47], v[152:155], v[160:163]\n\tds_read_b128 v[144:147], %[xa] offset:28672\n\tds_read_b128 v[148:151], %[xa] offset:29696\n\t" \
        "s_waitcnt lgkmcnt(6)\n\tv_mfma_f32_16x16x32_bf16 v[164:167], v[44:47], v[156:159], v[164:167]\n\ts_waitcnt lgkmcnt(5)\n\t" \
        "v_mfma_f32_16x16x32_bf16 v[160:163], v[48:51], v[128:131], v[160:163]\n\tds_read_b128 v[152:155], %[xa] offset:30720\n\tds_read_b128 v[156:159], %[xa] offset:31744\n\t" \
        "s_waitcnt lgkmcnt(6)\n\tv_mfma_f32_16x16x32_bf16 v[164:167], v[48:51], v[132:135], v[164:167]\n\tbuffer_load_dwordx4 %[dv], %[ra], %[so] offen lds\n\ts_add_u32 m0, m0, 0x2000\n\t" \
        "s_add_u32 %[so], %[so], 0x2000\n\ts_waitcnt lgkmcnt(5)\n\tv_mfma_f32_16x16x32_bf16 v[160:163], v[52:55], v[136:139], v[160:163]\n\tds_read_b128 v[128:131], %[xa] offset:32768\n\t" \
        "ds_read_b128 v[132:135], %[xa] offset:33792\n\ts_waitcnt lgkmcnt(6)\n\tv_mfma_f32_16x16x32_bf16 v[164:167], v[52:55], v[140:143], v[164:167]\n\ts_waitcnt lgkmcnt(5)\n\t" \
        "v_mfma_f32_16x16x32_bf16 v[160:163], v[56:59], v[144:147], v[160:163]\n\tds_read_b128 v[136:139], %[xa] offset:34816\n\tds_read_b128 v[140:143], %[xa] offset:35840\n\t" \
        "s_waitcnt lgkmcnt(6)\n\tv_mfma_f32_16x16x32_bf16 v[164:167], v[56:59], v[148:151], v[164:167]\n\ts_waitcnt lgkmcnt(5)\n\t" \
        "v_mfma_f32_16x16x32_bf16 v[160:163], v[60:63], v[152:155], v[160:163]\n\tds_read_b128 v[144:147], %[xa] offset:36864\n\tds_read_b128 v[148:151], %[xa] offset:37888\n\t" \
        "s_waitcnt lgkmcnt(6)\n\tv_mfma_f32_16x16x32_bf16 v[164:167], v[60:63], v[156:159], v[164:167]\n\tbuffer_load_dwordx4 %[dv], %[ra], %[so] offen lds\n\ts_add_u32 m0, m0, 0x2000\n\t" \
        "s_add_u32 %[so], %[so], 0x2000\n\ts_waitcnt lgkmcnt(5)\n\tv_mfma_f32_16x16x32_bf16 v[160:163], v[64:67], v[128:131], v[160:163]\n\tds_read_b128 v[152:155], %[xa] offset:38912\n\t" \
        "ds_read_b128 v[156:159], %[xa] offset:39936\n\ts_waitcnt lgkmcnt(6)\n\tv_mfma_f32_16x16x32_bf16 v[164:167], v[64:67], v[132:135], v[164:167]\n\ts_waitcnt lgkmcnt(5)\n\t" \
        "v_mfma_f32_16x16x32_bf16 v[160:163], v[68:71], v[136:139], v[160:163]\n\tds_read_b128 v[128:131], %[xa] offset:40960\n\tds_read_b128 v[132:135], %[xa] offset:41984\n\t" \
        "s_waitcnt lgkmcnt(6)\n\tv_mfma_f32_16x16x32_bf16 v[164:167], v[68:71], v[140:143], v[164:167]\n\ts_waitcnt lgkmcnt(5)\n\t" \
        "v_mfma_f32_16x16x32_bf16 v[160:163], v[72:75], v[144:147], v[160:163]\n\tds_read_b128 v[136:139], %[xa] offset:43008\n\tds_read_b128 v[140:143], %[xa] offset:44032\n\t" \
        "s_waitcnt lgkmcnt(6)\n\tv_mfma_f32_16x16x32_bf16 v[164:167], v[72:75], v[148:151], v[164:167]\n\tbuffer_load_dwordx4 %[dv], %[ra], %[so] offen lds\n\ts_add_u32 m0, m0, 0x2000\n\t" \
        "s_add_u32 %[so], %[so], 0x2000\n\ts_waitcnt lgkmcnt(5)\n\tv_mfma_f32_16x16x32_bf16 v[160:163], v[76:79], v[152:155], v[160:163]\n\tds_read_b128 v[144:147], %[xa] offset:45056\n\t" \
        "ds_read_b128 v[148:151], %[xa] offset:46080\n\ts_waitcnt lgkmcnt(6)\n\tv_mfma_f32_16x16x32_bf16 v[164:167], v[76:79], v[156:159], v[164:167]\n\ts_waitcnt lgkmcnt(5)\n\t" \
        "v_mfma_f32_16x16x32_bf16 v[160:163], v[80:83], v[128:131], v[160:163]\n\tds_read_b128 v[152:155], %[xa] offset:47104\n\tds_read_b128 v[156:159], %[xa] offset:48128\n\t" \
        "s_waitcnt lgkmcnt(6)\n\tv_mfma_f32_16x16x32_bf16 v[164:167], v[80:83], v[132:135], v[164:167]\n\ts_waitcnt lgkmcnt(5)\n\t" \
        "v_mfma_f32_16x16x32_bf16 v[160:163], v[84:87], v[136:139], v[160:163]\n\tds_read_b128 v[128:131], %[xa] offset:49152\n\tds_read_b128 v[132:135], %[xa] offset:50176\n\t" \
        "s_waitcnt lgkmcnt(6)\n\tv_mfma_f32_16x16x32_bf16 v[164:167], v[84:87], v[140:143], v[164:167]\n\ts_cmp_lt_u32 %[kq], 3\n\ts_cbranch_scc1 2f\n\t" \
        "buffer_load_dwordx4 %[dv], %[ra], %[so] offen lds\n\t2:\ns_cbranch_vccz 4f\n\ts_waitcnt vmcnt(6)\n\ts_mov_b64 exec, 1\n\tds_write_b64 %[fw], v[170:171]\n\ts_mov_b64 exec, -1\n\t" \
        "s_waitcnt lgkmcnt(0)\n\t4:\ns_waitcnt lgkmcnt(5)\n\tv_mfma_f32_16x16x32_bf16 v[160:163], v[88:91], v[144:147], v[160:163]\n\tds_read_b128 v[136:139], %[xa] offset:51200\n\t" \
        "ds_read_b128 v[140:143], %[xa] offset:52224\n\ts_waitcnt lgkmcnt(6)\n\tv_mfma_f32_16x16x32_bf16 v[164:167], v[88:91], v[148:151], v[164:167]\n\ts_waitcnt lgkmcnt(5)\n\t" \
        "v_mfma_f32_16x16x32_bf16 v[160:163], v[92:95], v[152:155], v[160:163]\n\tds_read_b128 v[144:147], %[xa] offset:53248\n\tds_read_b128 v[148:151], %[xa] offset:54272\n\t" \
        "s_waitcnt lgkmcnt(6)\n\tv_mfma_f32_16x16x32_bf16 v[164:167], v[92:95], v[156:159], v[164:167]\n\ts_lshl_b32 %[so], %[ch], 16\n\ts_cmp_lt_u32 %[kq], 3\n\ts_cbranch_scc1 3f\n\t" \
        "s_waitcnt lgkmcnt(5)\n\tv_mfma_f32_16x16x32_bf16 v[160:163], v[96:99], v[128:131], v[160:163]\n\tds_read_b128 v[152:155], %[xa] offset:55296\n\t" \
        "ds_read_b128 v[156:159], %[xa] offset:56320\n\ts_waitcnt lgkmcnt(6)\n\tv_mfma_f32_16x16x32_bf16 v[164:167], v[96:99], v[132:135], v[164:167]\n\ts_waitcnt lgkmcnt(5)\n\t" \
        "v_mfma_f32_16x16x32_bf16 v[160:163], v[100:103], v[136:139], v[160:163]\n\ts_waitcnt lgkmcnt(4)\n\tv_mfma_f32_16x16x32_bf16 v[164:167], v[100:103], v[140:143], v[164:167]\n\t" \
        "s_waitcnt lgkmcnt(3)\n\tv_mfma_f32_16x16x32_bf16 v[160:163], v[104:107], v[144:147], v[160:163]\n\ts_waitcnt lgkmcnt(2)\n\t" \
        "v_mfma_f32_16x16x32_bf16 v[164:167], v[104:107], v[148:151], v[164:167]\n\ts_waitcnt lgkmcnt(1)\n\tv_mfma_f32_16x16x32_bf16 v[160:163], v[108:111], v[152:155], v[160:163]\n\t" \
        "s_waitcnt lgkmcnt(0)\n\tv_mfma_f32_16x16x32_bf16 v[164:167], v[108:111], v[156:159], v[164:167]\n\t3:\ns_mov_b32 m0, %[keep]\n\ts_nop 6\n\t" \
        "buffer_store_dwordx4 v[160:163], %[co], %[rc], %[so] offen sc1\n\tbuffer_store_dwordx4 v[164:167], %[co], %[rc], %[so] offen offset:1024 sc1\n\ts_waitcnt vmcnt(2) lgkmcnt(0)\n\t"
#define ASM_OPRE \
        "s_waitcnt vmcnt(8)\n\ts_mov_b32 %[keep], m0\n\tv_mov_b32 v172, 2\n\tv_mov_b32 v173, 0\n\ts_mov_b64 exec, %[em]\n\tglobal_atomic_add v168, v173, v172, %[qu] sc0\n\t" \
        "global_load_dword v169, v173, %[li] sc1\n\ts_mov_b64 exec, -1\n\ts_mov_b32 %[so], %[wb]\n\tbuffer_load_dwordx4 v[0:3], %[dv], %[rw], %[so] offen\n\t" \
        "buffer_load_dwordx4 v[4:7], %[dv], %[rw], %[so] offen offset:1024\n\tbuffer_load_dwordx4 v[8:11], %[dv], %[rw], %[so] offen offset:2048\n\t" \
        "buffer_load_dwordx4 v[12:15], %[dv], %[rw], %[so] offen offset:3072\n\ts_add_u32 %[so], %[so], 0x1000\n\tbuffer_load_dwordx4 v[16:19], %[dv], %[rw], %[so] offen\n\t" \
        "buffer_load_dwordx4 v[20:23], %[dv], %[rw], %[so] offen offset:1024\n\tbuffer_load_dwordx4 v[24:27], %[dv], %[rw], %[so] offen offset:2048\n\t" \
        "buffer_load_dwordx4 v[28:31], %[dv], %[rw], %[so] offen offset:3072\n\ts_add_u32 %[so], %[so], 0x1000\n\tbuffer_load_dwordx4 v[32:35], %[dv], %[rw], %[so] offen\n\t" \
        "buffer_load_dwordx4 v[36:39], %[dv], %[rw], %[so] offen offset:1024\n\tbuffer_load_dwordx4 v[40:43], %[dv], %[rw], %[so] offen offset:2048\n\t" \
        "buffer_load_dwordx4 v[44:47], %[dv], %[rw], %[so] offen offset:3072\n\ts_add_u32 %[so], %[so], 0x1000\n\tbuffer_load_dwordx4 v[48:51], %[dv], %[rw], %[so] offen\n\t" \
        "buffer_load_dwordx4 v[52:55], %[dv], %[rw], %[so] offen offset:1024\n\tbuffer_load_dwordx4 v[56:59], %[dv], %[rw], %[so] offen offset:2048\n\t" \
        "buffer_load_dwordx4 v[60:63], %[dv], %[rw], %[so] offen offset:3072\n\ts_add_u32 %[so], %[so], 0x1000\n\tbuffer_load_dwordx4 v[64:67], %[dv], %[rw], %[so] offen\n\t" \
        "buffer_load_dwordx4 v[68:71], %[dv], %[rw], %[so] offen offset:1024\n\tbuffer_load_dwordx4 v[72:75], %[dv], %[rw], %[so] offen offset:2048\n\t" \
        "buffer_load_dwordx4 v[76:79], %[dv], %[rw], %[so] offen offset:3072\n\ts_add_u32 %[so], %[so], 0x1000\n\tbuffer_load_dwordx4 v[80:83], %[dv], %[rw], %[so] offen\n\t" \
        "buffer_load_dwordx4 v[84:87], %[dv], %[rw], %[so] offen offset:1024\n\tbuffer_load_dwordx4 v[88:91], %[dv], %[rw], %[so] offen offset:2048\n\t" \
        "buffer_load_dwordx4 v[92:95], %[dv], %[rw], %[so] offen offset:3072\n\ts_add_u32 %[so], %[so], 0x1000\n\tbuffer_load_dwordx4 v[96:99], %[dv], %[rw], %[so] offen\n\t" \
        "buffer_load_dwordx4 v[100:103], %[dv], %[rw], %[so] offen offset:1024\n\tbuffer_load_dwordx4 v[104:107], %[dv], %[rw], %[so] offen offset:2048\n\t" \
        "buffer_load_dwordx4 v[108:111], %[dv], %[rw], %[so] offen offset:3072\n\ts_cmp_eq_u32 %[rem], 0\n\ts_cbranch_scc1 1f\n\ts_barrier\n\t1:\ns_waitcnt vmcnt(28)\n\tv_add_u32 v168, 1, v168\n\t" \
        "v_mov_b32 v172, 1\n\ts_mov_b64 exec, %[em]\n\tds_write_b64 %[fr], v[168:169]\n\ts_mov_b64 exec, -1\n\ts_waitcnt lgkmcnt(0)\n\ts_barrier\n\tds_read_b64 v[168:169], %[fr]\n\t" \
        "s_mov_b64 exec, %[ep]\n\tglobal_atomic_add %[c], v173, v172, %[qc] sc0\n\ts_mov_b64 exec, -1\n\ts_mov_b32 m0, %[lb]\n\ts_waitcnt lgkmcnt(0)\n\tv_readfirstlane_b32 %[pos], v168\n\t" \
        "v_readfirstlane_b32 %[e], v169\n\ts_nop 0\n\ts_sub_u32 %[pos], %[pos], 1\n\ts_lshr_b32 %[nx], %[pos], 3\n\ts_lshl_b32 %[nx], %[nx], 2\n\ts_lshr_b32 %[e], %[e], %[nx]\n\t" \
        "s_and_b32 %[e], %[e], 15\n\ts_cmp_lt_u32 %[pos], 64\n\ts_cselect_b32 %[e], %[e], 0\n\ts_lshl_b32 %[nx], %[e], 3\n\ts_and_b32 %[so], %[pos], 7\n\ts_add_u32 %[nx], %[nx], %[so]\n\t" \
        "s_sub_u32 %[nx], %[nx], 8\n\ts_cbranch_scc1 2f\n\ts_lshl_b32 %[so], %[nx], 18\n\ts_add_u32 %[so], %[so], %[sb]\n\tbuffer_load_dwordx4 %[dv], %[ra], %[so] offen lds\n\t" \
        "s_add_u32 m0, m0, 0x2000\n\ts_add_u32 %[so], %[so], 0x2000\n\tbuffer_load_dwordx4 %[dv], %[ra], %[so] offen lds\n\ts_add_u32 m0, m0, 0x2000\n\ts_add_u32 %[so], %[so], 0x2000\n\t" \
        "buffer_load_dwordx4 %[dv], %[ra], %[so] offen lds\n\ts_add_u32 m0, m0, 0x2000\n\ts_add_u32 %[so], %[so], 0x2000\n\tbuffer_load_dwordx4 %[dv], %[ra], %[so] offen lds\n\t" \
        "s_add_u32 m0, m0, 0x2000\n\ts_add_u32 %[so], %[so], 0x2000\n\tbuffer_load_dwordx4 %[dv], %[ra], %[so] offen lds\n\ts_add_u32 m0, m0, 0x2000\n\ts_add_u32 %[so], %[so], 0x2000\n\t" \
        "buffer_load_dwordx4 %[dv], %[ra], %[so] offen lds\n\ts_add_u32 m0, m0, 0x2000\n\ts_add_u32 %[so], %[so], 0x2000\n\ts_cmp_lt_u32 %[kq], 3\n\ts_cbranch_scc1 2f\n\t" \
        "buffer_load_dwordx4 %[dv], %[ra], %[so] offen lds\n\t2:\ns_mov_b32 m0, %[keep]\n\ts_waitcnt vmcnt(0)\n\t"
#define ASM_HK \
        "s_mov_b32 %[cnt], 0\n\t1:\nv_mov_b64 v[168:169], 0\n\tv_mov_b64 v[170:171], 0\n\tv_mov_b64 v[172:173], 0\n\tv_mov_b64 v[174:175], 0\n\ts_waitcnt lgkmcnt(0)\n\ts_barrier\n\ts_barrier\n\t" \
        "s_nop 5\n\tds_read_b128 v[136:139], %[ma]\n\tds_read_b128 v[140:143], %[ma] offset:1024\n\tds_read_b128 v[144:147], %[ma] offset:2048\n\tds_read_b128 v[148:151], %[ma] offset:3072\n\t" \
        "ds_read_b128 v[152:155], %[ma] offset:4096\n\tds_read_b128 v[156:159], %[ma] offset:5120\n\tds_read_b128 v[160:163], %[ma] offset:6144\n\tds_read_b128 v[164:167], %[ma] offset:7168\n\t" \
        "s_waitcnt lgkmcnt(0)\n\ts_barrier\n\tv_smfmac_f32_16x16x64_bf16 v[168:171], v[136:139], %[w0], %[ix]\n\tv_smfmac_f32_16x16x64_bf16 v[168:171], v[140:143], %[w1], %[ix]\n\t" \
        "v_smfmac_f32_16x16x64_bf16 v[168:171], v[144:147], %[w2], %[ix]\n\tv_smfmac_f32_16x16x64_bf16 v[168:171], v[148:151], %[w3], %[ix]\n\t" \
        "v_smfmac_f32_16x16x64_bf16 v[168:171], v[152:155], %[w4], %[ix]\n\tv_smfmac_f32_16x16x64_bf16 v[168:171], v[156:159], %[w5], %[ix]\n\t" \
        "v_smfmac_f32_16x16x64_bf16 v[168:171], v[160:163], %[w6], %[ix]\n\tv_smfmac_f32_16x16x64_bf16 v[168:171], v[164:167], %[w7], %[ix]\n\ts_nop 2\n\t" \
        "v_smfmac_f32_16x16x64_bf16 v[172:175], v[136:139], %[w8], %[ix]\n\ts_nop 2\n\tv_smfmac_f32_16x16x64_bf16 v[172:175], v[140:143], %[w9], %[ix]\n\tv_permlane32_swap_b32 v168, v170\n\t" \
        "v_smfmac_f32_16x16x64_bf16 v[172:175], v[144:147], %[w10], %[ix]\n\tv_permlane32_swap_b32 v169, v171\n\tv_smfmac_f32_16x16x64_bf16 v[172:175], v[148:151], %[w11], %[ix]\n\t" \
        "v_add_f32 v168, v168, v170\n\tv_smfmac_f32_16x16x64_bf16 v[172:175], v[152:155], %[w12], %[ix]\n\tv_add_f32 v169, v169, v171\n\t" \
        "v_smfmac_f32_16x16x64_bf16 v[172:175], v[156:159], %[w13], %[ix]\n\tv_mul_f32 v168, %[c2], v168\n\tv_smfmac_f32_16x16x64_bf16 v[172:175], v[160:163], %[w14], %[ix]\n\t" \
        "v_mul_f32 v169, %[c2], v169\n\tv_smfmac_f32_16x16x64_bf16 v[172:175], v[164:167], %[w15], %[ix]\n\tv_cvt_pk_bf16_f32 v170, v168, v169\n\tv_cmp_u_f32 vcc, v168, v169\n\ts_nop 0\n\t" \
        "ds_write_b32 %[qw], v170\n\ts_nop 3\n\tv_permlane32_swap_b32 v172, v174\n\tv_permlane32_swap_b32 v173, v175\n\tv_add_f32 v172, v172, v174\n\tv_add_f32 v173, v173, v175\n\t" \
        "v_mul_f32 v172, %[c2], v172\n\tv_mul_f32 v173, %[c2], v173\n\tv_cvt_pk_bf16_f32 v174, v172, v173\n\tds_write_b32 %[qw], v174 offset:1024\n\ts_barrier\n\ts_barrier\n\ts_cbranch_vccnz 5f\n\t" \
        "4:\ns_sleep 1\n\tds_write_b32 %[qw], %[tg] offset:4096\n\tv_mov_b32 v168, -1\n\tds_write_b32 %[qo], v168\n\tds_write_b32 %[qo], v168 offset:1024\n\ts_branch 8f\n\t5:\n" \
        "s_cmp_eq_u32 %[s], 0\n\ts_cbranch_scc1 4b\n\tv_max3_u32 v170, v136, v137, v138\n\tv_max3_u32 v170, v170, v139, v139\n\tv_max3_u32 v170, v140, v141, v170\n\t" \
        "v_max3_u32 v170, v170, v143, v142\n\tv_max3_u32 v170, v144, v145, v170\n\tv_max3_u32 v170, v170, v147, v146\n\tv_max3_u32 v170, v148, v149, v170\n\tv_max3_u32 v170, v170, v151, v150\n\t" \
        "v_max3_u32 v170, v152, v153, v170\n\tv_max3_u32 v170, v170, v155, v154\n\tv_max3_u32 v170, v156, v157, v170\n\tv_max3_u32 v170, v170, v159, v158\n\tv_max3_u32 v170, v160, v161, v170\n\t" \
        "v_max3_u32 v170, v170, v163, v162\n\tv_max3_u32 v170, v164, v165, v170\n\tv_max3_u32 v170, v170, v167, v166\n\tv_cmp_eq_u32 vcc, -1, v170\n\ts_cbranch_vccz 4b\n\t" \
        "s_add_u32 %[cnt], %[cnt], 1\n\ts_cmp_gt_u32 %[cnt], %[cap]\n\ts_cselect_b32 %[cap], 0, %[cap]\n\ts_cbranch_scc1 4b\n\ts_branch 1b\n\t8:\n"
#define ASM_HQ \
        "s_mov_b32 %[cnt], 0\n\t1:\nv_mov_b64 v[168:169], 0\n\tv_mov_b64 v[170:171], 0\n\tv_mov_b64 v[172:173], 0\n\tv_mov_b64 v[174:175], 0\n\ts_waitcnt lgkmcnt(0)\n\ts_barrier\n\t" \
        "ds_read_b128 v[136:139], %[ma]\n\tds_read_b128 v[140:143], %[ma] offset:1024\n\tds_read_b128 v[144:147], %[ma] offset:2048\n\tds_read_b128 v[148:151], %[ma] offset:3072\n\ts_barrier\n\t" \
        "s_waitcnt lgkmcnt(3)\n\tv_smfmac_f32_16x16x64_bf16 v[168:171], v[136:139], %[w0], %[ix]\n\tds_read_b128 v[152:155], %[ma] offset:4096\n\t" \
        "v_smfmac_f32_16x16x64_bf16 v[172:175], v[136:139], %[w8], %[ix]\n\tds_read_b128 v[156:159], %[ma] offset:5120\n\ts_waitcnt lgkmcnt(4)\n\t" \
        "v_smfmac_f32_16x16x64_bf16 v[168:171], v[140:143], %[w1], %[ix]\n\tds_read_b128 v[160:163], %[ma] offset:6144\n\tv_smfmac_f32_16x16x64_bf16 v[172:175], v[140:143], %[w9], %[ix]\n\t" \
        "ds_read_b128 v[164:167], %[ma] offset:7168\n\ts_waitcnt lgkmcnt(5)\n\tv_smfmac_f32_16x16x64_bf16 v[168:171], v[144:147], %[w2], %[ix]\n\ts_waitcnt lgkmcnt(4)\n\t" \
        "v_smfmac_f32_16x16x64_bf16 v[168:171], v[148:151], %[w3], %[ix]\n\ts_waitcnt lgkmcnt(3)\n\tv_smfmac_f32_16x16x64_bf16 v[168:171], v[152:155], %[w4], %[ix]\n\ts_waitcnt lgkmcnt(2)\n\t" \
        "v_smfmac_f32_16x16x64_bf16 v[168:171], v[156:159], %[w5], %[ix]\n\ts_waitcnt lgkmcnt(1)\n\tv_smfmac_f32_16x16x64_bf16 v[168:171], v[160:163], %[w6], %[ix]\n\ts_waitcnt lgkmcnt(0)\n\t" \
        "v_smfmac_f32_16x16x64_bf16 v[168:171], v[164:167], %[w7], %[ix]\n\ts_nop 2\n\tv_smfmac_f32_16x16x64_bf16 v[172:175], v[144:147], %[w10], %[ix]\n\ts_nop 2\n\t" \
        "v_smfmac_f32_16x16x64_bf16 v[172:175], v[148:151], %[w11], %[ix]\n\tv_permlane32_swap_b32 v168, v170\n\tv_smfmac_f32_16x16x64_bf16 v[172:175], v[152:155], %[w12], %[ix]\n\t" \
        "v_permlane32_swap_b32 v169, v171\n\tv_smfmac_f32_16x16x64_bf16 v[172:175], v[156:159], %[w13], %[ix]\n\tv_add_f32 v168, v168, v170\n\t" \
        "v_smfmac_f32_16x16x64_bf16 v[172:175], v[160:163], %[w14], %[ix]\n\tv_add_f32 v169, v169, v171\n\tv_smfmac_f32_16x16x64_bf16 v[172:175], v[164:167], %[w15], %[ix]\n\ts_barrier\n\t" \
        "v_cvt_pk_bf16_f32 v170, v168, v169\n\tv_cmp_u_f32 vcc, v168, v169\n\ts_nop 0\n\tds_write_b32 %[qw], v170\n\ts_nop 2\n\tv_permlane32_swap_b32 v172, v174\n\t" \
        "v_permlane32_swap_b32 v173, v175\n\tv_add_f32 v172, v172, v174\n\tv_add_f32 v173, v173, v175\n\tv_cvt_pk_bf16_f32 v174, v172, v173\n\tds_write_b32 %[qw], v174 offset:1024\n\t" \
        "s_cbranch_vccnz 5f\n\t4:\ns_barrier\n\ts_barrier\n\ts_sleep 1\n\tds_write_b32 %[qw], %[tg] offset:4096\n\tv_mov_b32 v168, -1\n\tds_write_b32 %[qo], v168\n\t" \
        "ds_write_b32 %[qo], v168 offset:1024\n\ts_branch 8f\n\t5:\ns_cmp_eq_u32 %[s], 0\n\ts_cbranch_scc1 4b\n\tv_max3_u32 v170, v136, v137, v138\n\tv_max3_u32 v170, v170, v139, v139\n\t" \
        "v_max3_u32 v170, v140, v141, v170\n\tv_max3_u32 v170, v170, v143, v142\n\tv_max3_u32 v170, v144, v145, v170\n\tv_max3_u32 v170, v170, v147, v146\n\tv_max3_u32 v170, v148, v149, v170\n\t" \
        "v_max3_u32 v170, v170, v151, v150\n\tv_max3_u32 v170, v152, v153, v170\n\tv_max3_u32 v170, v170, v155, v154\n\tv_max3_u32 v170, v156, v157, v170\n\tv_max3_u32 v170, v170, v159, v158\n\t" \
        "v_max3_u32 v170, v160, v161, v170\n\tv_max3_u32 v170, v170, v163, v162\n\tv_max3_u32 v170, v164, v165, v170\n\tv_max3_u32 v170, v170, v167, v166\n\tv_cmp_eq_u32 vcc, -1, v170\n\t" \
        "s_cbranch_vccz 4b\n\ts_add_u32 %[cnt], %[cnt], 1\n\ts_cmp_gt_u32 %[cnt], %[cap]\n\ts_cselect_b32 %[cap], 0, %[cap]\n\ts_cbranch_scc1 4b\n\ts_barrier\n\ts_barrier\n\ts_branch 1b\n\t8:\n"
#define ASM_HP \
        "s_mov_b32 %[cnt], 0\n\ts_mov_b32 %[keep], m0\n\ts_cmp_eq_u32 %[s], 0\n\ts_cbranch_scc1 2f\n\t1:\ns_mov_b32 m0, %[lm]\n\ts_add_u32 %[f2], %[fb], 0x1000\n\t" \
        "buffer_load_dwordx4 %[o], %[rs], %[fb] offen sc1 lds\n\tbuffer_load_dwordx4 %[o], %[rs], %[fb] offen offset:1024 sc1 lds\n\t" \
        "buffer_load_dwordx4 %[o], %[rs], %[fb] offen offset:2048 sc1 lds\n\tbuffer_load_dwordx4 %[o], %[rs], %[fb] offen offset:3072 sc1 lds\n\ts_add_u32 m0, m0, 0x1000\n\ts_nop 0\n\t" \
        "buffer_load_dwordx4 %[o], %[rs], %[f2] offen sc1 lds\n\tbuffer_load_dwordx4 %[o], %[rs], %[f2] offen offset:1024 sc1 lds\n\t" \
        "buffer_load_dwordx4 %[o], %[rs], %[f2] offen offset:2048 sc1 lds\n\tbuffer_load_dwordx4 %[o], %[rs], %[f2] offen offset:3072 sc1 lds\n\t2:\nv_mov_b64 v[168:169], 0\n\t" \
        "v_mov_b64 v[170:171], 0\n\tv_mov_b64 v[172:173], 0\n\tv_mov_b64 v[174:175], 0\n\ts_waitcnt vmcnt(4) lgkmcnt(0)\n\ts_barrier\n\tds_read_b128 v[136:139], %[ma]\n\t" \
        "ds_read_b128 v[140:143], %[ma] offset:1024\n\tds_read_b128 v[144:147], %[ma] offset:2048\n\tds_read_b128 v[148:151], %[ma] offset:3072\n\ts_waitcnt vmcnt(0)\n\ts_memrealtime s[94:95]\n\t" \
        "s_barrier\n\ts_waitcnt lgkmcnt(3)\n\tv_smfmac_f32_16x16x64_bf16 v[168:171], v[136:139], %[w0], %[ix]\n\tds_read_b128 v[152:155], %[ma] offset:4096\n\t" \
        "v_smfmac_f32_16x16x64_bf16 v[172:175], v[136:139], %[w8], %[ix]\n\tds_read_b128 v[156:159], %[ma] offset:5120\n\ts_waitcnt lgkmcnt(4)\n\t" \
        "v_smfmac_f32_16x16x64_bf16 v[168:171], v[140:143], %[w1], %[ix]\n\tds_read_b128 v[160:163], %[ma] offset:6144\n\tv_smfmac_f32_16x16x64_bf16 v[172:175], v[140:143], %[w9], %[ix]\n\t" \
        "ds_read_b128 v[164:167], %[ma] offset:7168\n\ts_waitcnt lgkmcnt(5)\n\tv_smfmac_f32_16x16x64_bf16 v[168:171], v[144:147], %[w2], %[ix]\n\ts_waitcnt lgkmcnt(4)\n\t" \
        "v_smfmac_f32_16x16x64_bf16 v[168:171], v[148:151], %[w3], %[ix]\n\ts_waitcnt lgkmcnt(3)\n\tv_smfmac_f32_16x16x64_bf16 v[168:171], v[152:155], %[w4], %[ix]\n\ts_waitcnt lgkmcnt(2)\n\t" \
        "v_smfmac_f32_16x16x64_bf16 v[168:171], v[156:159], %[w5], %[ix]\n\ts_waitcnt lgkmcnt(1)\n\tv_smfmac_f32_16x16x64_bf16 v[168:171], v[160:163], %[w6], %[ix]\n\ts_waitcnt lgkmcnt(0)\n\t" \
        "v_smfmac_f32_16x16x64_bf16 v[168:171], v[164:167], %[w7], %[ix]\n\ts_nop 2\n\tv_smfmac_f32_16x16x64_bf16 v[172:175], v[144:147], %[w10], %[ix]\n\ts_nop 2\n\t" \
        "v_smfmac_f32_16x16x64_bf16 v[172:175], v[148:151], %[w11], %[ix]\n\tv_permlane32_swap_b32 v168, v170\n\tv_smfmac_f32_16x16x64_bf16 v[172:175], v[152:155], %[w12], %[ix]\n\t" \
        "v_permlane32_swap_b32 v169, v171\n\tv_smfmac_f32_16x16x64_bf16 v[172:175], v[156:159], %[w13], %[ix]\n\tv_add_f32 v168, v168, v170\n\t" \
        "v_smfmac_f32_16x16x64_bf16 v[172:175], v[160:163], %[w14], %[ix]\n\tv_add_f32 v169, v169, v171\n\tv_smfmac_f32_16x16x64_bf16 v[172:175], v[164:167], %[w15], %[ix]\n\ts_barrier\n\t" \
        "v_cmp_u_f32 vcc, v168, v169\n\ts_lshl_b32 %[f2], s94, 6\n\ts_sub_u32 %[tm], %[f2], %[due]\n\ts_cmp_eq_u32 %[cnt], 0\n\ts_cselect_b32 s95, %[k0], %[k1]\n\ts_sub_u32 %[f2], %[f2], s95\n\t" \
        "s_cmp_le_u32 %[tm], %[l1]\n\ts_cselect_b32 %[f2], %[dp], %[f2]\n\tv_mov_b32 v171, %[f2]\n\ts_cbranch_vccnz 6f\n\tbuffer_store_dword v171, %[av], %[rs], %[ao] offen sc1\n\t7:\n" \
        "v_cvt_pk_bf16_f32 v170, v168, v169\n\tv_permlane32_swap_b32 v172, v174\n\tv_permlane32_swap_b32 v173, v175\n\tds_write_b32 %[qw], v170\n\tv_add_f32 v172, v172, v174\n\t" \
        "v_add_f32 v173, v173, v175\n\tv_cvt_pk_bf16_f32 v174, v172, v173\n\tds_write_b32 %[qw], v174 offset:1024\n\ts_cbranch_vccnz 5f\n\t4:\ns_barrier\n\ts_barrier\n\ts_mov_b32 m0, %[keep]\n\t" \
        "s_sleep 1\n\tds_write_b32 %[qw], %[tg] offset:4096\n\tv_mov_b32 v168, -1\n\tds_write_b32 %[qo], v168\n\tds_write_b32 %[qo], v168 offset:1024\n\ts_branch 8f\n\t5:\ns_cmp_eq_u32 %[s], 0\n\t" \
        "s_cbranch_scc1 4b\n\tv_max3_u32 v170, v136, v137, v138\n\tv_max3_u32 v170, v170, v139, v139\n\tv_max3_u32 v170, v140, v141, v170\n\tv_max3_u32 v170, v170, v143, v142\n\t" \
        "v_max3_u32 v170, v144, v145, v170\n\tv_max3_u32 v170, v170, v147, v146\n\tv_max3_u32 v170, v148, v149, v170\n\tv_max3_u32 v170, v170, v151, v150\n\tv_max3_u32 v170, v152, v153, v170\n\t" \
        "v_max3_u32 v170, v170, v155, v154\n\tv_max3_u32 v170, v156, v157, v170\n\tv_max3_u32 v170, v170, v159, v158\n\tv_max3_u32 v170, v160, v161, v170\n\tv_max3_u32 v170, v170, v163, v162\n\t" \
        "v_max3_u32 v170, v164, v165, v170\n\tv_max3_u32 v170, v170, v167, v166\n\tv_cmp_eq_u32 vcc, -1, v170\n\ts_cbranch_vccz 4b\n\ts_add_u32 %[cnt], %[cnt], 1\n\ts_cmp_gt_u32 %[cnt], %[cap]\n\t" \
        "s_cselect_b32 %[cap], 0, %[cap]\n\ts_cbranch_scc1 4b\n\ts_barrier\n\ts_barrier\n\ts_branch 2b\n\t6:\ns_cmp_eq_u32 %[s], 0\n\ts_cbranch_scc1 7b\n\ts_mov_b32 m0, %[lm]\n\t" \
        "s_add_u32 %[f2], %[fb], 0x1000\n\tbuffer_load_dwordx4 %[o], %[rs], %[fb] offen sc1 lds\n\tbuffer_load_dwordx4 %[o], %[rs], %[fb] offen offset:1024 sc1 lds\n\t" \
        "buffer_load_dwordx4 %[o], %[rs], %[fb] offen offset:2048 sc1 lds\n\tbuffer_load_dwordx4 %[o], %[rs], %[fb] offen offset:3072 sc1 lds\n\ts_add_u32 m0, m0, 0x1000\n\ts_nop 0\n\t" \
        "buffer_load_dwordx4 %[o], %[rs], %[f2] offen sc1 lds\n\tbuffer_load_dwordx4 %[o], %[rs], %[f2] offen offset:1024 sc1 lds\n\t" \
        "buffer_load_dwordx4 %[o], %[rs], %[f2] offen offset:2048 sc1 lds\n\tbuffer_load_dwordx4 %[o], %[rs], %[f2] offen offset:3072 sc1 lds\n\ts_branch 7b\n\t8:\n"
#define ASM_H4 \
        "s_mov_b32 %[cnt], 0\n\t1:\nv_mov_b64 v[168:169], 0\n\tv_mov_b64 v[170:171], 0\n\tv_mov_b64 v[172:173], 0\n\tv_mov_b64 v[174:175], 0\n\ts_waitcnt lgkmcnt(0)\n\ts_barrier\n\t" \
        "ds_read_b128 v[136:139], %[ma]\n\tds_read_b128 v[140:143], %[ma] offset:1024\n\tds_read_b128 v[144:147], %[ma] offset:2048\n\tds_read_b128 v[148:151], %[ma] offset:3072\n\ts_barrier\n\t" \
        "s_waitcnt lgkmcnt(3)\n\tv_smfmac_f32_16x16x64_bf16 v[168:171], v[136:139], %[w0], %[ix]\n\tds_read_b128 v[152:155], %[ma] offset:4096\n\t" \
        "v_smfmac_f32_16x16x64_bf16 v[172:175], v[136:139], %[w8], %[ix]\n\tds_read_b128 v[156:159], %[ma] offset:5120\n\ts_waitcnt lgkmcnt(4)\n\t" \
        "v_smfmac_f32_16x16x64_bf16 v[168:171], v[140:143], %[w1], %[ix]\n\tds_read_b128 v[160:163], %[ma] offset:6144\n\tv_smfmac_f32_16x16x64_bf16 v[172:175], v[140:143], %[w9], %[ix]\n\t" \
        "ds_read_b128 v[164:167], %[ma] offset:7168\n\ts_waitcnt lgkmcnt(5)\n\tv_smfmac_f32_16x16x64_bf16 v[168:171], v[144:147], %[w2], %[ix]\n\ts_waitcnt lgkmcnt(4)\n\t" \
        "v_smfmac_f32_16x16x64_bf16 v[168:171], v[148:151], %[w3], %[ix]\n\ts_waitcnt lgkmcnt(3)\n\tv_smfmac_f32_16x16x64_bf16 v[168:171], v[152:155], %[w4], %[ix]\n\ts_waitcnt lgkmcnt(2)\n\t" \
        "v_smfmac_f32_16x16x64_bf16 v[168:171], v[156:159], %[w5], %[ix]\n\ts_waitcnt lgkmcnt(1)\n\tv_smfmac_f32_16x16x64_bf16 v[168:171], v[160:163], %[w6], %[ix]\n\ts_waitcnt lgkmcnt(0)\n\t" \
        "v_smfmac_f32_16x16x64_bf16 v[168:171], v[164:167], %[w7], %[ix]\n\ts_nop 2\n\tv_smfmac_f32_16x16x64_bf16 v[172:175], v[144:147], %[w10], %[ix]\n\ts_nop 2\n\t" \
        "v_smfmac_f32_16x16x64_bf16 v[172:175], v[148:151], %[w11], %[ix]\n\tv_permlane32_swap_b32 v168, v170\n\tv_smfmac_f32_16x16x64_bf16 v[172:175], v[152:155], %[w12], %[ix]\n\t" \
        "v_permlane32_swap_b32 v169, v171\n\tv_smfmac_f32_16x16x64_bf16 v[172:175], v[156:159], %[w13], %[ix]\n\tv_add_f32 v168, v168, v170\n\t" \
        "v_smfmac_f32_16x16x64_bf16 v[172:175], v[160:163], %[w14], %[ix]\n\tv_add_f32 v169, v169, v171\n\tv_smfmac_f32_16x16x64_bf16 v[172:175], v[164:167], %[w15], %[ix]\n\ts_barrier\n\t" \
        "v_cvt_pk_bf16_f32 v170, v168, v169\n\tv_cmp_u_f32 vcc, v168, v169\n\ts_nop 0\n\tds_write_b32 %[qw], v170\n\ts_nop 2\n\tv_permlane32_swap_b32 v172, v174\n\t" \
        "v_permlane32_swap_b32 v173, v175\n\tv_add_f32 v172, v172, v174\n\tv_add_f32 v173, v173, v175\n\tv_cvt_pk_bf16_f32 v174, v172, v173\n\tds_write_b32 %[qw], v174 offset:1024\n\t" \
        "s_cbranch_vccnz 5f\n\t4:\ns_mov_b32 s95, 0\n\ts_nop 12\n\tds_read_b64_tr_b16 v[136:137], %[fr] offset:0\n\tds_read_b64_tr_b16 v[138:139], %[fr] offset:256\n\t" \
        "ds_read_b64_tr_b16 v[140:141], %[fr] offset:512\n\tds_read_b64_tr_b16 v[142:143], %[fr] offset:768\n\tds_read_b64_tr_b16 v[144:145], %[fr] offset:1024\n\t" \
        "ds_read_b64_tr_b16 v[146:147], %[fr] offset:1280\n\tds_read_b64_tr_b16 v[148:149], %[fr] offset:1536\n\tds_read_b64_tr_b16 v[150:151], %[fr] offset:1792\n\ts_nop 15\n\t" \
        "ds_read_b64_tr_b16 v[152:153], %[fr] offset:2048\n\tds_read_b64_tr_b16 v[154:155], %[fr] offset:2304\n\tds_read_b64_tr_b16 v[156:157], %[fr] offset:2560\n\t" \
        "ds_read_b64_tr_b16 v[158:159], %[fr] offset:2816\n\ts_barrier\n\tds_read_b64_tr_b16 v[160:161], %[fr] offset:3072\n\tds_read_b64_tr_b16 v[162:163], %[fr] offset:3328\n\t" \
        "ds_read_b64_tr_b16 v[164:165], %[fr] offset:3584\n\tds_read_b64_tr_b16 v[166:167], %[fr] offset:3840\n\ts_waitcnt lgkmcnt(4)\n\t" \
        "v_mfma_f32_16x16x32_bf16 v[216:219], v[152:155], v[136:139], 0\n\tv_mfma_f32_16x16x32_bf16 v[216:219], v[156:159], v[140:143], v[216:219]\n\ts_waitcnt lgkmcnt(2)\n\t" \
        "v_mfma_f32_16x16x32_bf16 v[216:219], v[160:163], v[144:147], v[216:219]\n\ts_waitcnt lgkmcnt(0)\n\tv_mfma_f32_16x16x32_bf16 v[216:219], v[164:167], v[148:151], v[216:219]\n\ts_nop 4\n\t" \
        "s_barrier\n\tds_read_b128 v[212:215], %[sr]\n\tds_read_b128 v[208:211], %[er]\n\tv_exp_f32 v216, v216\n\tv_exp_f32 v217, v217\n\tv_exp_f32 v218, v218\n\tv_exp_f32 v219, v219\n\t" \
        "s_waitcnt lgkmcnt(1)\n\tv_pk_add_f32 v[212:213], v[212:213], v[214:215]\n\tv_pk_add_f32 v[226:227], v[216:217], v[218:219]\n\tv_pk_add_f32 v[226:227], v[226:227], v[212:213]\n\t" \
        "v_add_f32 v224, v226, v227\n\tv_add_f32 v225, v226, v227\n\ts_nop 1\n\tv_permlane16_swap_b32 v224, v225\n\tv_add_f32 v225, v224, v225\n\tv_rcp_f32 v226, v225\n\t" \
        "v_subrev_u32 v225, %[lo], v225\n\t3:\nv_cmp_lt_u32 vcc, %[sp], v225\n\tv_pk_mul_f32 v[216:217], v[226:227], v[216:217] op_sel_hi:[0,1]\n\t" \
        "v_pk_mul_f32 v[218:219], v[226:227], v[218:219] op_sel_hi:[0,1]\n\ts_waitcnt lgkmcnt(0)\n\tv_pk_mul_f32 v[208:209], v[226:227], v[208:209] op_sel_hi:[0,1]\n\t" \
        "v_pk_mul_f32 v[210:211], v[226:227], v[210:211] op_sel_hi:[0,1]\n\tv_cvt_pk_bf16_f32 v170, v216, |v217|\n\tv_cvt_pk_bf16_f32 v171, v218, |v219|\n\tv_cvt_pk_bf16_f32 v168, v208, |v209|\n\t" \
        "v_cvt_pk_bf16_f32 v169, v210, |v211|\n\ts_cbranch_vccnz 9f\n\tbuffer_store_dwordx2 v[170:171], %[pm], %[rs], %[po] offen sc1\n\t" \
        "buffer_store_dwordx2 v[168:169], %[px], %[rs], %[po] offen sc1\n\tv_mov_b64 v[144:145], -1\n\ts_nop 0\n\tbuffer_store_dwordx2 v[144:145], %[px], %[rs], %[pq] offen sc1\n\t" \
        "buffer_store_dwordx2 v[144:145], %[pm], %[rs], %[pq] offen sc1\n\ts_branch 8f\n\t9:\nds_write_b32 %[qw], %[tg] offset:4096\n\tds_read_b128 v[176:179], %[kx] offset:0\n\t" \
        "ds_read_b128 v[180:183], %[kx] offset:64\n\tds_read_b128 v[184:187], %[kx] offset:4096\n\tds_read_b128 v[188:191], %[kx] offset:4160\n\tds_read_b128 v[192:195], %[kx] offset:512\n\t" \
        "ds_read_b128 v[196:199], %[kx] offset:576\n\tds_read_b128 v[200:203], %[kx] offset:4608\n\tds_read_b128 v[204:207], %[kx] offset:4672\n\t6:\nds_read_b32 v220, %[ta]\n\t" \
        "s_sub_u32 %[cn], %[cn], 1\n\ts_waitcnt lgkmcnt(0)\n\tv_cmp_ne_u32 vcc, v220, %[tg]\n\ts_cmp_lt_i32 %[cn], 0\n\ts_cbranch_scc1 7f\n\ts_cbranch_vccnz 6b\n\t7:\n" \
        "ds_read_b64_tr_b16 v[136:137], %[fr] offset:0\n\tds_read_b64_tr_b16 v[138:139], %[fr] offset:256\n\tds_read_b64_tr_b16 v[140:141], %[fr] offset:512\n\t" \
        "ds_read_b64_tr_b16 v[142:143], %[fr] offset:768\n\tds_read_b64_tr_b16 v[144:145], %[fr] offset:1024\n\tds_read_b64_tr_b16 v[146:147], %[fr] offset:1280\n\t" \
        "ds_read_b64_tr_b16 v[148:149], %[fr] offset:1536\n\tds_read_b64_tr_b16 v[150:151], %[fr] offset:1792\n\ts_waitcnt lgkmcnt(0)\n\tds_read_b64_tr_b16 v[152:153], %[fr] offset:2048\n\t" \
        "ds_read_b64_tr_b16 v[154:155], %[fr] offset:2304\n\tds_read_b64_tr_b16 v[156:157], %[fr] offset:2560\n\tds_read_b64_tr_b16 v[158:159], %[fr] offset:2816\n\t" \
        "ds_read_b64_tr_b16 v[160:161], %[fr] offset:3072\n\tds_read_b64_tr_b16 v[162:163], %[fr] offset:3328\n\tds_read_b64_tr_b16 v[164:165], %[fr] offset:3584\n\t" \
        "ds_read_b64_tr_b16 v[166:167], %[fr] offset:3840\n\ts_waitcnt lgkmcnt(0)\n\tv_mfma_f32_16x16x32_bf16 v[208:211], v[176:179], v[136:139], 0\n\t" \
        "v_mfma_f32_16x16x32_bf16 v[212:215], v[180:183], v[136:139], 0\n\tv_mfma_f32_16x16x32_bf16 v[208:211], v[184:187], v[140:143], v[208:211]\n\t" \
        "v_mfma_f32_16x16x32_bf16 v[212:215], v[188:191], v[140:143], v[212:215]\n\tv_mfma_f32_16x16x32_bf16 v[208:211], v[192:195], v[144:147], v[208:211]\n\t" \
        "v_mfma_f32_16x16x32_bf16 v[212:215], v[196:199], v[144:147], v[212:215]\n\tv_mfma_f32_16x16x32_bf16 v[208:211], v[200:203], v[148:151], v[208:211]\n\t" \
        "v_mfma_f32_16x16x32_bf16 v[212:215], v[204:207], v[148:151], v[212:215]\n\tv_mfma_f32_16x16x32_bf16 v[216:219], v[152:155], v[136:139], 0\n\t" \
        "v_mfma_f32_16x16x32_bf16 v[216:219], v[156:159], v[140:143], v[216:219]\n\tv_mfma_f32_16x16x32_bf16 v[216:219], v[160:163], v[144:147], v[216:219]\n\t" \
        "v_mfma_f32_16x16x32_bf16 v[216:219], v[164:167], v[148:151], v[216:219]\n\ts_nop 3\n\tv_mov_b32_dpp v208, v212 quad_perm:[0,1,2,3] row_mask:0xf bank_mask:0xc\n\t" \
        "v_mov_b32_dpp v209, v213 quad_perm:[0,1,2,3] row_mask:0xf bank_mask:0xc\n\tv_mov_b32_dpp v210, v214 quad_perm:[0,1,2,3] row_mask:0xf bank_mask:0xc\n\t" \
        "v_mov_b32_dpp v211, v215 quad_perm:[0,1,2,3] row_mask:0xf bank_mask:0xc\n\tv_mov_b32 v223, 0\n\ts_add_u32 s95, s95, 1\n\ts_cmp_eq_u32 s95, 1\n\ts_cbranch_scc1 10f\n\t" \
        "v_max3_f32 v223, v208, v209, v210\n\tv_max3_f32 v223, v223, v211, v216\n\tv_max3_f32 v223, v223, v217, v218\n\tv_max_f32 v223, v223, v219\n\ts_nop 1\n\t" \
        "v_max_f32_dpp v223, v223, v223 row_ror:8 row_mask:0xf bank_mask:0xf\n\tv_mov_b32 v224, v223\n\ts_nop 1\n\tv_permlane16_swap_b32 v223, v224\n\ts_nop 1\n\tv_max_f32 v223, v223, v224\n\t" \
        "v_mov_b32 v224, v223\n\ts_nop 1\n\tv_permlane32_swap_b32 v223, v224\n\ts_nop 1\n\tv_max_f32 v223, v223, v224\n\t10:\nv_sub_f32 v208, v208, v223\n\tv_sub_f32 v209, v209, v223\n\t" \
        "v_sub_f32 v210, v210, v223\n\tv_sub_f32 v211, v211, v223\n\tv_sub_f32 v216, v216, v223\n\tv_sub_f32 v217, v217, v223\n\tv_sub_f32 v218, v218, v223\n\tv_sub_f32 v219, v219, v223\n\t" \
        "v_exp_f32 v208, v208\n\tv_exp_f32 v209, v209\n\tv_exp_f32 v210, v210\n\tv_exp_f32 v211, v211\n\tv_exp_f32 v216, v216\n\tv_exp_f32 v217, v217\n\tv_exp_f32 v218, v218\n\t" \
        "v_exp_f32 v219, v219\n\tv_add_f32 v224, v208, v209\n\tv_add_f32 v225, v210, v211\n\tv_add_f32 v224, v224, v225\n\ts_nop 1\n\t" \
        "v_add_f32_dpp v225, v224, v224 row_ror:8 row_mask:0xf bank_mask:0xf\n\tv_add_f32_dpp v224, v224, v224 row_ror:8 row_mask:0xf bank_mask:0xf\n\ts_nop 1\n\tv_permlane32_swap_b32 v224, v225\n\t" \
        "s_nop 1\n\tv_pk_add_f32 v[226:227], v[216:217], v[218:219]\n\tv_pk_add_f32 v[226:227], v[226:227], v[224:225]\n\tv_add_f32 v224, v226, v227\n\tv_add_f32 v225, v226, v227\n\ts_nop 1\n\t" \
        "v_permlane16_swap_b32 v224, v225\n\tv_add_f32 v225, v224, v225\n\tv_rcp_f32 v226, v225\n\tv_subrev_u32 v225, %[lo], v225\n\ts_cbranch_scc1 3b\n\tv_mov_b32 v225, 0\n\ts_branch 3b\n\t5:\n" \
        "s_cmp_eq_u32 %[s], 0\n\ts_cbranch_scc1 4b\n\tv_max3_u32 v170, v136, v137, v138\n\tv_max3_u32 v170, v170, v139, v139\n\tv_max3_u32 v170, v140, v141, v170\n\t" \
        "v_max3_u32 v170, v170, v143, v142\n\tv_max3_u32 v170, v144, v145, v170\n\tv_max3_u32 v170, v170, v147, v146\n\tv_max3_u32 v170, v148, v149, v170\n\tv_max3_u32 v170, v170, v151, v150\n\t" \
        "v_max3_u32 v170, v152, v153, v170\n\tv_max3_u32 v170, v170, v155, v154\n\tv_max3_u32 v170, v156, v157, v170\n\tv_max3_u32 v170, v170, v159, v158\n\tv_max3_u32 v170, v160, v161, v170\n\t" \
        "v_max3_u32 v170, v170, v163, v162\n\tv_max3_u32 v170, v164, v165, v170\n\tv_max3_u32 v170, v170, v167, v166\n\tv_cmp_eq_u32 vcc, -1, v170\n\ts_cbranch_vccz 4b\n\t" \
        "s_add_u32 %[cnt], %[cnt], 1\n\ts_cmp_gt_u32 %[cnt], %[cap]\n\ts_cselect_b32 %[cap], 0, %[cap]\n\ts_cbranch_scc1 4b\n\ts_barrier\n\ts_barrier\n\ts_branch 1b\n\t8:\n" \
        "ds_write_b32 %[qw], %[tg] offset:4096\n\tv_mov_b32 v168, -1\n\tds_write_b32 %[qo], v168\n\tds_write_b32 %[qo], v168 offset:1024\n\t"
#define ASM_H5 \
        "ds_read_b128 v[176:179], %[kx] offset:0\n\tds_read_b128 v[180:183], %[kx] offset:4096\n\tds_read_b128 v[184:187], %[kx] offset:512\n\tds_read_b128 v[188:191], %[kx] offset:4608\n\t" \
        "s_mov_b32 %[cnt], 0\n\t1:\nv_mov_b64 v[168:169], 0\n\tv_mov_b64 v[170:171], 0\n\tv_mov_b64 v[172:173], 0\n\tv_mov_b64 v[174:175], 0\n\ts_waitcnt lgkmcnt(0)\n\ts_barrier\n\t" \
        "ds_read_b128 v[136:139], %[ma]\n\tds_read_b128 v[140:143], %[ma] offset:1024\n\tds_read_b128 v[144:147], %[ma] offset:2048\n\tds_read_b128 v[148:151], %[ma] offset:3072\n\ts_barrier\n\t" \
        "s_waitcnt lgkmcnt(3)\n\tv_smfmac_f32_16x16x64_bf16 v[168:171], v[136:139], %[w0], %[ix]\n\tds_read_b128 v[152:155], %[ma] offset:4096\n\t" \
        "v_smfmac_f32_16x16x64_bf16 v[172:175], v[136:139], %[w8], %[ix]\n\tds_read_b128 v[156:159], %[ma] offset:5120\n\ts_waitcnt lgkmcnt(4)\n\t" \
        "v_smfmac_f32_16x16x64_bf16 v[168:171], v[140:143], %[w1], %[ix]\n\tds_read_b128 v[160:163], %[ma] offset:6144\n\tv_smfmac_f32_16x16x64_bf16 v[172:175], v[140:143], %[w9], %[ix]\n\t" \
        "ds_read_b128 v[164:167], %[ma] offset:7168\n\ts_waitcnt lgkmcnt(5)\n\tv_smfmac_f32_16x16x64_bf16 v[168:171], v[144:147], %[w2], %[ix]\n\ts_waitcnt lgkmcnt(4)\n\t" \
        "v_smfmac_f32_16x16x64_bf16 v[168:171], v[148:151], %[w3], %[ix]\n\ts_waitcnt lgkmcnt(3)\n\tv_smfmac_f32_16x16x64_bf16 v[168:171], v[152:155], %[w4], %[ix]\n\ts_waitcnt lgkmcnt(2)\n\t" \
        "v_smfmac_f32_16x16x64_bf16 v[168:171], v[156:159], %[w5], %[ix]\n\ts_waitcnt lgkmcnt(1)\n\tv_smfmac_f32_16x16x64_bf16 v[168:171], v[160:163], %[w6], %[ix]\n\ts_waitcnt lgkmcnt(0)\n\t" \
        "v_smfmac_f32_16x16x64_bf16 v[168:171], v[164:167], %[w7], %[ix]\n\ts_nop 2\n\tv_smfmac_f32_16x16x64_bf16 v[172:175], v[144:147], %[w10], %[ix]\n\ts_nop 2\n\t" \
        "v_smfmac_f32_16x16x64_bf16 v[172:175], v[148:151], %[w11], %[ix]\n\tv_permlane32_swap_b32 v168, v170\n\tv_smfmac_f32_16x16x64_bf16 v[172:175], v[152:155], %[w12], %[ix]\n\t" \
        "v_permlane32_swap_b32 v169, v171\n\tv_smfmac_f32_16x16x64_bf16 v[172:175], v[156:159], %[w13], %[ix]\n\tv_add_f32 v168, v168, v170\n\t" \
        "v_smfmac_f32_16x16x64_bf16 v[172:175], v[160:163], %[w14], %[ix]\n\tv_add_f32 v169, v169, v171\n\tv_smfmac_f32_16x16x64_bf16 v[172:175], v[164:167], %[w15], %[ix]\n\ts_barrier\n\t" \
        "v_cvt_pk_bf16_f32 v170, v168, v169\n\tv_cmp_u_f32 vcc, v168, v169\n\ts_nop 0\n\tds_write_b32 %[qw], v170\n\ts_nop 2\n\tv_permlane32_swap_b32 v172, v174\n\t" \
        "v_permlane32_swap_b32 v173, v175\n\tv_add_f32 v172, v172, v174\n\tv_add_f32 v173, v173, v175\n\tv_cvt_pk_bf16_f32 v174, v172, v173\n\tds_write_b32 %[qw], v174 offset:1024\n\t" \
        "s_cbranch_vccnz 5f\n\t4:\ns_nop 15\n\ts_nop 0\n\tds_read_b64_tr_b16 v[136:137], %[fr] offset:0\n\tds_read_b64_tr_b16 v[138:139], %[fr] offset:256\n\t" \
        "ds_read_b64_tr_b16 v[140:141], %[fr] offset:512\n\tds_read_b64_tr_b16 v[142:143], %[fr] offset:768\n\tds_read_b64_tr_b16 v[144:145], %[fr] offset:1024\n\t" \
        "ds_read_b64_tr_b16 v[146:147], %[fr] offset:1280\n\tds_read_b64_tr_b16 v[148:149], %[fr] offset:1536\n\tds_read_b64_tr_b16 v[150:151], %[fr] offset:1792\n\ts_waitcnt lgkmcnt(6)\n\t" \
        "v_mfma_f32_16x16x32_bf16 v[208:211], v[176:179], v[136:139], 0\n\ts_waitcnt lgkmcnt(4)\n\tv_mfma_f32_16x16x32_bf16 v[208:211], v[180:183], v[140:143], v[208:211]\n\ts_waitcnt lgkmcnt(2)\n\t" \
        "v_mfma_f32_16x16x32_bf16 v[208:211], v[184:187], v[144:147], v[208:211]\n\ts_waitcnt lgkmcnt(0)\n\tv_mfma_f32_16x16x32_bf16 v[208:211], v[188:191], v[148:151], v[208:211]\n\t" \
        "s_mov_b64 exec, %[em]\n\ts_nop 6\n\tv_exp_f32 v208, v208\n\tv_exp_f32 v209, v209\n\ts_barrier\n\tv_exp_f32 v210, v210\n\tv_exp_f32 v211, v211\n\tv_add_f32 v212, v208, v209\n\t" \
        "v_add_f32 v213, v210, v211\n\tds_write_b128 %[ew], v[208:211]\n\tv_add_f32 v212, v212, v213\n\tds_write_b32 %[sw], v212\n\ts_barrier\n\ts_mov_b64 exec, -1\n\ts_sleep 1\n\t" \
        "ds_write_b32 %[qw], %[tg] offset:4096\n\tv_mov_b32 v168, -1\n\tds_write_b32 %[qo], v168\n\tds_write_b32 %[qo], v168 offset:1024\n\tds_write_b32 %[so], v168\n\ts_branch 8f\n\t5:\n" \
        "s_cmp_eq_u32 %[s], 0\n\ts_cbranch_scc1 4b\n\tv_max3_u32 v170, v136, v137, v138\n\tv_max3_u32 v170, v170, v139, v139\n\tv_max3_u32 v170, v140, v141, v170\n\t" \
        "v_max3_u32 v170, v170, v143, v142\n\tv_max3_u32 v170, v144, v145, v170\n\tv_max3_u32 v170, v170, v147, v146\n\tv_max3_u32 v170, v148, v149, v170\n\tv_max3_u32 v170, v170, v151, v150\n\t" \
        "v_max3_u32 v170, v152, v153, v170\n\tv_max3_u32 v170, v170, v155, v154\n\tv_max3_u32 v170, v156, v157, v170\n\tv_max3_u32 v170, v170, v159, v158\n\tv_max3_u32 v170, v160, v161, v170\n\t" \
        "v_max3_u32 v170, v170, v163, v162\n\tv_max3_u32 v170, v164, v165, v170\n\tv_max3_u32 v170, v170, v167, v166\n\tv_cmp_eq_u32 vcc, -1, v170\n\ts_cbranch_vccz 4b\n\t" \
        "s_add_u32 %[cnt], %[cnt], 1\n\ts_cmp_gt_u32 %[cnt], %[cap]\n\ts_cselect_b32 %[cap], 0, %[cap]\n\ts_cbranch_scc1 4b\n\ts_barrier\n\ts_barrier\n\ts_branch 1b\n\t8:\n"
#define ASM_VV \
        "s_mov_b32 %[cnt], 0\n\t1:\nv_mov_b64 v[168:169], 0\n\tv_mov_b64 v[170:171], 0\n\tv_mov_b64 v[172:173], 0\n\tv_mov_b64 v[174:175], 0\n\ts_waitcnt lgkmcnt(0)\n\ts_barrier\n\t" \
        "ds_read_b128 v[136:139], %[ma]\n\tds_read_b128 v[140:143], %[ma] offset:1024\n\tds_read_b128 v[144:147], %[ma] offset:2048\n\tds_read_b128 v[148:151], %[ma] offset:3072\n\ts_barrier\n\t" \
        "s_waitcnt lgkmcnt(3)\n\tv_smfmac_f32_16x16x64_bf16 v[168:171], v[136:139], %[w0], %[ix]\n\tds_read_b128 v[152:155], %[ma] offset:4096\n\t" \
        "v_smfmac_f32_16x16x64_bf16 v[172:175], v[136:139], %[w8], %[ix]\n\tds_read_b128 v[156:159], %[ma] offset:5120\n\ts_waitcnt lgkmcnt(4)\n\t" \
        "v_smfmac_f32_16x16x64_bf16 v[168:171], v[140:143], %[w1], %[ix]\n\tds_read_b128 v[160:163], %[ma] offset:6144\n\tv_smfmac_f32_16x16x64_bf16 v[172:175], v[140:143], %[w9], %[ix]\n\t" \
        "ds_read_b128 v[164:167], %[ma] offset:7168\n\ts_waitcnt lgkmcnt(5)\n\tv_smfmac_f32_16x16x64_bf16 v[168:171], v[144:147], %[w2], %[ix]\n\ts_waitcnt lgkmcnt(4)\n\t" \
        "v_smfmac_f32_16x16x64_bf16 v[168:171], v[148:151], %[w3], %[ix]\n\ts_waitcnt lgkmcnt(3)\n\tv_smfmac_f32_16x16x64_bf16 v[168:171], v[152:155], %[w4], %[ix]\n\ts_waitcnt lgkmcnt(2)\n\t" \
        "v_smfmac_f32_16x16x64_bf16 v[168:171], v[156:159], %[w5], %[ix]\n\ts_waitcnt lgkmcnt(1)\n\tv_smfmac_f32_16x16x64_bf16 v[168:171], v[160:163], %[w6], %[ix]\n\ts_waitcnt lgkmcnt(0)\n\t" \
        "v_smfmac_f32_16x16x64_bf16 v[168:171], v[164:167], %[w7], %[ix]\n\ts_nop 2\n\tv_smfmac_f32_16x16x64_bf16 v[172:175], v[144:147], %[w10], %[ix]\n\ts_nop 2\n\t" \
        "v_smfmac_f32_16x16x64_bf16 v[172:175], v[148:151], %[w11], %[ix]\n\tv_permlane32_swap_b32 v168, v170\n\tv_smfmac_f32_16x16x64_bf16 v[172:175], v[152:155], %[w12], %[ix]\n\t" \
        "v_permlane32_swap_b32 v169, v171\n\tv_smfmac_f32_16x16x64_bf16 v[172:175], v[156:159], %[w13], %[ix]\n\tv_add_f32 v168, v168, v170\n\t" \
        "v_smfmac_f32_16x16x64_bf16 v[172:175], v[160:163], %[w14], %[ix]\n\tv_add_f32 v169, v169, v171\n\tv_smfmac_f32_16x16x64_bf16 v[172:175], v[164:167], %[w15], %[ix]\n\t" \
        "v_cvt_pk_bf16_f32 v170, v168, v169\n\tv_cmp_u_f32 vcc, v168, v169\n\ts_nop 0\n\tds_write_b32 %[qw], v170\n\ts_nop 3\n\tv_permlane32_swap_b32 v172, v174\n\t" \
        "v_permlane32_swap_b32 v173, v175\n\ts_waitcnt lgkmcnt(0)\n\ts_barrier\n\tv_add_f32 v172, v172, v174\n\tv_add_f32 v173, v173, v175\n\tv_cvt_pk_bf16_f32 v174, v172, v173\n\t" \
        "ds_write_b32 %[qw], v174 offset:1024\n\ts_barrier\n\ts_waitcnt lgkmcnt(0)\n\ts_barrier\n\ts_cbranch_vccnz 5f\n\t4:\ns_branch 8f\n\t5:\ns_cmp_eq_u32 %[s], 0\n\ts_cbranch_scc1 4b\n\t" \
        "v_max3_u32 v170, v136, v137, v138\n\tv_max3_u32 v170, v170, v139, v139\n\tv_max3_u32 v170, v140, v141, v170\n\tv_max3_u32 v170, v170, v143, v142\n\tv_max3_u32 v170, v144, v145, v170\n\t" \
        "v_max3_u32 v170, v170, v147, v146\n\tv_max3_u32 v170, v148, v149, v170\n\tv_max3_u32 v170, v170, v151, v150\n\tv_max3_u32 v170, v152, v153, v170\n\tv_max3_u32 v170, v170, v155, v154\n\t" \
        "v_max3_u32 v170, v156, v157, v170\n\tv_max3_u32 v170, v170, v159, v158\n\tv_max3_u32 v170, v160, v161, v170\n\tv_max3_u32 v170, v170, v163, v162\n\tv_max3_u32 v170, v164, v165, v170\n\t" \
        "v_max3_u32 v170, v170, v167, v166\n\tv_cmp_eq_u32 vcc, -1, v170\n\ts_cbranch_vccz 4b\n\ts_add_u32 %[cnt], %[cnt], 1\n\ts_cmp_gt_u32 %[cnt], %[cap]\n\ts_cselect_b32 %[cap], 0, %[cap]\n\t" \
        "s_cbranch_scc1 4b\n\ts_branch 1b\n\t8:\n"
#define ASM_VP \
        "s_mov_b32 %[cnt], 0\n\ts_mov_b32 %[keep], m0\n\ts_cmp_eq_u32 %[s], 0\n\ts_cbranch_scc1 2f\n\t1:\ns_mov_b32 m0, %[lm]\n\ts_add_u32 %[f2], %[fb], 0x1000\n\t" \
        "buffer_load_dwordx4 %[o], %[rs], %[fb] offen sc1 lds\n\tbuffer_load_dwordx4 %[o], %[rs], %[fb] offen offset:1024 sc1 lds\n\t" \
        "buffer_load_dwordx4 %[o], %[rs], %[fb] offen offset:2048 sc1 lds\n\tbuffer_load_dwordx4 %[o], %[rs], %[fb] offen offset:3072 sc1 lds\n\ts_add_u32 m0, m0, 0x1000\n\ts_nop 0\n\t" \
        "buffer_load_dwordx4 %[o], %[rs], %[f2] offen sc1 lds\n\tbuffer_load_dwordx4 %[o], %[rs], %[f2] offen offset:1024 sc1 lds\n\t" \
        "buffer_load_dwordx4 %[o], %[rs], %[f2] offen offset:2048 sc1 lds\n\tbuffer_load_dwordx4 %[o], %[rs], %[f2] offen offset:3072 sc1 lds\n\t2:\nv_mov_b64 v[168:169], 0\n\t" \
        "v_mov_b64 v[170:171], 0\n\tv_mov_b64 v[172:173], 0\n\tv_mov_b64 v[174:175], 0\n\ts_waitcnt vmcnt(4) lgkmcnt(0)\n\ts_barrier\n\tds_read_b128 v[136:139], %[ma]\n\t" \
        "ds_read_b128 v[140:143], %[ma] offset:1024\n\tds_read_b128 v[144:147], %[ma] offset:2048\n\tds_read_b128 v[148:151], %[ma] offset:3072\n\ts_waitcnt vmcnt(0)\n\ts_barrier\n\t" \
        "s_waitcnt lgkmcnt(3)\n\tv_smfmac_f32_16x16x64_bf16 v[168:171], v[136:139], %[w0], %[ix]\n\tds_read_b128 v[152:155], %[ma] offset:4096\n\t" \
        "v_smfmac_f32_16x16x64_bf16 v[172:175], v[136:139], %[w8], %[ix]\n\tds_read_b128 v[156:159], %[ma] offset:5120\n\ts_waitcnt lgkmcnt(4)\n\t" \
        "v_smfmac_f32_16x16x64_bf16 v[168:171], v[140:143], %[w1], %[ix]\n\tds_read_b128 v[160:163], %[ma] offset:6144\n\tv_smfmac_f32_16x16x64_bf16 v[172:175], v[140:143], %[w9], %[ix]\n\t" \
        "ds_read_b128 v[164:167], %[ma] offset:7168\n\ts_waitcnt lgkmcnt(5)\n\tv_smfmac_f32_16x16x64_bf16 v[168:171], v[144:147], %[w2], %[ix]\n\ts_waitcnt lgkmcnt(4)\n\t" \
        "v_smfmac_f32_16x16x64_bf16 v[168:171], v[148:151], %[w3], %[ix]\n\ts_waitcnt lgkmcnt(3)\n\tv_smfmac_f32_16x16x64_bf16 v[168:171], v[152:155], %[w4], %[ix]\n\ts_waitcnt lgkmcnt(2)\n\t" \
        "v_smfmac_f32_16x16x64_bf16 v[168:171], v[156:159], %[w5], %[ix]\n\ts_waitcnt lgkmcnt(1)\n\tv_smfmac_f32_16x16x64_bf16 v[168:171], v[160:163], %[w6], %[ix]\n\ts_waitcnt lgkmcnt(0)\n\t" \
        "v_smfmac_f32_16x16x64_bf16 v[168:171], v[164:167], %[w7], %[ix]\n\ts_nop 2\n\tv_smfmac_f32_16x16x64_bf16 v[172:175], v[144:147], %[w10], %[ix]\n\ts_nop 2\n\t" \
        "v_smfmac_f32_16x16x64_bf16 v[172:175], v[148:151], %[w11], %[ix]\n\tv_permlane32_swap_b32 v168, v170\n\tv_smfmac_f32_16x16x64_bf16 v[172:175], v[152:155], %[w12], %[ix]\n\t" \
        "v_permlane32_swap_b32 v169, v171\n\tv_smfmac_f32_16x16x64_bf16 v[172:175], v[156:159], %[w13], %[ix]\n\tv_add_f32 v168, v168, v170\n\t" \
        "v_smfmac_f32_16x16x64_bf16 v[172:175], v[160:163], %[w14], %[ix]\n\tv_add_f32 v169, v169, v171\n\tv_smfmac_f32_16x16x64_bf16 v[172:175], v[164:167], %[w15], %[ix]\n\t" \
        "v_cvt_pk_bf16_f32 v170, v168, v169\n\tv_cmp_u_f32 vcc, v168, v169\n\ts_nop 0\n\tds_write_b32 %[qw], v170\n\ts_nop 3\n\tv_permlane32_swap_b32 v172, v174\n\t" \
        "v_permlane32_swap_b32 v173, v175\n\ts_waitcnt lgkmcnt(0)\n\ts_barrier\n\tv_add_f32 v172, v172, v174\n\tv_add_f32 v173, v173, v175\n\tv_cvt_pk_bf16_f32 v174, v172, v173\n\t" \
        "ds_write_b32 %[qw], v174 offset:1024\n\ts_barrier\n\ts_cbranch_vccnz 6f\n\t7:\ns_waitcnt lgkmcnt(0)\n\ts_barrier\n\ts_cbranch_vccnz 5f\n\t4:\ns_mov_b32 m0, %[keep]\n\ts_branch 8f\n\t5:\n" \
        "s_cmp_eq_u32 %[s], 0\n\ts_cbranch_scc1 4b\n\tv_max3_u32 v170, v136, v137, v138\n\tv_max3_u32 v170, v170, v139, v139\n\tv_max3_u32 v170, v140, v141, v170\n\t" \
        "v_max3_u32 v170, v170, v143, v142\n\tv_max3_u32 v170, v144, v145, v170\n\tv_max3_u32 v170, v170, v147, v146\n\tv_max3_u32 v170, v148, v149, v170\n\tv_max3_u32 v170, v170, v151, v150\n\t" \
        "v_max3_u32 v170, v152, v153, v170\n\tv_max3_u32 v170, v170, v155, v154\n\tv_max3_u32 v170, v156, v157, v170\n\tv_max3_u32 v170, v170, v159, v158\n\tv_max3_u32 v170, v160, v161, v170\n\t" \
        "v_max3_u32 v170, v170, v163, v162\n\tv_max3_u32 v170, v164, v165, v170\n\tv_max3_u32 v170, v170, v167, v166\n\tv_cmp_eq_u32 vcc, -1, v170\n\ts_cbranch_vccz 4b\n\t" \
        "s_add_u32 %[cnt], %[cnt], 1\n\ts_cmp_gt_u32 %[cnt], %[cap]\n\ts_cselect_b32 %[cap], 0, %[cap]\n\ts_cbranch_scc1 4b\n\ts_branch 2b\n\t6:\ns_cmp_eq_u32 %[s], 0\n\ts_cbranch_scc1 7b\n\t" \
        "s_mov_b32 m0, %[lm]\n\ts_add_u32 %[f2], %[fb], 0x1000\n\tbuffer_load_dwordx4 %[o], %[rs], %[fb] offen sc1 lds\n\tbuffer_load_dwordx4 %[o], %[rs], %[fb] offen offset:1024 sc1 lds\n\t" \
        "buffer_load_dwordx4 %[o], %[rs], %[fb] offen offset:2048 sc1 lds\n\tbuffer_load_dwordx4 %[o], %[rs], %[fb] offen offset:3072 sc1 lds\n\ts_add_u32 m0, m0, 0x1000\n\ts_nop 0\n\t" \
        "buffer_load_dwordx4 %[o], %[rs], %[f2] offen sc1 lds\n\tbuffer_load_dwordx4 %[o], %[rs], %[f2] offen offset:1024 sc1 lds\n\t" \
        "buffer_load_dwordx4 %[o], %[rs], %[f2] offen offset:2048 sc1 lds\n\tbuffer_load_dwordx4 %[o], %[rs], %[f2] offen offset:3072 sc1 lds\n\ts_branch 7b\n\t8:\n"
#define ASM_VO \
        "s_mov_b32 %[cnt], 0\n\t1:\nv_mov_b64 v[192:193], 0\n\tv_mov_b64 v[194:195], 0\n\tv_mov_b64 v[196:197], 0\n\tv_mov_b64 v[198:199], 0\n\tv_mov_b64 v[200:201], 0\n\tv_mov_b64 v[202:203], 0\n\t" \
        "v_mov_b64 v[204:205], 0\n\tv_mov_b64 v[206:207], 0\n\ts_waitcnt lgkmcnt(0)\n\ts_barrier\n\ts_barrier\n\ts_nop 5\n\tds_read_b128 v[136:139], %[ma]\n\t" \
        "ds_read_b128 v[140:143], %[ma] offset:1024\n\tds_read_b128 v[144:147], %[ma] offset:2048\n\tds_read_b128 v[148:151], %[ma] offset:3072\n\tds_read_b128 v[152:155], %[ma] offset:4096\n\t" \
        "ds_read_b128 v[156:159], %[ma] offset:5120\n\tds_read_b128 v[160:163], %[ma] offset:6144\n\tds_read_b128 v[164:167], %[ma] offset:7168\n\ts_waitcnt lgkmcnt(0)\n\ts_barrier\n\t" \
        "ds_read_b64_tr_b16 v[176:177], %[fr] offset:0\n\tds_read_b64_tr_b16 v[178:179], %[fr] offset:256\n\tds_read_b64_tr_b16 v[180:181], %[fr] offset:512\n\t" \
        "ds_read_b64_tr_b16 v[182:183], %[fr] offset:768\n\ts_barrier\n\ts_waitcnt lgkmcnt(2)\n\tv_smfmac_f32_16x16x64_bf16 v[192:195], v[176:179], %[w0], %[ix]\n\t" \
        "v_smfmac_f32_16x16x64_bf16 v[196:199], v[176:179], %[w4], %[ix]\n\ts_waitcnt lgkmcnt(0)\n\tv_smfmac_f32_16x16x64_bf16 v[192:195], v[180:183], %[w1], %[ix]\n\ts_barrier\n\t" \
        "v_smfmac_f32_16x16x64_bf16 v[196:199], v[180:183], %[w5], %[ix]\n\tds_read_b64_tr_b16 v[184:185], %[fr] offset:1024\n\tds_read_b64_tr_b16 v[186:187], %[fr] offset:1280\n\t" \
        "v_smfmac_f32_16x16x64_bf16 v[200:203], v[176:179], %[w8], %[ix]\n\tds_read_b64_tr_b16 v[188:189], %[fr] offset:1536\n\tds_read_b64_tr_b16 v[190:191], %[fr] offset:1792\n\t" \
        "v_smfmac_f32_16x16x64_bf16 v[204:207], v[176:179], %[w12], %[ix]\n\tv_smfmac_f32_16x16x64_bf16 v[200:203], v[180:183], %[w9], %[ix]\n\t" \
        "v_smfmac_f32_16x16x64_bf16 v[204:207], v[180:183], %[w13], %[ix]\n\ts_waitcnt lgkmcnt(2)\n\tv_smfmac_f32_16x16x64_bf16 v[192:195], v[184:187], %[w2], %[ix]\n\t" \
        "v_smfmac_f32_16x16x64_bf16 v[196:199], v[184:187], %[w6], %[ix]\n\ts_waitcnt lgkmcnt(0)\n\tv_smfmac_f32_16x16x64_bf16 v[192:195], v[188:191], %[w3], %[ix]\n\t" \
        "v_smfmac_f32_16x16x64_bf16 v[196:199], v[188:191], %[w7], %[ix]\n\ts_nop 2\n\tv_smfmac_f32_16x16x64_bf16 v[200:203], v[184:187], %[w10], %[ix]\n\ts_nop 2\n\t" \
        "v_smfmac_f32_16x16x64_bf16 v[204:207], v[184:187], %[w14], %[ix]\n\tv_cmp_u_f32 vcc, v192, v193\n\tv_cmp_u_f32 s[94:95], v194, v195\n\t" \
        "v_smfmac_f32_16x16x64_bf16 v[200:203], v[188:191], %[w11], %[ix]\n\tv_cvt_pk_bf16_f32 v208, v192, v193\n\tv_cvt_pk_bf16_f32 v210, v196, v197\n\t" \
        "v_smfmac_f32_16x16x64_bf16 v[204:207], v[188:191], %[w15], %[ix]\n\tv_cvt_pk_bf16_f32 v209, v194, v195\n\tv_cvt_pk_bf16_f32 v211, v198, v199\n\tv_min_u32 v208, -2, v208\n\t" \
        "s_or_b64 vcc, vcc, s[94:95]\n\tv_min_u32 v210, -2, v210\n\tv_min_u32 v209, -2, v209\n\tv_min_u32 v211, -2, v211\n\tv_permlane16_swap_b32 v208, v210\n\ts_nop 0\n\t" \
        "v_permlane16_swap_b32 v209, v211\n\ts_cbranch_vccnz 5f\n\t4:\nbuffer_store_dwordx4 v[208:211], %[so0], %[rs], 0 offen sc1\n\tv_cvt_pk_bf16_f32 v212, v200, v201\n\t" \
        "v_cvt_pk_bf16_f32 v214, v204, v205\n\tv_cvt_pk_bf16_f32 v213, v202, v203\n\tv_cvt_pk_bf16_f32 v215, v206, v207\n\tv_min_u32 v212, -2, v212\n\tv_min_u32 v214, -2, v214\n\t" \
        "v_min_u32 v213, -2, v213\n\tv_min_u32 v215, -2, v215\n\tv_permlane16_swap_b32 v212, v214\n\ts_nop 0\n\tv_permlane16_swap_b32 v213, v215\n\t" \
        "buffer_store_dwordx4 v[212:215], %[so1], %[rs], 0 offen sc1\n\ts_branch 8f\n\t5:\ns_cmp_eq_u32 %[s], 0\n\ts_cbranch_scc1 4b\n\tv_max3_u32 v170, v136, v137, v138\n\t" \
        "v_max3_u32 v170, v170, v139, v139\n\tv_max3_u32 v170, v140, v141, v170\n\tv_max3_u32 v170, v170, v143, v142\n\tv_max3_u32 v170, v144, v145, v170\n\tv_max3_u32 v170, v170, v147, v146\n\t" \
        "v_max3_u32 v170, v148, v149, v170\n\tv_max3_u32 v170, v170, v151, v150\n\tv_max3_u32 v170, v152, v153, v170\n\tv_max3_u32 v170, v170, v155, v154\n\tv_max3_u32 v170, v156, v157, v170\n\t" \
        "v_max3_u32 v170, v170, v159, v158\n\tv_max3_u32 v170, v160, v161, v170\n\tv_max3_u32 v170, v170, v163, v162\n\tv_max3_u32 v170, v164, v165, v170\n\tv_max3_u32 v170, v170, v167, v166\n\t" \
        "v_cmp_eq_u32 vcc, -1, v170\n\ts_cbranch_vccz 4b\n\ts_add_u32 %[cnt], %[cnt], 1\n\ts_cmp_gt_u32 %[cnt], %[cap]\n\ts_cselect_b32 %[cap], 0, %[cap]\n\ts_cbranch_scc1 4b\n\ts_branch 1b\n\t8:\n"
#define ASM_CSTEP \
        "s_mov_b32 %[cnt], 0\n\ts_mov_b32 s94, %[ab]\n\tv_mov_b32 v248, %[fl]\n\tv_mov_b32 v249, %[tg]\n\tv_add_u32 v36, %[lx0], %[l16]\n\tv_add_u32 v68, %[lx1], %[l16]\n\t" \
        "v_add_u32 v100, %[lx2], %[l16]\n\tv_add_u32 v132, %[lx3], %[l16]\n\ts_mov_b32 %[tn], %[pa]\n\t22:\ns_memrealtime vcc\n\ts_waitcnt lgkmcnt(0)\n\ts_lshl_b32 vcc_lo, vcc_lo, 6\n\t" \
        "s_sub_u32 vcc_lo, %[tn], vcc_lo\n\ts_sub_u32 vcc_hi, vcc_lo, 1\n\ts_cmp_gt_u32 vcc_hi, 0x40000\n\ts_cbranch_scc1 23f\n\ts_cmp_lt_u32 vcc_lo, 0x200\n\ts_cbranch_scc1 22b\n\ts_sleep 1\n\t" \
        "s_branch 22b\n\t23:\nds_read_b32 v250, v248\n\ts_waitcnt lgkmcnt(0)\n\ts_buffer_load_dword %[aw], %[rs], s94\n\tv_readfirstlane_b32 s93, v250\n\ts_nop 0\n\ts_cmp_lg_u32 s93, %[tg]\n\t" \
        "s_cbranch_scc1 24f\n\tds_read_b128 v[8:11], v36\n\tds_read_b128 v[12:15], v36 offset:1024\n\tds_read_b128 v[16:19], v36 offset:2048\n\tds_read_b128 v[20:23], v36 offset:3072\n\t" \
        "ds_read_b128 v[24:27], v36 offset:4096\n\tds_read_b128 v[28:31], v36 offset:5120\n\tds_read_b128 v[32:35], v36 offset:6144\n\tds_read_b128 v[36:39], v36 offset:7168\n\t" \
        "ds_read_b128 v[40:43], v68\n\tds_read_b128 v[44:47], v68 offset:1024\n\tds_read_b128 v[48:51], v68 offset:2048\n\tds_read_b128 v[52:55], v68 offset:3072\n\t" \
        "ds_read_b128 v[56:59], v68 offset:4096\n\tds_read_b128 v[60:63], v68 offset:5120\n\tds_read_b128 v[64:67], v68 offset:6144\n\tds_read_b128 v[68:71], v68 offset:7168\n\t" \
        "ds_read_b128 v[72:75], v100\n\tds_read_b128 v[76:79], v100 offset:1024\n\tds_read_b128 v[80:83], v100 offset:2048\n\tds_read_b128 v[84:87], v100 offset:3072\n\t" \
        "ds_read_b128 v[88:91], v100 offset:4096\n\tds_read_b128 v[92:95], v100 offset:5120\n\tds_read_b128 v[96:99], v100 offset:6144\n\tds_read_b128 v[100:103], v100 offset:7168\n\t" \
        "ds_read_b128 v[104:107], v132\n\tds_read_b128 v[108:111], v132 offset:1024\n\tds_read_b128 v[112:115], v132 offset:2048\n\tds_read_b128 v[116:119], v132 offset:3072\n\t" \
        "ds_read_b128 v[120:123], v132 offset:4096\n\tds_read_b128 v[124:127], v132 offset:5120\n\tds_read_b128 v[128:131], v132 offset:6144\n\tds_read_b128 v[132:135], v132 offset:7168\n\t24:\n" \
        "s_waitcnt lgkmcnt(0)\n\ts_andn2_b32 s95, %[aw], 63\n\ts_cmp_eq_u32 %[aw], -1\n\ts_cbranch_scc1 2f\n\t4:\ns_add_u32 %[tn], s95, %[ax]\n\t5:\ns_memrealtime vcc\n\ts_waitcnt lgkmcnt(0)\n\t" \
        "s_lshl_b32 vcc_lo, vcc_lo, 6\n\ts_sub_u32 vcc_lo, %[tn], vcc_lo\n\ts_sub_u32 vcc_hi, vcc_lo, 1\n\ts_cmp_gt_u32 vcc_hi, 0x40000\n\ts_cbranch_scc1 6f\n\ts_cmp_lt_u32 vcc_lo, 0x200\n\t" \
        "s_cbranch_scc1 5b\n\ts_sleep 1\n\ts_branch 5b\n\t6:\nbuffer_load_dwordx4 v[216:219], %[l16], %[rs], %[vb0] offen sc1\n\t" \
        "buffer_load_dwordx4 v[220:223], %[l16], %[rs], %[vb0] offen offset:1024 sc1\n\tbuffer_load_dwordx4 v[224:227], %[l16], %[rs], %[vb1] offen sc1\n\t" \
        "buffer_load_dwordx4 v[228:231], %[l16], %[rs], %[vb1] offen offset:1024 sc1\n\tbuffer_load_dwordx4 v[232:235], %[l16], %[rs], %[vb2] offen sc1\n\t" \
        "buffer_load_dwordx4 v[236:239], %[l16], %[rs], %[vb2] offen offset:1024 sc1\n\tbuffer_load_dwordx4 v[240:243], %[l16], %[rs], %[vb3] offen sc1\n\t" \
        "buffer_load_dwordx4 v[244:247], %[l16], %[rs], %[vb3] offen offset:1024 sc1\n\ts_add_u32 %[tn], s95, %[ap]\n\t7:\ns_memrealtime vcc\n\ts_waitcnt lgkmcnt(0)\n\t" \
        "s_lshl_b32 vcc_lo, vcc_lo, 6\n\ts_sub_u32 vcc_lo, %[tn], vcc_lo\n\ts_sub_u32 vcc_hi, vcc_lo, 1\n\ts_cmp_gt_u32 vcc_hi, 0x40000\n\ts_cbranch_scc1 8f\n\ts_cmp_lt_u32 vcc_lo, 0x200\n\t" \
        "s_cbranch_scc1 7b\n\ts_sleep 1\n\ts_branch 7b\n\t8:\nbuffer_load_dwordx4 v[200:203], %[pm], %[rs], %[pb0] offen offset:1024 sc1\n\t" \
        "buffer_load_dwordx4 v[204:207], %[pm], %[rs], %[pb1] offen offset:1024 sc1\n\tbuffer_load_dwordx4 v[208:211], %[pm], %[rs], %[pb2] offen offset:1024 sc1\n\t" \
        "buffer_load_dwordx4 v[212:215], %[pm], %[rs], %[pb3] offen offset:1024 sc1\n\tbuffer_load_dwordx4 v[136:139], %[l16], %[rs], %[pb0] offen sc1\n\t" \
        "buffer_load_dwordx4 v[140:143], %[l16], %[rs], %[pb0] offen offset:1280 sc1\n\tbuffer_load_dwordx4 v[144:147], %[l16], %[rs], %[pb0] offen offset:2560 sc1\n\t" \
        "buffer_load_dwordx4 v[148:151], %[l16], %[rs], %[pb0] offen offset:3840 sc1\n\tbuffer_load_dwordx4 v[152:155], %[l16], %[rs], %[pb1] offen sc1\n\t" \
        "buffer_load_dwordx4 v[156:159], %[l16], %[rs], %[pb1] offen offset:1280 sc1\n\tbuffer_load_dwordx4 v[160:163], %[l16], %[rs], %[pb1] offen offset:2560 sc1\n\t" \
        "buffer_load_dwordx4 v[164:167], %[l16], %[rs], %[pb1] offen offset:3840 sc1\n\tbuffer_load_dwordx4 v[168:171], %[l16], %[rs], %[pb2] offen sc1\n\t" \
        "buffer_load_dwordx4 v[172:175], %[l16], %[rs], %[pb2] offen offset:1280 sc1\n\tbuffer_load_dwordx4 v[176:179], %[l16], %[rs], %[pb2] offen offset:2560 sc1\n\t" \
        "buffer_load_dwordx4 v[180:183], %[l16], %[rs], %[pb2] offen offset:3840 sc1\n\tbuffer_load_dwordx4 v[184:187], %[l16], %[rs], %[pb3] offen sc1\n\t" \
        "buffer_load_dwordx4 v[188:191], %[l16], %[rs], %[pb3] offen offset:1280 sc1\n\tbuffer_load_dwordx4 v[192:195], %[l16], %[rs], %[pb3] offen offset:2560 sc1\n\t" \
        "buffer_load_dwordx4 v[196:199], %[l16], %[rs], %[pb3] offen offset:3840 sc1\n\tds_write_b32 v248, v249 offset:4\n\ts_mov_b32 %[tn], 0\n\ts_cmp_lg_u32 s93, %[tg]\n\ts_cbranch_scc1 20f\n\t9:\n" \
        "v_mov_b64 v[248:249], 0\n\tv_mov_b64 v[250:251], 0\n\ts_cmp_eq_u32 %[on], 0\n\ts_cbranch_scc1 17f\n\ts_waitcnt vmcnt(19)\n\t" \
        "v_smfmac_f32_16x16x64_bf16 v[248:251], v[200:203], v[216:223], %[ix]\n\ts_waitcnt vmcnt(18)\n\tv_smfmac_f32_16x16x64_bf16 v[248:251], v[204:207], v[224:231], %[ix]\n\ts_waitcnt vmcnt(17)\n\t" \
        "v_smfmac_f32_16x16x64_bf16 v[248:251], v[208:211], v[232:239], %[ix]\n\ts_waitcnt vmcnt(16)\n\tv_smfmac_f32_16x16x64_bf16 v[248:251], v[212:215], v[240:247], %[ix]\n\t" \
        "s_waitcnt vmcnt(15) lgkmcnt(14)\n\tv_smfmac_f32_16x16x64_bf16 v[248:251], v[136:139], v[8:15], %[ix]\n\ts_waitcnt vmcnt(14) lgkmcnt(14)\n\t" \
        "v_smfmac_f32_16x16x64_bf16 v[248:251], v[140:143], v[16:23], %[ix]\n\ts_waitcnt vmcnt(13) lgkmcnt(14)\n\tv_smfmac_f32_16x16x64_bf16 v[248:251], v[144:147], v[24:31], %[ix]\n\t" \
        "s_waitcnt vmcnt(12) lgkmcnt(14)\n\tv_smfmac_f32_16x16x64_bf16 v[248:251], v[148:151], v[32:39], %[ix]\n\ts_waitcnt vmcnt(11) lgkmcnt(14)\n\t" \
        "v_smfmac_f32_16x16x64_bf16 v[248:251], v[152:155], v[40:47], %[ix]\n\ts_waitcnt vmcnt(10) lgkmcnt(14)\n\tv_smfmac_f32_16x16x64_bf16 v[248:251], v[156:159], v[48:55], %[ix]\n\t" \
        "s_waitcnt vmcnt(9) lgkmcnt(14)\n\tv_smfmac_f32_16x16x64_bf16 v[248:251], v[160:163], v[56:63], %[ix]\n\ts_waitcnt vmcnt(8) lgkmcnt(14)\n\t" \
        "v_smfmac_f32_16x16x64_bf16 v[248:251], v[164:167], v[64:71], %[ix]\n\ts_waitcnt vmcnt(7) lgkmcnt(14)\n\tv_smfmac_f32_16x16x64_bf16 v[248:251], v[168:171], v[72:79], %[ix]\n\t" \
        "s_waitcnt vmcnt(6) lgkmcnt(12)\n\tv_smfmac_f32_16x16x64_bf16 v[248:251], v[172:175], v[80:87], %[ix]\n\ts_waitcnt vmcnt(5) lgkmcnt(10)\n\t" \
        "v_smfmac_f32_16x16x64_bf16 v[248:251], v[176:179], v[88:95], %[ix]\n\ts_waitcnt vmcnt(4) lgkmcnt(8)\n\tv_smfmac_f32_16x16x64_bf16 v[248:251], v[180:183], v[96:103], %[ix]\n\t" \
        "s_waitcnt vmcnt(3) lgkmcnt(6)\n\tv_smfmac_f32_16x16x64_bf16 v[248:251], v[184:187], v[104:111], %[ix]\n\ts_waitcnt vmcnt(2) lgkmcnt(4)\n\t" \
        "v_smfmac_f32_16x16x64_bf16 v[248:251], v[188:191], v[112:119], %[ix]\n\ts_waitcnt vmcnt(1) lgkmcnt(2)\n\tv_smfmac_f32_16x16x64_bf16 v[248:251], v[192:195], v[120:127], %[ix]\n\t" \
        "s_waitcnt vmcnt(0) lgkmcnt(0)\n\tv_smfmac_f32_16x16x64_bf16 v[248:251], v[196:199], v[128:135], %[ix]\n\ts_nop 7\n\tv_permlane32_swap_b32 v248, v250\n\tv_permlane32_swap_b32 v249, v251\n\t" \
        "v_pk_add_f32 v[248:249], v[248:249], v[250:251]\n\tv_cvt_pk_bf16_f32 v250, v248, v249\n\tv_cmp_u_f32 vcc, v248, v249\n\ts_nop 0\n\t" \
        "v_mov_b32_dpp v251, v250 quad_perm:[1,0,3,2] row_mask:0xf bank_mask:0xf bound_ctrl:1\n\tv_perm_b32 v248, v251, v250, %[sel]\n\ts_nop 1\n\t" \
        "v_mov_b32_dpp v249, v248 row_shl:2 row_mask:0xf bank_mask:0xf bound_ctrl:1\n\tv_mov_b32_dpp v250, v248 row_shl:4 row_mask:0xf bank_mask:0xf bound_ctrl:1\n\t" \
        "v_mov_b32_dpp v251, v248 row_shl:6 row_mask:0xf bank_mask:0xf bound_ctrl:1\n\ts_cbranch_vccnz 12f\n\t11:\nbuffer_store_dwordx4 v[248:251], %[mo], %[rs], %[mb] offen sc1\n\t15:\ns_barrier\n\t" \
        "v_add_u32 v136, %[vs0], %[l16]\n\tv_add_u32 v137, %[vs1], %[l16]\n\tv_add_u32 v138, %[vs2], %[l16]\n\tv_add_u32 v139, %[vs3], %[l16]\n\tds_write_b128 v136, v[216:219]\n\t" \
        "ds_write_b128 v136, v[220:223] offset:1024\n\tds_write_b128 v137, v[224:227]\n\tds_write_b128 v137, v[228:231] offset:1024\n\tds_write_b128 v138, v[232:235]\n\t" \
        "ds_write_b128 v138, v[236:239] offset:1024\n\tds_write_b128 v139, v[240:243]\n\tds_write_b128 v139, v[244:247] offset:1024\n\ts_branch 19f\n\t12:\ns_cmp_eq_u32 %[tn], 1\n\t" \
        "s_cbranch_scc1 13f\n\ts_add_u32 %[cnt], %[cnt], 1\n\ts_and_b32 vcc_lo, %[cnt], 0xffff\n\ts_cmp_gt_u32 vcc_lo, %[cap]\n\ts_cbranch_scc1 14f\n\ts_cmp_lt_u32 vcc_lo, 6\n\ts_cbranch_scc1 18f\n\t" \
        "v_max3_u32 v248, v136, v137, v137\n\tv_max3_u32 v248, v248, v138, v139\n\tv_max3_u32 v248, v248, v140, v141\n\tv_max3_u32 v248, v248, v142, v143\n\tv_max3_u32 v248, v248, v144, v145\n\t" \
        "v_max3_u32 v248, v248, v146, v147\n\tv_max3_u32 v248, v248, v148, v149\n\tv_max3_u32 v248, v248, v150, v151\n\tv_max3_u32 v248, v248, v152, v153\n\tv_max3_u32 v248, v248, v154, v155\n\t" \
        "v_max3_u32 v248, v248, v156, v157\n\tv_max3_u32 v248, v248, v158, v159\n\tv_max3_u32 v248, v248, v160, v161\n\tv_max3_u32 v248, v248, v162, v163\n\tv_max3_u32 v248, v248, v164, v165\n\t" \
        "v_max3_u32 v248, v248, v166, v167\n\tv_max3_u32 v248, v248, v168, v169\n\tv_max3_u32 v248, v248, v170, v171\n\tv_max3_u32 v248, v248, v172, v173\n\tv_max3_u32 v248, v248, v174, v175\n\t" \
        "v_max3_u32 v248, v248, v176, v177\n\tv_max3_u32 v248, v248, v178, v179\n\tv_max3_u32 v248, v248, v180, v181\n\tv_max3_u32 v248, v248, v182, v183\n\tv_max3_u32 v248, v248, v184, v185\n\t" \
        "v_max3_u32 v248, v248, v186, v187\n\tv_max3_u32 v248, v248, v188, v189\n\tv_max3_u32 v248, v248, v190, v191\n\tv_max3_u32 v248, v248, v192, v193\n\tv_max3_u32 v248, v248, v194, v195\n\t" \
        "v_max3_u32 v248, v248, v196, v197\n\tv_max3_u32 v248, v248, v198, v199\n\tv_max3_u32 v248, v248, v200, v201\n\tv_max3_u32 v248, v248, v202, v203\n\tv_max3_u32 v248, v248, v204, v205\n\t" \
        "v_max3_u32 v248, v248, v206, v207\n\tv_max3_u32 v248, v248, v208, v209\n\tv_max3_u32 v248, v248, v210, v211\n\tv_max3_u32 v248, v248, v212, v213\n\tv_max3_u32 v248, v248, v214, v215\n\t" \
        "v_max3_u32 v248, v248, v216, v217\n\tv_max3_u32 v248, v248, v218, v219\n\tv_max3_u32 v248, v248, v220, v221\n\tv_max3_u32 v248, v248, v222, v223\n\tv_max3_u32 v248, v248, v224, v225\n\t" \
        "v_max3_u32 v248, v248, v226, v227\n\tv_max3_u32 v248, v248, v228, v229\n\tv_max3_u32 v248, v248, v230, v231\n\tv_max3_u32 v248, v248, v232, v233\n\tv_max3_u32 v248, v248, v234, v235\n\t" \
        "v_max3_u32 v248, v248, v236, v237\n\tv_max3_u32 v248, v248, v238, v239\n\tv_max3_u32 v248, v248, v240, v241\n\tv_max3_u32 v248, v248, v242, v243\n\tv_max3_u32 v248, v248, v244, v245\n\t" \
        "v_max3_u32 v248, v248, v246, v247\n\tv_cmp_eq_u32 vcc, -1, v248\n\ts_cbranch_vccnz 18f\n\ts_mov_b32 %[tn], 1\n\ts_branch 9b\n\t14:\ns_mov_b32 %[cap], 0\n\ts_cmp_eq_u32 %[on], 0\n\t" \
        "s_cbranch_scc1 15b\n\t13:\nv_min_u32 v248, -2, v248\n\tv_min_u32 v249, -2, v249\n\tv_min_u32 v250, -2, v250\n\tv_min_u32 v251, -2, v251\n\ts_branch 11b\n\t18:\n" \
        "buffer_load_dwordx4 v[216:219], %[l16], %[rs], %[vb0] offen sc1\n\tbuffer_load_dwordx4 v[220:223], %[l16], %[rs], %[vb0] offen offset:1024 sc1\n\t" \
        "buffer_load_dwordx4 v[224:227], %[l16], %[rs], %[vb1] offen sc1\n\tbuffer_load_dwordx4 v[228:231], %[l16], %[rs], %[vb1] offen offset:1024 sc1\n\t" \
        "buffer_load_dwordx4 v[232:235], %[l16], %[rs], %[vb2] offen sc1\n\tbuffer_load_dwordx4 v[236:239], %[l16], %[rs], %[vb2] offen offset:1024 sc1\n\t" \
        "buffer_load_dwordx4 v[240:243], %[l16], %[rs], %[vb3] offen sc1\n\tbuffer_load_dwordx4 v[244:247], %[l16], %[rs], %[vb3] offen offset:1024 sc1\n\t" \
        "buffer_load_dwordx4 v[200:203], %[pm], %[rs], %[pb0] offen offset:1024 sc1\n\tbuffer_load_dwordx4 v[204:207], %[pm], %[rs], %[pb1] offen offset:1024 sc1\n\t" \
        "buffer_load_dwordx4 v[208:211], %[pm], %[rs], %[pb2] offen offset:1024 sc1\n\tbuffer_load_dwordx4 v[212:215], %[pm], %[rs], %[pb3] offen offset:1024 sc1\n\t" \
        "buffer_load_dwordx4 v[136:139], %[l16], %[rs], %[pb0] offen sc1\n\tbuffer_load_dwordx4 v[140:143], %[l16], %[rs], %[pb0] offen offset:1280 sc1\n\t" \
        "buffer_load_dwordx4 v[144:147], %[l16], %[rs], %[pb0] offen offset:2560 sc1\n\tbuffer_load_dwordx4 v[148:151], %[l16], %[rs], %[pb0] offen offset:3840 sc1\n\t" \
        "buffer_load_dwordx4 v[152:155], %[l16], %[rs], %[pb1] offen sc1\n\tbuffer_load_dwordx4 v[156:159], %[l16], %[rs], %[pb1] offen offset:1280 sc1\n\t" \
        "buffer_load_dwordx4 v[160:163], %[l16], %[rs], %[pb1] offen offset:2560 sc1\n\tbuffer_load_dwordx4 v[164:167], %[l16], %[rs], %[pb1] offen offset:3840 sc1\n\t" \
        "buffer_load_dwordx4 v[168:171], %[l16], %[rs], %[pb2] offen sc1\n\tbuffer_load_dwordx4 v[172:175], %[l16], %[rs], %[pb2] offen offset:1280 sc1\n\t" \
        "buffer_load_dwordx4 v[176:179], %[l16], %[rs], %[pb2] offen offset:2560 sc1\n\tbuffer_load_dwordx4 v[180:183], %[l16], %[rs], %[pb2] offen offset:3840 sc1\n\t" \
        "buffer_load_dwordx4 v[184:187], %[l16], %[rs], %[pb3] offen sc1\n\tbuffer_load_dwordx4 v[188:191], %[l16], %[rs], %[pb3] offen offset:1280 sc1\n\t" \
        "buffer_load_dwordx4 v[192:195], %[l16], %[rs], %[pb3] offen offset:2560 sc1\n\tbuffer_load_dwordx4 v[196:199], %[l16], %[rs], %[pb3] offen offset:3840 sc1\n\ts_branch 9b\n\t20:\n" \
        "ds_read_b32 v250, v248\n\ts_add_u32 %[tn], %[tn], 0x40000\n\ts_waitcnt lgkmcnt(0)\n\tv_readfirstlane_b32 s93, v250\n\ts_cbranch_scc1 21f\n\ts_cmp_lg_u32 s93, %[tg]\n\ts_cbranch_scc1 20b\n\t" \
        "21:\nds_read_b128 v[8:11], v36\n\tds_read_b128 v[12:15], v36 offset:1024\n\tds_read_b128 v[16:19], v36 offset:2048\n\tds_read_b128 v[20:23], v36 offset:3072\n\t" \
        "ds_read_b128 v[24:27], v36 offset:4096\n\tds_read_b128 v[28:31], v36 offset:5120\n\tds_read_b128 v[32:35], v36 offset:6144\n\tds_read_b128 v[36:39], v36 offset:7168\n\t" \
        "ds_read_b128 v[40:43], v68\n\tds_read_b128 v[44:47], v68 offset:1024\n\tds_read_b128 v[48:51], v68 offset:2048\n\tds_read_b128 v[52:55], v68 offset:3072\n\t" \
        "ds_read_b128 v[56:59], v68 offset:4096\n\tds_read_b128 v[60:63], v68 offset:5120\n\tds_read_b128 v[64:67], v68 offset:6144\n\tds_read_b128 v[68:71], v68 offset:7168\n\t" \
        "ds_read_b128 v[72:75], v100\n\tds_read_b128 v[76:79], v100 offset:1024\n\tds_read_b128 v[80:83], v100 offset:2048\n\tds_read_b128 v[84:87], v100 offset:3072\n\t" \
        "ds_read_b128 v[88:91], v100 offset:4096\n\tds_read_b128 v[92:95], v100 offset:5120\n\tds_read_b128 v[96:99], v100 offset:6144\n\tds_read_b128 v[100:103], v100 offset:7168\n\t" \
        "ds_read_b128 v[104:107], v132\n\tds_read_b128 v[108:111], v132 offset:1024\n\tds_read_b128 v[112:115], v132 offset:2048\n\tds_read_b128 v[116:119], v132 offset:3072\n\t" \
        "ds_read_b128 v[120:123], v132 offset:4096\n\tds_read_b128 v[124:127], v132 offset:5120\n\tds_read_b128 v[128:131], v132 offset:6144\n\tds_read_b128 v[132:135], v132 offset:7168\n\t" \
        "s_mov_b32 %[tn], 0\n\ts_branch 9b\n\t17:\ns_waitcnt vmcnt(0) lgkmcnt(0)\n\tv_max3_u32 v248, v216, v217, v217\n\tv_max3_u32 v248, v248, v218, v219\n\tv_max3_u32 v248, v248, v220, v221\n\t" \
        "v_max3_u32 v248, v248, v222, v223\n\tv_max3_u32 v248, v248, v224, v225\n\tv_max3_u32 v248, v248, v226, v227\n\tv_max3_u32 v248, v248, v228, v229\n\tv_max3_u32 v248, v248, v230, v231\n\t" \
        "v_max3_u32 v248, v248, v232, v233\n\tv_max3_u32 v248, v248, v234, v235\n\tv_max3_u32 v248, v248, v236, v237\n\tv_max3_u32 v248, v248, v238, v239\n\tv_max3_u32 v248, v248, v240, v241\n\t" \
        "v_max3_u32 v248, v248, v242, v243\n\tv_max3_u32 v248, v248, v244, v245\n\tv_max3_u32 v248, v248, v246, v247\n\tv_cmp_eq_u32 vcc, -1, v248\n\ts_cbranch_vccz 15b\n\ts_branch 12b\n\t2:\n" \
        "s_add_u32 vcc_hi, %[ab], 704\n\t3:\ns_waitcnt lgkmcnt(0)\n\ts_andn2_b32 s95, %[aw], 63\n\ts_cmp_lg_u32 %[aw], -1\n\ts_cbranch_scc1 4b\n\ts_cmp_eq_u32 s94, vcc_hi\n\ts_cbranch_scc1 1f\n\t" \
        "s_add_u32 s94, s94, 64\n\ts_buffer_load_dword %[aw], %[rs], s94\n\ts_branch 3b\n\t1:\ns_memrealtime vcc\n\ts_waitcnt lgkmcnt(0)\n\ts_lshl_b32 s95, vcc_lo, 6\n\t" \
        "s_sub_u32 s95, s95, 0x40000\n\ts_branch 4b\n\t19:\ns_waitcnt lgkmcnt(0)\n\t"
#define ASM_ATT \
        "s_mov_b32 %[keep], m0\n\ts_mov_b32 %[sk], 0\n\tv_mov_b32 v187, %[fo]\n\tv_mov_b64 v[128:129], 0\n\tv_mov_b64 v[130:131], 0\n\tv_mov_b64 v[132:133], 0\n\tv_mov_b64 v[134:135], 0\n\t" \
        "v_mov_b64 v[136:137], 0\n\tv_mov_b64 v[138:139], 0\n\tv_mov_b64 v[140:141], 0\n\tv_mov_b64 v[142:143], 0\n\tv_mov_b64 v[184:185], 0\n\ts_cmp_lg_u32 %[rem], 0\n\ts_cbranch_scc1 1f\n\t" \
        "v_mov_b32 v190, 0xff800000\n\tv_cmp_le_i32 vcc, 0, %[dq]\n\ts_nop 1\n\tv_cndmask_b32 v128, v190, v128, vcc\n\tv_cmp_le_i32 vcc, 1, %[dq]\n\ts_nop 1\n\tv_cndmask_b32 v129, v190, v129, vcc\n\t" \
        "v_cmp_le_i32 vcc, 2, %[dq]\n\ts_nop 1\n\tv_cndmask_b32 v130, v190, v130, vcc\n\tv_cmp_le_i32 vcc, 3, %[dq]\n\ts_nop 1\n\tv_cndmask_b32 v131, v190, v131, vcc\n\tv_cmp_le_i32 vcc, 8, %[dq]\n\t" \
        "s_nop 1\n\tv_cndmask_b32 v132, v190, v132, vcc\n\tv_cmp_le_i32 vcc, 9, %[dq]\n\ts_nop 1\n\tv_cndmask_b32 v133, v190, v133, vcc\n\tv_cmp_le_i32 vcc, 10, %[dq]\n\ts_nop 1\n\t" \
        "v_cndmask_b32 v134, v190, v134, vcc\n\tv_cmp_le_i32 vcc, 11, %[dq]\n\ts_nop 1\n\tv_cndmask_b32 v135, v190, v135, vcc\n\tv_cmp_le_i32 vcc, 16, %[dq]\n\ts_nop 1\n\t" \
        "v_cndmask_b32 v136, v190, v136, vcc\n\tv_cmp_le_i32 vcc, 17, %[dq]\n\ts_nop 1\n\tv_cndmask_b32 v137, v190, v137, vcc\n\tv_cmp_le_i32 vcc, 18, %[dq]\n\ts_nop 1\n\t" \
        "v_cndmask_b32 v138, v190, v138, vcc\n\tv_cmp_le_i32 vcc, 19, %[dq]\n\ts_nop 1\n\tv_cndmask_b32 v139, v190, v139, vcc\n\tv_cmp_le_i32 vcc, 24, %[dq]\n\ts_nop 1\n\t" \
        "v_cndmask_b32 v140, v190, v140, vcc\n\tv_cmp_le_i32 vcc, 25, %[dq]\n\ts_nop 1\n\tv_cndmask_b32 v141, v190, v141, vcc\n\tv_cmp_le_i32 vcc, 26, %[dq]\n\ts_nop 1\n\t" \
        "v_cndmask_b32 v142, v190, v142, vcc\n\tv_cmp_le_i32 vcc, 27, %[dq]\n\ts_nop 1\n\tv_cndmask_b32 v143, v190, v143, vcc\n\ts_nop 1\n\t1:\ns_barrier\n\tds_read_b128 v[152:155], v187 offset:0\n\t" \
        "ds_read_b128 v[156:159], v187 offset:512\n\tds_read_b128 v[160:163], v187 offset:2048\n\tds_read_b128 v[164:167], v187 offset:2560\n\ts_waitcnt lgkmcnt(3)\n\t" \
        "v_mfma_f32_32x32x16_bf16 v[96:111], v[152:155], v[64:67], v[128:143]\n\tds_read_b128 v[168:171], v187 offset:4096\n\tv_mov_b64 v[0:1], 0\n\tv_mov_b64 v[2:3], 0\n\tv_mov_b64 v[4:5], 0\n\t" \
        "v_mov_b64 v[6:7], 0\n\ts_waitcnt lgkmcnt(3)\n\tv_mfma_f32_32x32x16_bf16 v[96:111], v[156:159], v[68:71], v[96:111]\n\tds_read_b128 v[172:175], v187 offset:4608\n\tv_mov_b64 v[8:9], 0\n\t" \
        "v_mov_b64 v[10:11], 0\n\tv_mov_b64 v[12:13], 0\n\tv_mov_b64 v[14:15], 0\n\ts_waitcnt lgkmcnt(3)\n\tv_mfma_f32_32x32x16_bf16 v[96:111], v[160:163], v[72:75], v[96:111]\n\t" \
        "ds_read_b128 v[176:179], v187 offset:6144\n\tv_mov_b64 v[16:17], 0\n\tv_mov_b64 v[18:19], 0\n\tv_mov_b64 v[20:21], 0\n\tv_mov_b64 v[22:23], 0\n\ts_waitcnt lgkmcnt(3)\n\t" \
        "v_mfma_f32_32x32x16_bf16 v[96:111], v[164:167], v[76:79], v[96:111]\n\tds_read_b128 v[180:183], v187 offset:6656\n\tv_mov_b64 v[24:25], 0\n\tv_mov_b64 v[26:27], 0\n\t" \
        "v_mov_b64 v[28:29], 0\n\tv_mov_b64 v[30:31], 0\n\ts_waitcnt lgkmcnt(3)\n\tv_mfma_f32_32x32x16_bf16 v[96:111], v[168:171], v[80:83], v[96:111]\n\tds_read_b128 v[152:155], v187 offset:8192\n\t" \
        "v_mov_b64 v[32:33], 0\n\tv_mov_b64 v[34:35], 0\n\tv_mov_b64 v[36:37], 0\n\tv_mov_b64 v[38:39], 0\n\ts_waitcnt lgkmcnt(3)\n\t" \
        "v_mfma_f32_32x32x16_bf16 v[96:111], v[172:175], v[84:87], v[96:111]\n\tds_read_b128 v[156:159], v187 offset:8704\n\tv_mov_b64 v[40:41], 0\n\tv_mov_b64 v[42:43], 0\n\t" \
        "v_mov_b64 v[44:45], 0\n\tv_mov_b64 v[46:47], 0\n\ts_waitcnt lgkmcnt(3)\n\tv_mfma_f32_32x32x16_bf16 v[96:111], v[176:179], v[88:91], v[96:111]\n\t" \
        "ds_read_b128 v[160:163], v187 offset:10240\n\tv_mov_b64 v[48:49], 0\n\tv_mov_b64 v[50:51], 0\n\tv_mov_b64 v[52:53], 0\n\tv_mov_b64 v[54:55], 0\n\ts_waitcnt lgkmcnt(3)\n\t" \
        "v_mfma_f32_32x32x16_bf16 v[96:111], v[180:183], v[92:95], v[96:111]\n\tds_read_b128 v[164:167], v187 offset:10752\n\tv_mov_b64 v[56:57], 0\n\tv_mov_b64 v[58:59], 0\n\t" \
        "v_mov_b64 v[60:61], 0\n\tv_mov_b64 v[62:63], 0\n\ts_nop 6\n\tv_max3_f32 v186, v96, v97, v98\n\tv_max3_f32 v186, v186, v99, v100\n\tv_max3_f32 v186, v186, v101, v102\n\t" \
        "v_max3_f32 v186, v186, v103, v104\n\tv_max3_f32 v186, v186, v105, v106\n\tv_max3_f32 v186, v186, v107, v108\n\tv_max3_f32 v186, v186, v109, v110\n\tv_max_f32 v186, v186, v111\n\t" \
        "v_mov_b32 v190, v186\n\ts_nop 1\n\tv_permlane32_swap_b32 v186, v190\n\ts_nop 1\n\tv_max_f32 v186, v186, v190\n\tv_sub_f32 v128, v128, v186\n\tv_sub_f32 v129, v129, v186\n\t" \
        "v_sub_f32 v130, v130, v186\n\tv_sub_f32 v131, v131, v186\n\tv_sub_f32 v132, v132, v186\n\tv_sub_f32 v133, v133, v186\n\tv_sub_f32 v134, v134, v186\n\tv_sub_f32 v135, v135, v186\n\t" \
        "v_sub_f32 v136, v136, v186\n\tv_sub_f32 v137, v137, v186\n\tv_sub_f32 v138, v138, v186\n\tv_sub_f32 v139, v139, v186\n\tv_sub_f32 v140, v140, v186\n\tv_sub_f32 v141, v141, v186\n\t" \
        "v_sub_f32 v142, v142, v186\n\tv_sub_f32 v143, v143, v186\n\tv_sub_f32 v96, v96, v186\n\tv_sub_f32 v97, v97, v186\n\tv_sub_f32 v98, v98, v186\n\tv_sub_f32 v99, v99, v186\n\t" \
        "v_sub_f32 v100, v100, v186\n\tv_sub_f32 v101, v101, v186\n\tv_sub_f32 v102, v102, v186\n\tv_sub_f32 v103, v103, v186\n\tv_sub_f32 v104, v104, v186\n\tv_sub_f32 v105, v105, v186\n\t" \
        "v_sub_f32 v106, v106, v186\n\tv_sub_f32 v107, v107, v186\n\tv_sub_f32 v108, v108, v186\n\tv_sub_f32 v109, v109, v186\n\tv_sub_f32 v110, v110, v186\n\tv_sub_f32 v111, v111, v186\n\t" \
        "s_branch 40f\n\t2:\ns_waitcnt lgkmcnt(6)\n\tv_mfma_f32_32x32x16_bf16 v[0:15], v[152:155], v[144:147], v[0:15]\n\tds_read_b64_tr_b16 v[168:169], v188 offset:9216\n\t" \
        "ds_read_b64_tr_b16 v[170:171], v189 offset:9216\n\tv_cvt_pk_bf16_f32 v148, v120, v121\n\tv_exp_f32 v123, v123\n\tv_exp_f32 v124, v124\n\ts_waitcnt lgkmcnt(6)\n\t" \
        "v_mfma_f32_32x32x16_bf16 v[16:31], v[156:159], v[144:147], v[16:31]\n\tds_read_b64_tr_b16 v[172:173], v188 offset:11264\n\tds_read_b64_tr_b16 v[174:175], v189 offset:11264\n\t" \
        "v_cvt_pk_bf16_f32 v149, v122, v123\n\tv_exp_f32 v125, v125\n\ts_waitcnt lgkmcnt(6)\n\tv_mfma_f32_32x32x16_bf16 v[32:47], v[160:163], v[144:147], v[32:47]\n\t" \
        "ds_read_b64_tr_b16 v[176:177], v188 offset:13312\n\tds_read_b64_tr_b16 v[178:179], v189 offset:13312\n\tv_exp_f32 v126, v126\n\tv_cvt_pk_bf16_f32 v150, v124, v125\n\tv_exp_f32 v127, v127\n\t" \
        "s_waitcnt lgkmcnt(6)\n\tv_mfma_f32_32x32x16_bf16 v[48:63], v[164:167], v[144:147], v[48:63]\n\tds_read_b64_tr_b16 v[180:181], v188 offset:15360\n\t" \
        "ds_read_b64_tr_b16 v[182:183], v189 offset:15360\n\tv_add_f32 v185, v185, v119\n\tv_cvt_pk_bf16_f32 v151, v126, v127\n\tv_add_f32 v184, v184, v120\n\tv_add_f32 v185, v185, v121\n\t" \
        "s_waitcnt lgkmcnt(6)\n\tv_mfma_f32_32x32x16_bf16 v[0:15], v[168:171], v[148:151], v[0:15]\n\tds_read_b128 v[152:155], v187 offset:8192\n\tv_max3_f32 v186, v96, v97, v98\n\t" \
        "v_add_f32 v184, v184, v122\n\tv_max3_f32 v186, v186, v99, v100\n\tv_max3_f32 v186, v186, v101, v102\n\ts_waitcnt lgkmcnt(5)\n\t" \
        "v_mfma_f32_32x32x16_bf16 v[16:31], v[172:175], v[148:151], v[16:31]\n\tds_read_b128 v[156:159], v187 offset:8704\n\tv_add_f32 v185, v185, v123\n\tv_max3_f32 v186, v186, v103, v104\n\t" \
        "v_max3_f32 v186, v186, v105, v106\n\ts_waitcnt lgkmcnt(4)\n\tv_mfma_f32_32x32x16_bf16 v[32:47], v[176:179], v[148:151], v[32:47]\n\tds_read_b128 v[160:163], v187 offset:10240\n\t" \
        "v_add_f32 v184, v184, v124\n\tv_max3_f32 v186, v186, v107, v108\n\tv_max3_f32 v186, v186, v109, v110\n\tv_add_f32 v185, v185, v125\n\ts_waitcnt lgkmcnt(3)\n\t" \
        "v_mfma_f32_32x32x16_bf16 v[48:63], v[180:183], v[148:151], v[48:63]\n\tds_read_b128 v[164:167], v187 offset:10752\n\tv_max_f32 v186, v186, v111\n\tv_add_f32 v184, v184, v126\n\t" \
        "v_cmp_lt_f32 vcc, 0x41000000, v186\n\tv_add_f32 v185, v185, v127\n\ts_cbranch_vccnz 41f\n\t40:\ns_cmp_lt_i32 %[tb], 1\n\ts_cbranch_scc1 31f\n\ts_waitcnt vmcnt(6)\n\ts_barrier\n\t30:\n" \
        "s_cmp_gt_i32 %[tb], 0\n\ts_cselect_b64 vcc, -1, 0\n\ts_sub_u32 %[tb], %[tb], 1\n\ts_cmp_lt_u32 %[rem], 2\n\ts_cbranch_scc1 43f\n\t42:\ns_sub_u32 %[rem], %[rem], 1\n\ts_waitcnt lgkmcnt(3)\n\t" \
        "v_mfma_f32_32x32x16_bf16 v[112:127], v[152:155], v[64:67], v[128:143]\n\tds_read_b128 v[168:171], v187 offset:12288\n\tv_exp_f32 v96, v96\n\tv_exp_f32 v97, v97\n\ts_waitcnt lgkmcnt(3)\n\t" \
        "v_mfma_f32_32x32x16_bf16 v[112:127], v[156:159], v[68:71], v[112:127]\n\tds_read_b128 v[172:175], v187 offset:12800\n\tv_add_f32 v184, v184, v96\n\tv_exp_f32 v98, v98\n\t" \
        "v_add_f32 v185, v185, v97\n\ts_cbranch_vccz 32f\n\ts_sub_u32 %[t0], %[sk], 0x8000\n\ts_and_b32 %[t0], %[t0], 0x18000\n\ts_add_u32 m0, %[t0], %[lb]\n\ts_add_u32 %[t0], %[so], 0x1000000\n\t" \
        "buffer_load_dwordx4 %[ko], %[rs], %[so] offen sc1 lds\n\t32:\ns_waitcnt lgkmcnt(3)\n\tv_mfma_f32_32x32x16_bf16 v[112:127], v[160:163], v[72:75], v[112:127]\n\t" \
        "ds_read_b128 v[176:179], v187 offset:14336\n\tv_exp_f32 v99, v99\n\tv_add_f32 v184, v184, v98\n\tv_exp_f32 v100, v100\n\ts_waitcnt lgkmcnt(3)\n\t" \
        "v_mfma_f32_32x32x16_bf16 v[112:127], v[164:167], v[76:79], v[112:127]\n\tds_read_b128 v[180:183], v187 offset:14848\n\tv_cvt_pk_bf16_f32 v144, v96, v97\n\tv_exp_f32 v101, v101\n\t" \
        "s_cbranch_vccz 33f\n\tbuffer_load_dwordx4 %[ko], %[rs], %[so] offen offset:1024 sc1 lds\n\t33:\ns_waitcnt lgkmcnt(3)\n\t" \
        "v_mfma_f32_32x32x16_bf16 v[112:127], v[168:171], v[80:83], v[112:127]\n\tv_add_u32 v188, %[sk], %[vb]\n\tv_xor_b32 v189, 0x80, v188\n\tds_read_b64_tr_b16 v[152:153], v188 offset:0\n\t" \
        "ds_read_b64_tr_b16 v[154:155], v189 offset:0\n\tv_add_f32 v185, v185, v99\n\tv_cvt_pk_bf16_f32 v145, v98, v99\n\tv_exp_f32 v102, v102\n\tv_add_f32 v184, v184, v100\n\t" \
        "s_waitcnt lgkmcnt(4)\n\tv_mfma_f32_32x32x16_bf16 v[112:127], v[172:175], v[84:87], v[112:127]\n\tds_read_b64_tr_b16 v[156:157], v188 offset:2048\n\t" \
        "ds_read_b64_tr_b16 v[158:159], v189 offset:2048\n\tv_cvt_pk_bf16_f32 v146, v100, v101\n\tv_exp_f32 v103, v103\n\tv_add_f32 v185, v185, v101\n\ts_cbranch_vccz 34f\n\t" \
        "s_add_u32 m0, m0, 0x4000\n\ts_add_u32 %[so], %[so], 0x80000\n\tbuffer_load_dwordx4 %[vo], %[rs], %[t0] offen sc1 lds\n\t34:\ns_waitcnt lgkmcnt(5)\n\t" \
        "v_mfma_f32_32x32x16_bf16 v[112:127], v[176:179], v[88:91], v[112:127]\n\tds_read_b64_tr_b16 v[160:161], v188 offset:4096\n\tds_read_b64_tr_b16 v[162:163], v189 offset:4096\n\t" \
        "v_exp_f32 v104, v104\n\tv_add_f32 v184, v184, v102\n\tv_cvt_pk_bf16_f32 v147, v102, v103\n\ts_waitcnt lgkmcnt(6)\n\tv_mfma_f32_32x32x16_bf16 v[112:127], v[180:183], v[92:95], v[112:127]\n\t" \
        "ds_read_b64_tr_b16 v[164:165], v188 offset:6144\n\tds_read_b64_tr_b16 v[166:167], v189 offset:6144\n\tv_exp_f32 v105, v105\n\tv_exp_f32 v106, v106\n\ts_cbranch_vccz 35f\n\t" \
        "buffer_load_dwordx4 %[vo], %[rs], %[t0] offen offset:1024 sc1 lds\n\t35:\ns_add_u32 %[sk], %[sk], 0x8000\n\ts_and_b32 %[sk], %[sk], 0x18000\n\ts_waitcnt lgkmcnt(6)\n\t" \
        "v_mfma_f32_32x32x16_bf16 v[0:15], v[152:155], v[144:147], v[0:15]\n\tds_read_b64_tr_b16 v[168:169], v188 offset:1024\n\tds_read_b64_tr_b16 v[170:171], v189 offset:1024\n\t" \
        "v_cvt_pk_bf16_f32 v148, v104, v105\n\tv_exp_f32 v107, v107\n\tv_exp_f32 v108, v108\n\ts_waitcnt lgkmcnt(6)\n\tv_mfma_f32_32x32x16_bf16 v[16:31], v[156:159], v[144:147], v[16:31]\n\t" \
        "ds_read_b64_tr_b16 v[172:173], v188 offset:3072\n\tds_read_b64_tr_b16 v[174:175], v189 offset:3072\n\tv_cvt_pk_bf16_f32 v149, v106, v107\n\tv_exp_f32 v109, v109\n\ts_waitcnt lgkmcnt(6)\n\t" \
        "v_mfma_f32_32x32x16_bf16 v[32:47], v[160:163], v[144:147], v[32:47]\n\tds_read_b64_tr_b16 v[176:177], v188 offset:5120\n\tds_read_b64_tr_b16 v[178:179], v189 offset:5120\n\t" \
        "v_exp_f32 v110, v110\n\tv_cvt_pk_bf16_f32 v150, v108, v109\n\tv_exp_f32 v111, v111\n\ts_waitcnt lgkmcnt(6)\n\tv_mfma_f32_32x32x16_bf16 v[48:63], v[164:167], v[144:147], v[48:63]\n\t" \
        "ds_read_b64_tr_b16 v[180:181], v188 offset:7168\n\tds_read_b64_tr_b16 v[182:183], v189 offset:7168\n\tv_add_f32 v185, v185, v103\n\tv_cvt_pk_bf16_f32 v151, v110, v111\n\t" \
        "v_add_f32 v184, v184, v104\n\tv_add_f32 v185, v185, v105\n\ts_waitcnt lgkmcnt(6)\n\tv_mfma_f32_32x32x16_bf16 v[0:15], v[168:171], v[148:151], v[0:15]\n\tv_add_u32 v187, %[sk], %[fo]\n\t" \
        "ds_read_b128 v[152:155], v187 offset:0\n\tv_max3_f32 v186, v112, v113, v114\n\tv_add_f32 v184, v184, v106\n\tv_max3_f32 v186, v186, v115, v116\n\tv_max3_f32 v186, v186, v117, v118\n\t" \
        "s_waitcnt lgkmcnt(5)\n\tv_mfma_f32_32x32x16_bf16 v[16:31], v[172:175], v[148:151], v[16:31]\n\tds_read_b128 v[156:159], v187 offset:512\n\tv_add_f32 v185, v185, v107\n\t" \
        "v_max3_f32 v186, v186, v119, v120\n\tv_max3_f32 v186, v186, v121, v122\n\ts_waitcnt lgkmcnt(4)\n\tv_mfma_f32_32x32x16_bf16 v[32:47], v[176:179], v[148:151], v[32:47]\n\t" \
        "ds_read_b128 v[160:163], v187 offset:2048\n\tv_add_f32 v184, v184, v108\n\tv_max3_f32 v186, v186, v123, v124\n\tv_max3_f32 v186, v186, v125, v126\n\tv_add_f32 v185, v185, v109\n\t" \
        "s_waitcnt lgkmcnt(3)\n\tv_mfma_f32_32x32x16_bf16 v[48:63], v[180:183], v[148:151], v[48:63]\n\tds_read_b128 v[164:167], v187 offset:2560\n\tv_max_f32 v186, v186, v127\n\t" \
        "v_add_f32 v184, v184, v110\n\tv_cmp_lt_f32 vcc, 0x41000000, v186\n\tv_add_f32 v185, v185, v111\n\ts_cbranch_vccnz 61f\n\t60:\ns_cmp_lt_u32 %[rem], 2\n\ts_cbranch_scc1 63f\n\t62:\n" \
        "s_sub_u32 %[rem], %[rem], 1\n\ts_waitcnt lgkmcnt(3)\n\tv_mfma_f32_32x32x16_bf16 v[96:111], v[152:155], v[64:67], v[128:143]\n\tds_read_b128 v[168:171], v187 offset:4096\n\t" \
        "v_exp_f32 v112, v112\n\tv_exp_f32 v113, v113\n\ts_waitcnt lgkmcnt(3)\n\tv_mfma_f32_32x32x16_bf16 v[96:111], v[156:159], v[68:71], v[96:111]\n\tds_read_b128 v[172:175], v187 offset:4608\n\t" \
        "v_add_f32 v184, v184, v112\n\tv_exp_f32 v114, v114\n\tv_add_f32 v185, v185, v113\n\ts_waitcnt lgkmcnt(3)\n\tv_mfma_f32_32x32x16_bf16 v[96:111], v[160:163], v[72:75], v[96:111]\n\t" \
        "ds_read_b128 v[176:179], v187 offset:6144\n\tv_exp_f32 v115, v115\n\tv_add_f32 v184, v184, v114\n\tv_exp_f32 v116, v116\n\ts_waitcnt lgkmcnt(3)\n\t" \
        "v_mfma_f32_32x32x16_bf16 v[96:111], v[164:167], v[76:79], v[96:111]\n\tds_read_b128 v[180:183], v187 offset:6656\n\tv_cvt_pk_bf16_f32 v144, v112, v113\n\tv_exp_f32 v117, v117\n\t" \
        "s_waitcnt lgkmcnt(3)\n\tv_mfma_f32_32x32x16_bf16 v[96:111], v[168:171], v[80:83], v[96:111]\n\tds_read_b64_tr_b16 v[152:153], v188 offset:8192\n\t" \
        "ds_read_b64_tr_b16 v[154:155], v189 offset:8192\n\tv_add_f32 v185, v185, v115\n\tv_cvt_pk_bf16_f32 v145, v114, v115\n\tv_exp_f32 v118, v118\n\tv_add_f32 v184, v184, v116\n\t" \
        "s_waitcnt lgkmcnt(4)\n\tv_mfma_f32_32x32x16_bf16 v[96:111], v[172:175], v[84:87], v[96:111]\n\tds_read_b64_tr_b16 v[156:157], v188 offset:10240\n\t" \
        "ds_read_b64_tr_b16 v[158:159], v189 offset:10240\n\tv_cvt_pk_bf16_f32 v146, v116, v117\n\tv_exp_f32 v119, v119\n\tv_add_f32 v185, v185, v117\n\ts_waitcnt lgkmcnt(5)\n\t" \
        "v_mfma_f32_32x32x16_bf16 v[96:111], v[176:179], v[88:91], v[96:111]\n\tds_read_b64_tr_b16 v[160:161], v188 offset:12288\n\tds_read_b64_tr_b16 v[162:163], v189 offset:12288\n\t" \
        "v_exp_f32 v120, v120\n\tv_add_f32 v184, v184, v118\n\tv_cvt_pk_bf16_f32 v147, v118, v119\n\ts_waitcnt lgkmcnt(6)\n\tv_mfma_f32_32x32x16_bf16 v[96:111], v[180:183], v[92:95], v[96:111]\n\t" \
        "ds_read_b64_tr_b16 v[164:165], v188 offset:14336\n\tds_read_b64_tr_b16 v[166:167], v189 offset:14336\n\tv_exp_f32 v121, v121\n\tv_exp_f32 v122, v122\n\ts_branch 2b\n\t31:\n" \
        "s_cmp_lt_i32 %[tb], 0\n\ts_cbranch_scc1 30b\n\ts_waitcnt vmcnt(0)\n\ts_barrier\n\ts_branch 30b\n\t41:\nv_mov_b32 v190, v186\n\ts_nop 1\n\tv_permlane32_swap_b32 v186, v190\n\ts_nop 1\n\t" \
        "v_max_f32 v186, v186, v190\n\tv_max_f32 v186, 0, v186\n\tv_sub_f32 v128, v128, v186\n\tv_sub_f32 v129, v129, v186\n\tv_sub_f32 v130, v130, v186\n\tv_sub_f32 v131, v131, v186\n\t" \
        "v_sub_f32 v132, v132, v186\n\tv_sub_f32 v133, v133, v186\n\tv_sub_f32 v134, v134, v186\n\tv_sub_f32 v135, v135, v186\n\tv_sub_f32 v136, v136, v186\n\tv_sub_f32 v137, v137, v186\n\t" \
        "v_sub_f32 v138, v138, v186\n\tv_sub_f32 v139, v139, v186\n\tv_sub_f32 v140, v140, v186\n\tv_sub_f32 v141, v141, v186\n\tv_sub_f32 v142, v142, v186\n\tv_sub_f32 v143, v143, v186\n\t" \
        "v_sub_f32 v96, v96, v186\n\tv_sub_f32 v97, v97, v186\n\tv_sub_f32 v98, v98, v186\n\tv_sub_f32 v99, v99, v186\n\tv_sub_f32 v100, v100, v186\n\tv_sub_f32 v101, v101, v186\n\t" \
        "v_sub_f32 v102, v102, v186\n\tv_sub_f32 v103, v103, v186\n\tv_sub_f32 v104, v104, v186\n\tv_sub_f32 v105, v105, v186\n\tv_sub_f32 v106, v106, v186\n\tv_sub_f32 v107, v107, v186\n\t" \
        "v_sub_f32 v108, v108, v186\n\tv_sub_f32 v109, v109, v186\n\tv_sub_f32 v110, v110, v186\n\tv_sub_f32 v111, v111, v186\n\tv_exp_f32 v190, -v186\n\ts_nop 0\n\tv_mul_f32 v184, v184, v190\n\t" \
        "v_mul_f32 v185, v185, v190\n\tv_mul_f32 v0, v0, v190\n\tv_mul_f32 v1, v1, v190\n\tv_mul_f32 v2, v2, v190\n\tv_mul_f32 v3, v3, v190\n\tv_mul_f32 v4, v4, v190\n\tv_mul_f32 v5, v5, v190\n\t" \
        "v_mul_f32 v6, v6, v190\n\tv_mul_f32 v7, v7, v190\n\tv_mul_f32 v8, v8, v190\n\tv_mul_f32 v9, v9, v190\n\tv_mul_f32 v10, v10, v190\n\tv_mul_f32 v11, v11, v190\n\tv_mul_f32 v12, v12, v190\n\t" \
        "v_mul_f32 v13, v13, v190\n\tv_mul_f32 v14, v14, v190\n\tv_mul_f32 v15, v15, v190\n\tv_mul_f32 v16, v16, v190\n\tv_mul_f32 v17, v17, v190\n\tv_mul_f32 v18, v18, v190\n\t" \
        "v_mul_f32 v19, v19, v190\n\tv_mul_f32 v20, v20, v190\n\tv_mul_f32 v21, v21, v190\n\tv_mul_f32 v22, v22, v190\n\tv_mul_f32 v23, v23, v190\n\tv_mul_f32 v24, v24, v190\n\t" \
        "v_mul_f32 v25, v25, v190\n\tv_mul_f32 v26, v26, v190\n\tv_mul_f32 v27, v27, v190\n\tv_mul_f32 v28, v28, v190\n\tv_mul_f32 v29, v29, v190\n\tv_mul_f32 v30, v30, v190\n\t" \
        "v_mul_f32 v31, v31, v190\n\tv_mul_f32 v32, v32, v190\n\tv_mul_f32 v33, v33, v190\n\tv_mul_f32 v34, v34, v190\n\tv_mul_f32 v35, v35, v190\n\tv_mul_f32 v36, v36, v190\n\t" \
        "v_mul_f32 v37, v37, v190\n\tv_mul_f32 v38, v38, v190\n\tv_mul_f32 v39, v39, v190\n\tv_mul_f32 v40, v40, v190\n\tv_mul_f32 v41, v41, v190\n\tv_mul_f32 v42, v42, v190\n\t" \
        "v_mul_f32 v43, v43, v190\n\tv_mul_f32 v44, v44, v190\n\tv_mul_f32 v45, v45, v190\n\tv_mul_f32 v46, v46, v190\n\tv_mul_f32 v47, v47, v190\n\tv_mul_f32 v48, v48, v190\n\t" \
        "v_mul_f32 v49, v49, v190\n\tv_mul_f32 v50, v50, v190\n\tv_mul_f32 v51, v51, v190\n\tv_mul_f32 v52, v52, v190\n\tv_mul_f32 v53, v53, v190\n\tv_mul_f32 v54, v54, v190\n\t" \
        "v_mul_f32 v55, v55, v190\n\tv_mul_f32 v56, v56, v190\n\tv_mul_f32 v57, v57, v190\n\tv_mul_f32 v58, v58, v190\n\tv_mul_f32 v59, v59, v190\n\tv_mul_f32 v60, v60, v190\n\t" \
        "v_mul_f32 v61, v61, v190\n\tv_mul_f32 v62, v62, v190\n\tv_mul_f32 v63, v63, v190\n\ts_branch 40b\n\t43:\ns_cmp_eq_u32 %[rem], 0\n\ts_cbranch_scc1 44f\n\ts_mov_b64 %[sv], vcc\n\t" \
        "v_mov_b32 v190, 0xff800000\n\tv_cmp_le_i32 vcc, 0, %[dq]\n\ts_nop 1\n\tv_cndmask_b32 v128, v190, v128, vcc\n\tv_cmp_le_i32 vcc, 1, %[dq]\n\ts_nop 1\n\tv_cndmask_b32 v129, v190, v129, vcc\n\t" \
        "v_cmp_le_i32 vcc, 2, %[dq]\n\ts_nop 1\n\tv_cndmask_b32 v130, v190, v130, vcc\n\tv_cmp_le_i32 vcc, 3, %[dq]\n\ts_nop 1\n\tv_cndmask_b32 v131, v190, v131, vcc\n\tv_cmp_le_i32 vcc, 8, %[dq]\n\t" \
        "s_nop 1\n\tv_cndmask_b32 v132, v190, v132, vcc\n\tv_cmp_le_i32 vcc, 9, %[dq]\n\ts_nop 1\n\tv_cndmask_b32 v133, v190, v133, vcc\n\tv_cmp_le_i32 vcc, 10, %[dq]\n\ts_nop 1\n\t" \
        "v_cndmask_b32 v134, v190, v134, vcc\n\tv_cmp_le_i32 vcc, 11, %[dq]\n\ts_nop 1\n\tv_cndmask_b32 v135, v190, v135, vcc\n\tv_cmp_le_i32 vcc, 16, %[dq]\n\ts_nop 1\n\t" \
        "v_cndmask_b32 v136, v190, v136, vcc\n\tv_cmp_le_i32 vcc, 17, %[dq]\n\ts_nop 1\n\tv_cndmask_b32 v137, v190, v137, vcc\n\tv_cmp_le_i32 vcc, 18, %[dq]\n\ts_nop 1\n\t" \
        "v_cndmask_b32 v138, v190, v138, vcc\n\tv_cmp_le_i32 vcc, 19, %[dq]\n\ts_nop 1\n\tv_cndmask_b32 v139, v190, v139, vcc\n\tv_cmp_le_i32 vcc, 24, %[dq]\n\ts_nop 1\n\t" \
        "v_cndmask_b32 v140, v190, v140, vcc\n\tv_cmp_le_i32 vcc, 25, %[dq]\n\ts_nop 1\n\tv_cndmask_b32 v141, v190, v141, vcc\n\tv_cmp_le_i32 vcc, 26, %[dq]\n\ts_nop 1\n\t" \
        "v_cndmask_b32 v142, v190, v142, vcc\n\tv_cmp_le_i32 vcc, 27, %[dq]\n\ts_nop 1\n\tv_cndmask_b32 v143, v190, v143, vcc\n\ts_nop 1\n\ts_mov_b64 vcc, %[sv]\n\ts_branch 42b\n\t44:\n" \
        "v_exp_f32 v96, v96\n\tv_exp_f32 v97, v97\n\tv_add_f32 v184, v184, v96\n\tv_exp_f32 v98, v98\n\tv_add_f32 v185, v185, v97\n\ts_cbranch_vccz 82f\n\ts_sub_u32 %[t0], %[sk], 0x8000\n\t" \
        "s_and_b32 %[t0], %[t0], 0x18000\n\ts_add_u32 m0, %[t0], %[lb]\n\ts_add_u32 %[t0], %[so], 0x1000000\n\tbuffer_load_dwordx4 %[ko], %[rs], %[so] offen sc1 lds\n\t82:\nv_exp_f32 v99, v99\n\t" \
        "v_add_f32 v184, v184, v98\n\tv_exp_f32 v100, v100\n\tv_cvt_pk_bf16_f32 v144, v96, v97\n\tv_exp_f32 v101, v101\n\ts_cbranch_vccz 83f\n\t" \
        "buffer_load_dwordx4 %[ko], %[rs], %[so] offen offset:1024 sc1 lds\n\t83:\nv_add_u32 v188, %[sk], %[vb]\n\tv_xor_b32 v189, 0x80, v188\n\tds_read_b64_tr_b16 v[152:153], v188 offset:0\n\t" \
        "ds_read_b64_tr_b16 v[154:155], v189 offset:0\n\tv_add_f32 v185, v185, v99\n\tv_cvt_pk_bf16_f32 v145, v98, v99\n\tv_exp_f32 v102, v102\n\tv_add_f32 v184, v184, v100\n\t" \
        "ds_read_b64_tr_b16 v[156:157], v188 offset:2048\n\tds_read_b64_tr_b16 v[158:159], v189 offset:2048\n\tv_cvt_pk_bf16_f32 v146, v100, v101\n\tv_exp_f32 v103, v103\n\t" \
        "v_add_f32 v185, v185, v101\n\ts_cbranch_vccz 84f\n\ts_add_u32 m0, m0, 0x4000\n\ts_add_u32 %[so], %[so], 0x80000\n\tbuffer_load_dwordx4 %[vo], %[rs], %[t0] offen sc1 lds\n\t84:\n" \
        "ds_read_b64_tr_b16 v[160:161], v188 offset:4096\n\tds_read_b64_tr_b16 v[162:163], v189 offset:4096\n\tv_exp_f32 v104, v104\n\tv_add_f32 v184, v184, v102\n\t" \
        "v_cvt_pk_bf16_f32 v147, v102, v103\n\tds_read_b64_tr_b16 v[164:165], v188 offset:6144\n\tds_read_b64_tr_b16 v[166:167], v189 offset:6144\n\tv_exp_f32 v105, v105\n\tv_exp_f32 v106, v106\n\t" \
        "s_cbranch_vccz 85f\n\tbuffer_load_dwordx4 %[vo], %[rs], %[t0] offen offset:1024 sc1 lds\n\t85:\ns_add_u32 %[sk], %[sk], 0x8000\n\ts_and_b32 %[sk], %[sk], 0x18000\n\ts_waitcnt lgkmcnt(6)\n\t" \
        "v_mfma_f32_32x32x16_bf16 v[0:15], v[152:155], v[144:147], v[0:15]\n\tds_read_b64_tr_b16 v[168:169], v188 offset:1024\n\tds_read_b64_tr_b16 v[170:171], v189 offset:1024\n\t" \
        "v_cvt_pk_bf16_f32 v148, v104, v105\n\tv_exp_f32 v107, v107\n\tv_exp_f32 v108, v108\n\ts_waitcnt lgkmcnt(6)\n\tv_mfma_f32_32x32x16_bf16 v[16:31], v[156:159], v[144:147], v[16:31]\n\t" \
        "ds_read_b64_tr_b16 v[172:173], v188 offset:3072\n\tds_read_b64_tr_b16 v[174:175], v189 offset:3072\n\tv_cvt_pk_bf16_f32 v149, v106, v107\n\tv_exp_f32 v109, v109\n\ts_waitcnt lgkmcnt(6)\n\t" \
        "v_mfma_f32_32x32x16_bf16 v[32:47], v[160:163], v[144:147], v[32:47]\n\tds_read_b64_tr_b16 v[176:177], v188 offset:5120\n\tds_read_b64_tr_b16 v[178:179], v189 offset:5120\n\t" \
        "v_exp_f32 v110, v110\n\tv_cvt_pk_bf16_f32 v150, v108, v109\n\tv_exp_f32 v111, v111\n\ts_waitcnt lgkmcnt(6)\n\tv_mfma_f32_32x32x16_bf16 v[48:63], v[164:167], v[144:147], v[48:63]\n\t" \
        "ds_read_b64_tr_b16 v[180:181], v188 offset:7168\n\tds_read_b64_tr_b16 v[182:183], v189 offset:7168\n\tv_add_f32 v185, v185, v103\n\tv_cvt_pk_bf16_f32 v151, v110, v111\n\t" \
        "v_add_f32 v184, v184, v104\n\tv_add_f32 v185, v185, v105\n\ts_waitcnt lgkmcnt(6)\n\tv_mfma_f32_32x32x16_bf16 v[0:15], v[168:171], v[148:151], v[0:15]\n\tv_add_f32 v184, v184, v106\n\t" \
        "s_waitcnt lgkmcnt(4)\n\tv_mfma_f32_32x32x16_bf16 v[16:31], v[172:175], v[148:151], v[16:31]\n\tv_add_f32 v185, v185, v107\n\tv_add_f32 v184, v184, v108\n\ts_waitcnt lgkmcnt(2)\n\t" \
        "v_mfma_f32_32x32x16_bf16 v[32:47], v[176:179], v[148:151], v[32:47]\n\tv_add_f32 v185, v185, v109\n\ts_waitcnt lgkmcnt(0)\n\t" \
        "v_mfma_f32_32x32x16_bf16 v[48:63], v[180:183], v[148:151], v[48:63]\n\tv_add_f32 v184, v184, v110\n\tv_add_f32 v185, v185, v111\n\ts_branch 99f\n\t61:\nv_mov_b32 v190, v186\n\ts_nop 1\n\t" \
        "v_permlane32_swap_b32 v186, v190\n\ts_nop 1\n\tv_max_f32 v186, v186, v190\n\tv_max_f32 v186, 0, v186\n\tv_sub_f32 v128, v128, v186\n\tv_sub_f32 v129, v129, v186\n\t" \
        "v_sub_f32 v130, v130, v186\n\tv_sub_f32 v131, v131, v186\n\tv_sub_f32 v132, v132, v186\n\tv_sub_f32 v133, v133, v186\n\tv_sub_f32 v134, v134, v186\n\tv_sub_f32 v135, v135, v186\n\t" \
        "v_sub_f32 v136, v136, v186\n\tv_sub_f32 v137, v137, v186\n\tv_sub_f32 v138, v138, v186\n\tv_sub_f32 v139, v139, v186\n\tv_sub_f32 v140, v140, v186\n\tv_sub_f32 v141, v141, v186\n\t" \
        "v_sub_f32 v142, v142, v186\n\tv_sub_f32 v143, v143, v186\n\tv_sub_f32 v112, v112, v186\n\tv_sub_f32 v113, v113, v186\n\tv_sub_f32 v114, v114, v186\n\tv_sub_f32 v115, v115, v186\n\t" \
        "v_sub_f32 v116, v116, v186\n\tv_sub_f32 v117, v117, v186\n\tv_sub_f32 v118, v118, v186\n\tv_sub_f32 v119, v119, v186\n\tv_sub_f32 v120, v120, v186\n\tv_sub_f32 v121, v121, v186\n\t" \
        "v_sub_f32 v122, v122, v186\n\tv_sub_f32 v123, v123, v186\n\tv_sub_f32 v124, v124, v186\n\tv_sub_f32 v125, v125, v186\n\tv_sub_f32 v126, v126, v186\n\tv_sub_f32 v127, v127, v186\n\t" \
        "v_exp_f32 v190, -v186\n\ts_nop 0\n\tv_mul_f32 v184, v184, v190\n\tv_mul_f32 v185, v185, v190\n\tv_mul_f32 v0, v0, v190\n\tv_mul_f32 v1, v1, v190\n\tv_mul_f32 v2, v2, v190\n\t" \
        "v_mul_f32 v3, v3, v190\n\tv_mul_f32 v4, v4, v190\n\tv_mul_f32 v5, v5, v190\n\tv_mul_f32 v6, v6, v190\n\tv_mul_f32 v7, v7, v190\n\tv_mul_f32 v8, v8, v190\n\tv_mul_f32 v9, v9, v190\n\t" \
        "v_mul_f32 v10, v10, v190\n\tv_mul_f32 v11, v11, v190\n\tv_mul_f32 v12, v12, v190\n\tv_mul_f32 v13, v13, v190\n\tv_mul_f32 v14, v14, v190\n\tv_mul_f32 v15, v15, v190\n\t" \
        "v_mul_f32 v16, v16, v190\n\tv_mul_f32 v17, v17, v190\n\tv_mul_f32 v18, v18, v190\n\tv_mul_f32 v19, v19, v190\n\tv_mul_f32 v20, v20, v190\n\tv_mul_f32 v21, v21, v190\n\t" \
        "v_mul_f32 v22, v22, v190\n\tv_mul_f32 v23, v23, v190\n\tv_mul_f32 v24, v24, v190\n\tv_mul_f32 v25, v25, v190\n\tv_mul_f32 v26, v26, v190\n\tv_mul_f32 v27, v27, v190\n\t" \
        "v_mul_f32 v28, v28, v190\n\tv_mul_f32 v29, v29, v190\n\tv_mul_f32 v30, v30, v190\n\tv_mul_f32 v31, v31, v190\n\tv_mul_f32 v32, v32, v190\n\tv_mul_f32 v33, v33, v190\n\t" \
        "v_mul_f32 v34, v34, v190\n\tv_mul_f32 v35, v35, v190\n\tv_mul_f32 v36, v36, v190\n\tv_mul_f32 v37, v37, v190\n\tv_mul_f32 v38, v38, v190\n\tv_mul_f32 v39, v39, v190\n\t" \
        "v_mul_f32 v40, v40, v190\n\tv_mul_f32 v41, v41, v190\n\tv_mul_f32 v42, v42, v190\n\tv_mul_f32 v43, v43, v190\n\tv_mul_f32 v44, v44, v190\n\tv_mul_f32 v45, v45, v190\n\t" \
        "v_mul_f32 v46, v46, v190\n\tv_mul_f32 v47, v47, v190\n\tv_mul_f32 v48, v48, v190\n\tv_mul_f32 v49, v49, v190\n\tv_mul_f32 v50, v50, v190\n\tv_mul_f32 v51, v51, v190\n\t" \
        "v_mul_f32 v52, v52, v190\n\tv_mul_f32 v53, v53, v190\n\tv_mul_f32 v54, v54, v190\n\tv_mul_f32 v55, v55, v190\n\tv_mul_f32 v56, v56, v190\n\tv_mul_f32 v57, v57, v190\n\t" \
        "v_mul_f32 v58, v58, v190\n\tv_mul_f32 v59, v59, v190\n\tv_mul_f32 v60, v60, v190\n\tv_mul_f32 v61, v61, v190\n\tv_mul_f32 v62, v62, v190\n\tv_mul_f32 v63, v63, v190\n\ts_branch 60b\n\t63:\n" \
        "s_cmp_eq_u32 %[rem], 0\n\ts_cbranch_scc1 64f\n\ts_mov_b64 %[sv], vcc\n\tv_mov_b32 v190, 0xff800000\n\tv_cmp_le_i32 vcc, 0, %[dq]\n\ts_nop 1\n\tv_cndmask_b32 v128, v190, v128, vcc\n\t" \
        "v_cmp_le_i32 vcc, 1, %[dq]\n\ts_nop 1\n\tv_cndmask_b32 v129, v190, v129, vcc\n\tv_cmp_le_i32 vcc, 2, %[dq]\n\ts_nop 1\n\tv_cndmask_b32 v130, v190, v130, vcc\n\tv_cmp_le_i32 vcc, 3, %[dq]\n\t" \
        "s_nop 1\n\tv_cndmask_b32 v131, v190, v131, vcc\n\tv_cmp_le_i32 vcc, 8, %[dq]\n\ts_nop 1\n\tv_cndmask_b32 v132, v190, v132, vcc\n\tv_cmp_le_i32 vcc, 9, %[dq]\n\ts_nop 1\n\t" \
        "v_cndmask_b32 v133, v190, v133, vcc\n\tv_cmp_le_i32 vcc, 10, %[dq]\n\ts_nop 1\n\tv_cndmask_b32 v134, v190, v134, vcc\n\tv_cmp_le_i32 vcc, 11, %[dq]\n\ts_nop 1\n\t" \
        "v_cndmask_b32 v135, v190, v135, vcc\n\tv_cmp_le_i32 vcc, 16, %[dq]\n\ts_nop 1\n\tv_cndmask_b32 v136, v190, v136, vcc\n\tv_cmp_le_i32 vcc, 17, %[dq]\n\ts_nop 1\n\t" \
        "v_cndmask_b32 v137, v190, v137, vcc\n\tv_cmp_le_i32 vcc, 18, %[dq]\n\ts_nop 1\n\tv_cndmask_b32 v138, v190, v138, vcc\n\tv_cmp_le_i32 vcc, 19, %[dq]\n\ts_nop 1\n\t" \
        "v_cndmask_b32 v139, v190, v139, vcc\n\tv_cmp_le_i32 vcc, 24, %[dq]\n\ts_nop 1\n\tv_cndmask_b32 v140, v190, v140, vcc\n\tv_cmp_le_i32 vcc, 25, %[dq]\n\ts_nop 1\n\t" \
        "v_cndmask_b32 v141, v190, v141, vcc\n\tv_cmp_le_i32 vcc, 26, %[dq]\n\ts_nop 1\n\tv_cndmask_b32 v142, v190, v142, vcc\n\tv_cmp_le_i32 vcc, 27, %[dq]\n\ts_nop 1\n\t" \
        "v_cndmask_b32 v143, v190, v143, vcc\n\ts_nop 1\n\ts_mov_b64 vcc, %[sv]\n\ts_branch 62b\n\t64:\nv_exp_f32 v112, v112\n\tv_exp_f32 v113, v113\n\tv_add_f32 v184, v184, v112\n\t" \
        "v_exp_f32 v114, v114\n\tv_add_f32 v185, v185, v113\n\tv_exp_f32 v115, v115\n\tv_add_f32 v184, v184, v114\n\tv_exp_f32 v116, v116\n\tv_cvt_pk_bf16_f32 v144, v112, v113\n\t" \
        "v_exp_f32 v117, v117\n\tds_read_b64_tr_b16 v[152:153], v188 offset:8192\n\tds_read_b64_tr_b16 v[154:155], v189 offset:8192\n\tv_add_f32 v185, v185, v115\n\t" \
        "v_cvt_pk_bf16_f32 v145, v114, v115\n\tv_exp_f32 v118, v118\n\tv_add_f32 v184, v184, v116\n\tds_read_b64_tr_b16 v[156:157], v188 offset:10240\n\t" \
        "ds_read_b64_tr_b16 v[158:159], v189 offset:10240\n\tv_cvt_pk_bf16_f32 v146, v116, v117\n\tv_exp_f32 v119, v119\n\tv_add_f32 v185, v185, v117\n\t" \
        "ds_read_b64_tr_b16 v[160:161], v188 offset:12288\n\tds_read_b64_tr_b16 v[162:163], v189 offset:12288\n\tv_exp_f32 v120, v120\n\tv_add_f32 v184, v184, v118\n\t" \
        "v_cvt_pk_bf16_f32 v147, v118, v119\n\tds_read_b64_tr_b16 v[164:165], v188 offset:14336\n\tds_read_b64_tr_b16 v[166:167], v189 offset:14336\n\tv_exp_f32 v121, v121\n\tv_exp_f32 v122, v122\n\t" \
        "s_waitcnt lgkmcnt(6)\n\tv_mfma_f32_32x32x16_bf16 v[0:15], v[152:155], v[144:147], v[0:15]\n\tds_read_b64_tr_b16 v[168:169], v188 offset:9216\n\t" \
        "ds_read_b64_tr_b16 v[170:171], v189 offset:9216\n\tv_cvt_pk_bf16_f32 v148, v120, v121\n\tv_exp_f32 v123, v123\n\tv_exp_f32 v124, v124\n\ts_waitcnt lgkmcnt(6)\n\t" \
        "v_mfma_f32_32x32x16_bf16 v[16:31], v[156:159], v[144:147], v[16:31]\n\tds_read_b64_tr_b16 v[172:173], v188 offset:11264\n\tds_read_b64_tr_b16 v[174:175], v189 offset:11264\n\t" \
        "v_cvt_pk_bf16_f32 v149, v122, v123\n\tv_exp_f32 v125, v125\n\ts_waitcnt lgkmcnt(6)\n\tv_mfma_f32_32x32x16_bf16 v[32:47], v[160:163], v[144:147], v[32:47]\n\t" \
        "ds_read_b64_tr_b16 v[176:177], v188 offset:13312\n\tds_read_b64_tr_b16 v[178:179], v189 offset:13312\n\tv_exp_f32 v126, v126\n\tv_cvt_pk_bf16_f32 v150, v124, v125\n\tv_exp_f32 v127, v127\n\t" \
        "s_waitcnt lgkmcnt(6)\n\tv_mfma_f32_32x32x16_bf16 v[48:63], v[164:167], v[144:147], v[48:63]\n\tds_read_b64_tr_b16 v[180:181], v188 offset:15360\n\t" \
        "ds_read_b64_tr_b16 v[182:183], v189 offset:15360\n\tv_add_f32 v185, v185, v119\n\tv_cvt_pk_bf16_f32 v151, v126, v127\n\tv_add_f32 v184, v184, v120\n\tv_add_f32 v185, v185, v121\n\t" \
        "s_waitcnt lgkmcnt(6)\n\tv_mfma_f32_32x32x16_bf16 v[0:15], v[168:171], v[148:151], v[0:15]\n\tv_add_f32 v184, v184, v122\n\ts_waitcnt lgkmcnt(4)\n\t" \
        "v_mfma_f32_32x32x16_bf16 v[16:31], v[172:175], v[148:151], v[16:31]\n\tv_add_f32 v185, v185, v123\n\tv_add_f32 v184, v184, v124\n\ts_waitcnt lgkmcnt(2)\n\t" \
        "v_mfma_f32_32x32x16_bf16 v[32:47], v[176:179], v[148:151], v[32:47]\n\tv_add_f32 v185, v185, v125\n\ts_waitcnt lgkmcnt(0)\n\t" \
        "v_mfma_f32_32x32x16_bf16 v[48:63], v[180:183], v[148:151], v[48:63]\n\tv_add_f32 v184, v184, v126\n\tv_add_f32 v185, v185, v127\n\ts_branch 99f\n\t99:\ns_mov_b32 m0, %[keep]\n\t" \
        "v_add_f32 v184, v184, v185\n\ts_nop 10\n\t"
#define CL8(a, b, c, d, e, f, g, h) "v" #a, "v" #b, "v" #c, "v" #d, "v" #e, "v" #f, "v" #g, "v" #h
#define CL128_159 CL8(128, 129, 130, 131, 132, 133, 134, 135), CL8(136, 137, 138, 139, 140, 141, 142, 143), CL8(144, 145, 146, 147, 148, 149, 150, 151), CL8(152, 153, 154, 155, 156, 157, 158, 159)
#define CL160_191 CL8(160, 161, 162, 163, 164, 165, 166, 167), CL8(168, 169, 170, 171, 172, 173, 174, 175), CL8(176, 177, 178, 179, 180, 181, 182, 183), CL8(184, 185, 186, 187, 188, 189, 190, 191)
#define CL16_63 CL8(16, 17, 18, 19, 20, 21, 22, 23), CL8(24, 25, 26, 27, 28, 29, 30, 31), CL8(32, 33, 34, 35, 36, 37, 38, 39), CL8(40, 41, 42, 43, 44, 45, 46, 47), CL8(48, 49, 50, 51, 52, 53, 54, 55), CL8(56, 57, 58, 59, 60, 61, 62, 63)
#define CL64_127 CL8(64, 65, 66, 67, 68, 69, 70, 71), CL8(72, 73, 74, 75, 76, 77, 78, 79), CL8(80, 81, 82, 83, 84, 85, 86, 87), CL8(88, 89, 90, 91, 92, 93, 94, 95), \
                 CL8(96, 97, 98, 99, 100, 101, 102, 103), CL8(104, 105, 106, 107, 108, 109, 110, 111), CL8(112, 113, 114, 115, 116, 117, 118, 119), CL8(120, 121, 122, 123, 124, 125, 126, 127)
#define CL224_251 CL8(224, 225, 226, 227, 228, 229, 230, 231), CL8(232, 233, 234, 235, 236, 237, 238, 239), CL8(240, 241, 242, 243, 244, 245, 246, 247), "v248", "v249", "v250", "v251"
#define CL192_223 CL8(192, 193, 194, 195, 196, 197, 198, 199), CL8(200, 201, 202, 203, 204, 205, 206, 207), CL8(208, 209, 210, 211, 212, 213, 214, 215), CL8(216, 217, 218, 219, 220, 221, 222, 223)
#define W128(w) "{v[0:15]}"(w[0]), "{v[16:31]}"(w[1]), "{v[32:47]}"(w[2]), "{v[48:63]}"(w[3]), "{v[64:79]}"(w[4]), "{v[80:95]}"(w[5]), "{v[96:111]}"(w[6]), "{v[112:127]}"(w[7])
__device__ __forceinline__ u32x16 cat16(bf16x8 a, bf16x8 b, bf16x8 c, bf16x8 d) {
    const u32x8 x = __builtin_shufflevector(__builtin_bit_cast(u32x4, a), __builtin_bit_cast(u32x4, b), 0, 1, 2, 3, 4, 5, 6, 7);
    const u32x8 y = __builtin_shufflevector(__builtin_bit_cast(u32x4, c), __builtin_bit_cast(u32x4, d), 0, 1, 2, 3, 4, 5, 6, 7);
    return __builtin_shufflevector(x, y, 0, 1, 2, 3, 4, 5, 6, 7, 8, 9, 10, 11, 12, 13, 14, 15); }
#define XIMG(n) ((((unsigned)(n) + 3u) & 3u) * 32768u + 32768u)
__device__ __forceinline__ void cols32(bf16x8* wf, int i, const char* p, int wid, int lane) {
    const unsigned r = 8 * (lane >> 4) + ((lane & 15) >> 2), c = 4 * (wid & 3) + (lane & 3);
#pragma unroll
    for (int k = 0; k < 4; ++k) wf[16 * (k & 1) + 2 * i + (k >> 1)] = cat8(tr_rd(p + (k >> 1) * 8192 + off_b(r, c) + (k & 1) * 8), tr_rd(p + (k >> 1) * 8192 + off_b(r + 4, c) + (k & 1) * 8)); }
template <bool PUB>
__device__ __forceinline__ void proj_ws(const bf16* __restrict__ A, const bf16* __restrict__ W, bf16* __restrict__ out, int c0, int cs,
                                        unsigned* prog, float sc, const bf16* __restrict__ Wx, u32x16 (&wx)[4], const Hello& hi, char* lds, int tid) {
    const int wid = __builtin_amdgcn_readfirstlane(tid >> 6), lane = tid & 63, l16 = lane & 15, g4 = lane >> 4;
    const unsigned lbase = (unsigned)(uintptr_t)lds;
    unsigned long long hv = 0;
    const u32x4 rsA = mk_rs(A, SEQ * DM * 2);
    const unsigned dvo = (unsigned)(((wid >> 1) * 8 + (lane >> 3)) * 1024 + ((wid & 1) * 32 + ((lane & 7) ^ (lane >> 3))) * 16);
    auto fetch = [&](int ch, int n) {
        const unsigned so = (unsigned)(min(ch, NSEG - 1) * 32768);
#pragma unroll
        for (int i = 0; i < 4; ++i) dma16<false>(rsA, dvo, so + i * 128, lbase + XIMG(n) + (4 * wid + i) * 1024); };
    fetch(c0, 0);
    bf16x8 wf[2][16];
    stream_w(W + (wid >> 2) * 128, HD * 2, [&](int i, int p) { return (unsigned)((64 * i + 16 * p) * (HD * 2)); }, [&](int i, const char* p) { cols32(&wf[0][0], i, p, wid, lane);
                 if (PUB && wid == 0 && i == 4) hv = hi.peek(); }, lds, wid, lane);
    if (PUB && wid == 0) hi.wait(hv);
    __syncthreads();
    fetch(c0 + cs, 1); fetch(c0 + 2 * cs, 2);
    u32x16 w[8];
#pragma unroll
    for (int i = 0; i < 8; ++i) w[i] = cat16(wf[i >> 2][(i & 3) * 4], wf[i >> 2][(i & 3) * 4 + 1], wf[i >> 2][(i & 3) * 4 + 2], wf[i >> 2][(i & 3) * 4 + 3]);
    VMWAIT(8);
    __builtin_amdgcn_s_barrier(); asm volatile("" ::: "memory");
    PRB(PUB ? 8 : 3);
    unsigned n, keep, im, t0, t1, sp = (unsigned)(c0 - cs) << 18, so = (unsigned)(c0 + 3 * cs) << 15;
#define PROJ_OUT [n] "=&s"(n), [keep] "=&s"(keep), [im] "=&s"(im), [t0] "=&s"(t0), [t1] "=&s"(t1), [sp] "+s"(sp), [so] "+s"(so)
#define PROJ_OPS W128(w), [xb] "v"(lbase + 32768 + (l16 >> 3) * 8192 + (l16 & 7) * 128 + (g4 ^ (l16 & 7)) * 16), [lb] "s"(pin_s(lbase + 32768 + wid * 4096)), \
                   [co] "v"((unsigned)(wid * 2048 + lane * 16)), [rc] "s"(mk_rs(out, SEQ * HD * 2)), [ra] "s"(rsA), [dv] "v"(dvo), \
                   [em] "s"(__builtin_amdgcn_ballot_w64(tid == 0)), [pg] "s"(prog), [sc] "s"(sc) \
                 : "memory", "scc", CL128_159, CL8(160, 161, 162, 163, 164, 165, 166, 167), CL8(168, 169, 170, 171, 172, 173, 174, 175), "v176", "v177", "v178", "v179", "v180", "v181", "v182"
    if (PUB) asm volatile(ASM_PROJ_PUB : PROJ_OUT, "=&{v[184:199]}"(wx[0]), "=&{v[200:215]}"(wx[1]), "=&{v[216:231]}"(wx[2]), "=&{v[232:247]}"(wx[3])
                          : [wv] "v"((unsigned)tid * 16u), [rw] "s"(mk_rs(Wx, DH * DM * 2)), PROJ_OPS);
    else asm volatile(ASM_PROJ_A : PROJ_OUT : PROJ_OPS);
    VMWAIT(0);
    __syncthreads();
    if (PUB && tid == 0) __hip_atomic_store(prog, (unsigned)n, __ATOMIC_RELAXED, __HIP_MEMORY_SCOPE_AGENT);
}

namespace attn {
constexpr float C2 = 1.4426950408889634f * ATT_SCALE;
#define SBAR() __builtin_amdgcn_sched_barrier(0)
template <class T>
__device__ __forceinline__ u32x4 pk4(const T& P, int B) {
    const unsigned a0 = cvtpk(P[B + 0], P[B + 1]), a1 = cvtpk(P[B + 2], P[B + 3]), b0 = cvtpk(P[B + 4], P[B + 5]), b1 = cvtpk(P[B + 6], P[B + 7]);
    const auto r0 = __builtin_amdgcn_permlane32_swap(a0, b0, false, false), r1 = __builtin_amdgcn_permlane32_swap(a1, b1, false, false);
    return u32x4{r0[0], r1[0], r0[1], r1[1]}; }
__device__ __forceinline__ void listed(unsigned* list, int b, unsigned c) {
    if (c != (b < 24 ? 5u : 6u)) return;
    list += (b >> 3) * 64;
    const unsigned p = __hip_atomic_fetch_add(list, 1u, __ATOMIC_RELAXED, __HIP_MEMORY_SCOPE_AGENT);
    __hip_atomic_fetch_add(list + 32, ((unsigned)(b & 7) + 1u) << (p * 4), __ATOMIC_RELAXED, __HIP_MEMORY_SCOPE_AGENT); }
__device__ __forceinline__ int pick(unsigned v, int own) {
    int best = 8, k = -1;
#pragma unroll
    for (int i = 0; i < 5; ++i) { const int c = (int)__builtin_amdgcn_readlane(v, i), lv = min(3 + (i < 3 ? c / 6 : c / 7), 8) - (i == own ? SLACK : 0);
        if (c < (i < 3 ? 30 : 35) && (lv < best || (lv == best && i == own))) { best = lv; k = i; } }
    return k; }
__device__ __forceinline__ int causal_block(int h, int qb, char* qkv, char* lds, unsigned* queue, int own, unsigned* qc, unsigned* list, int prev, volatile LAS int* lq, int seq) {
    int tid = threadIdx.x; asm volatile("" : "+v"(tid));
    const int wid = __builtin_amdgcn_readfirstlane(tid >> 6), lane = tid & 63, r32 = lane & 31, hi = lane >> 5;
    const int rho = wid < 4 ? wid : 11 - wid;
    const unsigned lbase = (unsigned)(uintptr_t)lds;
    const u32x4 rs = mk_rs(qkv, (unsigned)(3 * SZ_ACT));
    const __amdgpu_buffer_rsrc_t rsQ = __builtin_amdgcn_make_buffer_rsrc((void*)(qkv + 3 * SZ_ACT), 0, (int)SZ_ACT, 0x00020000);
    const unsigned ko = lane * 16, vo = (lane & 48) * 16 + ((lane & 15) ^ ((lane >> 4) << 2)) * 16;
    const unsigned sb = (unsigned)(((wid >> 2) * 32 + h) * 8192 + (wid & 3) * 2048) + (unsigned)SZ_ACT;
    const unsigned fo = (r32 >> 4) * 1024 + hi * 256 + (r32 & 15) * 16, qso = (unsigned)(((qb * 8 + rho) * 32 + h) * 8192);
    u32x16 q0, q1;
    unsigned cq = 0;
    asm volatile("s_mov_b64 exec, %3\n\tglobal_atomic_add %0, %1, %2, off sc0 sc1\n\ts_mov_b64 exec, -1"
                 : "+v"(cq) : "v"(qc + max(prev, 0) * 32), "v"(1u), "s"(__builtin_amdgcn_ballot_w64(tid == 0 && prev >= 0)) : "memory");
    {   unsigned keep, so = sb, t0;
#define QLD(r, v, o) "buffer_load_dwordx4 v[" r "], %[" v "], %[rs], %[qs] offen offset:" o " sc1\n\t"
#define KVLD "s_add_u32 %[t0], %[so], 0x1000000\n\tbuffer_load_dwordx4 %[ko], %[rs], %[so] offen sc1 lds\n\tbuffer_load_dwordx4 %[ko], %[rs], %[so] offen offset:1024 sc1 lds\n\t" \
             "s_add_u32 m0, m0, 0x4000\n\ts_add_u32 %[so], %[so], 0x80000\n\tbuffer_load_dwordx4 %[vo], %[rs], %[t0] offen sc1 lds\n\tbuffer_load_dwordx4 %[vo], %[rs], %[t0] offen offset:1024 sc1 lds\n\t" \
             "s_add_u32 m0, m0, 0x4000\n\t"
        asm volatile("s_mov_b32 %[keep], m0\n\ts_mov_b32 m0, %[lb]\n\t"
                     QLD("64:67", "f0", "0") QLD("68:71", "f0", "512") QLD("72:75", "f0", "2048") QLD("76:79", "f0", "2560")
                     QLD("80:83", "f1", "0") QLD("84:87", "f1", "512") QLD("88:91", "f1", "2048") QLD("92:95", "f1", "2560")
                     KVLD KVLD KVLD "s_mov_b32 m0, %[keep]\n\ts_waitcnt vmcnt(10)"
                     : "=&{v[64:79]}"(q0), "=&{v[80:95]}"(q1), [keep] "=&s"(keep), [so] "+s"(so), [t0] "=&s"(t0), "+v"(cq)
                     : [f0] "v"(fo), [f1] "v"(fo + 4096), [rs] "s"(rs), [qs] "s"(qso), [ko] "v"(ko), [vo] "v"(vo), [lb] "s"(pin_s(lbase + wid * 2048)) : "memory", "scc"); }
    listed(list, prev, cq);
    const unsigned g4v = ((lane >> 3) & 2) | ((lane >> 1) & 1);
    f32x16 o[4]; float l_reg;
    {   unsigned keep, sk, t0, so = sb + 3 * 524288u, rem = (unsigned)(8 * qb + rho), tb = (unsigned)(4 * qb + 1); unsigned long long sv;
        asm volatile(ASM_ATT
                     : "=&{v[0:15]}"(o[0]), "=&{v[16:31]}"(o[1]), "=&{v[32:47]}"(o[2]), "=&{v[48:63]}"(o[3]), "=&{v184}"(l_reg),
                       [keep] "=&s"(keep), [sk] "=&s"(sk), [t0] "=&s"(t0), [sv] "=&s"(sv), [so] "+s"(so), [rem] "+s"(rem), [tb] "+s"(tb)
                     : "{v[64:79]}"(q0), "{v[80:95]}"(q1), [fo] "v"(lbase + fo), [vb] "v"(lbase + 16384 + g4v * 256 + (hi ^ g4v) * 64 + ((lane >> 2) & 3) * 16 + (lane & 1) * 8),
                       [ko] "v"(ko), [vo] "v"(vo), [rs] "s"(rs), [lb] "s"(pin_s(lbase + wid * 2048)), [dq] "v"(r32 - 4 * hi)
                     : "memory", "scc", "vcc", CL8(96, 97, 98, 99, 100, 101, 102, 103), CL8(104, 105, 106, 107, 108, 109, 110, 111), CL8(112, 113, 114, 115, 116, 117, 118, 119),
                       CL8(120, 121, 122, 123, 124, 125, 126, 127), CL128_159, CL8(160, 161, 162, 163, 164, 165, 166, 167), CL8(168, 169, 170, 171, 172, 173, 174, 175),
                       CL8(176, 177, 178, 179, 180, 181, 182, 183), "v185", "v186", "v187", "v188", "v189", "v190"); }
    if (wid < 2) { VMWAIT(0); __builtin_amdgcn_s_barrier(); asm volatile("" ::: "memory"); }
    {
        auto rr = __builtin_amdgcn_permlane32_swap(__float_as_uint(l_reg), __float_as_uint(l_reg), false, false);
        const float rl = __builtin_amdgcn_rcpf(__uint_as_float(rr[0]) + __uint_as_float(rr[1]));
#pragma unroll
        for (int d = 0; d < 4; ++d) {
            o[d] *= rl;
            __builtin_amdgcn_raw_buffer_store_b128(pk4(o[d], 0), rsQ, fo + d * 2048, qso, 16); __builtin_amdgcn_raw_buffer_store_b128(pk4(o[d], 8), rsQ, fo + d * 2048 + 512, qso, 16);
        }
    }
    if (wid == 0) {
        const unsigned v = __hip_atomic_load(queue + min(lane, 5) * 32, __ATOMIC_RELAXED, __HIP_MEMORY_SCOPE_AGENT), pa = __builtin_amdgcn_readlane(v, 5);
        int k = (pa & 0xFFFFu) >= NPA ? pick(v, own) : -2;
        k = k == -1 && pa < (32u << 16) ? -2 : k;
        if (lane == 0) {
            lq[2] = seq * 8 + k + 2;
            if (k != -1) lq[0] = k < 0 ? 64 : (int)__hip_atomic_fetch_add(queue + k * 32, 1u, __ATOMIC_RELAXED, __HIP_MEMORY_SCOPE_AGENT); } }
    int k;
#pragma unroll 1
    for (int i = 0; i < (1 << 20); ++i) { k = __builtin_amdgcn_readfirstlane(lq[2]); if ((k >> 3) == seq) break; __builtin_amdgcn_s_sleep(2); }
    k = (k & 7) - 2;
    if (k == -1) return k;
    VMWAIT(0);
    return k;
}
__device__ __forceinline__ int attn_phase(char* qkv, unsigned* queue, unsigned* qcnt, unsigned* list, int own, int k, int L, unsigned* tmo, char* lds, volatile LAS int* lq) {
    int prev = -1, seq = 0;
    if (threadIdx.x == 0) lq[2] = 0;
#pragma unroll 1
    for (;;) {
        if (L < 0) {
            __syncthreads();
            if (threadIdx.x < 64) {
                if (threadIdx.x == 0 && prev >= 0) listed(list, prev, __hip_atomic_fetch_add(qcnt + prev * 32, 1u, __ATOMIC_RELAXED, __HIP_MEMORY_SCOPE_AGENT));
                wave_wait<8>(queue + 160, NPA, tmo, 0x30000u, 0xFFFFu);
                for (;;) {
                    k = pick(__hip_atomic_load(queue + min((int)threadIdx.x, 5) * 32, __ATOMIC_RELAXED, __HIP_MEMORY_SCOPE_AGENT), own);
                    if (k < 0) break;
                    if (threadIdx.x == 0) L = (int)__hip_atomic_fetch_add(queue + k * 32, 1u, __ATOMIC_RELAXED, __HIP_MEMORY_SCOPE_AGENT);
                    L = __builtin_amdgcn_readfirstlane(L);
                    if (L < (k < 3 ? 30 : 35)) break; }
                if (k < 0) wave_wait<8>(queue + 160, 32u << 16, tmo, 0x30002u);
                if (threadIdx.x == 0) { lq[0] = L; lq[1] = k; } }
            prev = -1;
            __syncthreads();
            k = __builtin_amdgcn_readfirstlane(lq[1]);
            if (k < 0) return -1;
            L = __builtin_amdgcn_readfirstlane(lq[0]) + (k < 3 ? 18 : 21);
        }
        const int r = k < 3 ? L / 6 : L / 7;
        const int kn = causal_block(k < 3 ? 6 * (k - r) + L : 7 * (k - r) - 3 + L, 7 - r, qkv, lds, queue, own, qcnt, list, prev, lq, ++seq);
        prev = 8 * k + 7 - r;
        if (kn == -1) return prev;
        __syncthreads();
        k = kn;
        L = __builtin_amdgcn_readfirstlane(lq[0]);
        L = L < (k < 3 ? 30 : 35) ? L + (k < 3 ? 18 : 21) : -1;
    }
}
}

constexpr int AUX_SC1 = 16;
constexpr int CHAIN_LDS = 163840;
__device__ __forceinline__ unsigned umax4(u32x4 v) { return max(max(v[0], v[1]), max(v[2], v[3])); }
__device__ __forceinline__ unsigned uor4(u32x4 v) { return (v[0] | v[1]) | (v[2] | v[3]); }
__device__ __forceinline__ unsigned canon(unsigned w) { return min(w, 0xFFFFFFFEu); }
__device__ __forceinline__ s16x4 pack4(f32x4 v) { const u32x2 w = {cvtpk(v[0], v[1]), cvtpk(v[2], v[3])}; return __builtin_bit_cast(s16x4, w); }
__device__ __forceinline__ f32x4 mfma32(u32x4 a, u32x4 b, f32x4 c) {
    return __builtin_amdgcn_mfma_f32_16x16x32_bf16(__builtin_bit_cast(bf16x8, a), __builtin_bit_cast(bf16x8, b), c, 0, 0, 0); }
__device__ __forceinline__ float xg_max(float x) {
    { auto r = __builtin_amdgcn_permlane16_swap(__float_as_uint(x), __float_as_uint(x), false, false); x = fmaxf(__uint_as_float(r[0]), __uint_as_float(r[1])); }
    { auto r = __builtin_amdgcn_permlane32_swap(__float_as_uint(x), __float_as_uint(x), false, false); x = fmaxf(__uint_as_float(r[0]), __uint_as_float(r[1])); }
    return x; }
__device__ __forceinline__ float xg_sum(float x) {
    { auto r = __builtin_amdgcn_permlane16_swap(__float_as_uint(x), __float_as_uint(x), false, false); x = __uint_as_float(r[0]) + __uint_as_float(r[1]); }
    { auto r = __builtin_amdgcn_permlane32_swap(__float_as_uint(x), __float_as_uint(x), false, false); x = __uint_as_float(r[0]) + __uint_as_float(r[1]); }
    return x; }
template <int IMM>
__device__ __forceinline__ void mload(u32x4& v, unsigned long long m, unsigned off, u32x4 rs, unsigned so) {
    asm volatile("s_mov_b64 exec, %1\n\tbuffer_load_dwordx4 %0, %2, %3, %4 offen offset:%5 sc1\n\ts_mov_b64 exec, -1"
                 : "+v"(v) : "s"(m), "v"(off), "s"(rs), "s"(so), "i"(IMM) : "memory");
}
template <int IMM>
__device__ __forceinline__ void aload(u32x4& v, unsigned off, u32x4 rs, unsigned so) {
    asm volatile("buffer_load_dwordx4 %0, %1, %2, %3 offen offset:%4 sc1" : "=v"(v) : "v"(off), "s"(rs), "s"(so), "i"(IMM) : "memory");
}
template <int N, int PRE, int... IMM>
__device__ __forceinline__ unsigned poll16(u32x4 rs, const unsigned (&off)[N], const unsigned (&sa)[N], u32x4 (&v)[N],
                                           unsigned* tmo, unsigned code, unsigned long long live = ~0ull) {
    constexpr int imm[N] = {IMM...};
    unsigned long long m[N];
    if (PRE >= 0) {
        unsigned x = 0, y = 0;
#pragma unroll
        for (int i = 0; i < N; ++i) { if (i < PRE) x = max(x, umax4(v[i])); else y = max(y, umax4(v[i])); }
        if (__builtin_expect((__builtin_amdgcn_ballot_w64(x == 0xFFFFFFFFu) | (__builtin_amdgcn_ballot_w64(y == 0xFFFFFFFFu) & live)) == 0, 1)) return 0;
    }
#pragma unroll
    for (int i = 0; i < N; ++i) m[i] = PRE < 0 ? ~0ull : __builtin_amdgcn_ballot_w64(umax4(v[i]) == 0xFFFFFFFFu) & (i < PRE ? ~0ull : live);
    unsigned spins = PRE >= 0 ? 1 : 0;
    for (;; ++spins) {
        [&]<int... I>(std::integer_sequence<int, I...>) { (mload<imm[I]>(v[I], m[I], off[I], rs, sa[I]), ...); }(std::make_integer_sequence<int, N>{});
        unsigned long long any = 0;
#pragma unroll
        for (int i = 0; i < N; ++i) {
            asm volatile("s_waitcnt vmcnt(0)" : "+v"(v[i]) :: "memory");
            m[i] = __builtin_amdgcn_ballot_w64(umax4(v[i]) == 0xFFFFFFFFu) & m[i]; any |= m[i]; }
        if (any == 0) break;
        if (bail(spins, SPIN_CAP, tmo, code)) break;
    }
    return spins;
}
__device__ __forceinline__ void until(unsigned t0, unsigned dt) {
    for (;;) { const unsigned e = (unsigned)__builtin_amdgcn_s_memtime() - t0; if (e >= dt) break; if (dt - e > 160u) __builtin_amdgcn_s_sleep(1); } }
constexpr unsigned ANCHOR = 5120;
__device__ __forceinline__ void until_a(unsigned aw, int s, unsigned da, unsigned t0, unsigned dt) {
    if (!ANCH || (aw & 63u) != (unsigned)(s & 63) || aw == ~0u) { until(t0, dt); return; }
    const unsigned tg = (aw & ~63u) + (da << 6);
    for (int i = 0; i < 200; ++i) {
        const int e = (int)(tg - ((unsigned)__builtin_amdgcn_s_memrealtime() << 6));
        if (e <= 0) break;
        if (e > (8 << 6)) __builtin_amdgcn_s_sleep(1); } }
__device__ __forceinline__ void cnt_add(unsigned* w) { __hip_atomic_fetch_add(w, 1u, __ATOMIC_RELAXED, __HIP_MEMORY_SCOPE_AGENT); }
#define DUR(k, v) do {} while (0)
#define DURT() 0u
#define DUROUT(r) do {} while (0)
#define ST(i) do {} while (0)
#define SP(i, x) x
#define GST(i) do {} while (0)

__device__ __forceinline__ void oproj_ws(int kq, int nq, unsigned* queue, const bf16* __restrict__ src, const bf16* __restrict__ W, float* __restrict__ part,
                                         int prev, unsigned* tmo, char* lds, int tid) {
    const int wid = __builtin_amdgcn_readfirstlane(tid >> 6), lane = tid & 63;
    volatile LAS unsigned* lf = (volatile LAS unsigned*)(lds + 140032);
    unsigned* const list = tmo + W_LIST + 64 * kq + 32, * const fin = tmo + W_FA;
    const int hb = kq < 3 ? 6 * kq : 7 * kq - 3;
    const u32x4 rsA = mk_rs(src, (unsigned)SZ_ACT), rsC = mk_rs(part + (size_t)kq * SEQ * DM, SEQ * DM * 4);
    const unsigned lbase = (unsigned)(uintptr_t)lds, co = (unsigned)((nq * 8 + wid) * 2048 + lane * 16), sb = pin_s(hb * 8192 + wid * 1024);
    auto fetch = [&](int ch, unsigned im) {
#pragma unroll
        for (int i = 0; i < 7; ++i) if (i < 6 || kq >= 3) dma16<false>(rsA, (unsigned)lane * 16u, (unsigned)(ch * 262144 + i * 8192) + sb, lbase + im * 65536 + (i * 8 + wid) * 1024); };
    int ch = 0, last = -1, dr = -1;
    unsigned pos = 0, e = 0, im = 1;
    u32x16 w[7];
    {   unsigned c = 0, keep, so;
        asm volatile(ASM_OPRE : "=&{v[0:15]}"(w[0]), "=&{v[16:31]}"(w[1]), "=&{v[32:47]}"(w[2]), "=&{v[48:63]}"(w[3]), "=&{v[64:79]}"(w[4]), "=&{v[80:95]}"(w[5]), "=&{v[96:111]}"(w[6]),
                       [c] "+v"(c), [keep] "=&s"(keep), [pos] "=&s"(pos), [e] "=&s"(e), [nx] "=&s"(ch), [so] "=&s"(so)
                     : [em] "s"(__builtin_amdgcn_ballot_w64(tid == 0)), [ep] "s"(__builtin_amdgcn_ballot_w64(tid == 0 && prev >= 0)), [qu] "s"(queue), [li] "s"(list),
                       [qc] "s"(tmo + W_QCNT + max(prev, 0) * 32), [wb] "s"(pin_s(((nq * 8 + wid) * 128 + hb * 4) * 1024)), [rw] "s"(mk_rs(W, HD * DM * 2)), [dv] "v"((unsigned)lane * 16u),
                       [rem] "s"(0), [fr] "v"(lbase + 140040), [lb] "s"(pin_s(lbase + 65536 + wid * 1024)), [sb] "s"(sb), [kq] "s"(kq), [ra] "s"(rsA)
                     : "memory", "scc", "v168", "v169", "v172", "v173");
        attn::listed(tmo + W_LIST, prev, c); }
#pragma unroll 1
    for (;; im ^= 1u) {
        if (e == 0u) {
            VMWAIT(0);
            LDS_BARRIER();
            if (tid == 0) { if (dr >= 0) cnt_add(fin + (dr >> 2)); if (last >= 0) cnt_add(fin + (last >> 2)); }
            dr = -1; last = -1;
            if (pos >= (unsigned)NSEG) break;
            if (wid == 0) {
                const unsigned lw = wave_wait<20>(list, 1u, tmo, 0x60000u + pos, 15u << ((pos >> 3) * 4));
                if (lane == 0) lf[im * 2 + 1] = lw; }
            LDS_BARRIER();
            ch = (int)(((__builtin_amdgcn_readfirstlane(lf[im * 2 + 1]) >> ((pos >> 3) * 4)) - 1u) & 7u) * 8 + (int)(pos & 7u);
            fetch(ch, im);
            VMWAIT(0);
        }
        __builtin_amdgcn_s_barrier(); asm volatile("" ::: "memory");
        unsigned keep, nx, so;
        asm volatile(ASM_OPROJ : [keep] "=&s"(keep), [pos] "=&s"(pos), [e] "=&s"(e), [nx] "=&s"(nx), [so] "=&s"(so)
                     : "{v[0:15]}"(w[0]), "{v[16:31]}"(w[1]), "{v[32:47]}"(w[2]), "{v[48:63]}"(w[3]), "{v[64:79]}"(w[4]), "{v[80:95]}"(w[5]), "{v[96:111]}"(w[6]),
                       [xa] "v"(lbase + im * 65536 + lane * 16), [fr] "v"(lbase + 140032 + im * 8), [fw] "v"(lbase + 140040 - im * 8),
                       [em] "s"(__builtin_amdgcn_ballot_w64(tid == 0)), [dr] "s"(dr), [fin] "s"(fin), [qu] "s"(queue), [li] "s"(list),
                       [lb] "s"(pin_s(lbase + 65536 - im * 65536 + wid * 1024)), [sb] "s"(sb), [ch] "s"(ch), [kq] "s"(kq), [dv] "v"((unsigned)lane * 16u),
                       [ra] "s"(rsA), [co] "v"(co), [rc] "s"(rsC)
                     : "memory", "scc", "vcc", CL128_159, CL160_191);
        dr = last; last = ch * 128; ch = (int)nx;
    }
}
__device__ __forceinline__ unsigned slot_off(unsigned j, unsigned pv) {
    const unsigned o = j * 16384u; return o + (((pv >> (((pv & 0x1FC000u) + o) >> 21)) & 1u) << 13); }
__device__ __forceinline__ unsigned calibrate(__amdgpu_buffer_rsrc_t rs, unsigned pg, unsigned pv0, unsigned* dbg) {
    const unsigned first = pg == 0 ? 0u : (pg << 21) - pv0;
    if (first + 16384u > (unsigned)SZ_RING) return 0u;
    unsigned best[2] = {~0u, ~0u};
    if (CALA) {
        unsigned v = 0;
        if ((threadIdx.x & 63) < 2 * CALN) v = (unsigned)__builtin_amdgcn_raw_ptr_buffer_atomic_add_i32(0, rs, first + (threadIdx.x & 1) * 8192u + ((threadIdx.x & 63) >> 1) * 384u, 0, AUX_SC1);
        asm volatile("s_waitcnt vmcnt(0)" : "+v"(v) :: "memory"); }
#pragma unroll 1
    for (int i = 0; i < CALN; ++i) {
#pragma unroll
        for (int b = 0; b < 2; ++b) {
            const unsigned long long t0 = __builtin_amdgcn_s_memtime();
            asm volatile("s_waitcnt lgkmcnt(0)" ::: "memory");
            unsigned v = __builtin_amdgcn_raw_buffer_load_b32(rs, first + b * 8192u + (unsigned)i * 384u, 0, AUX_SC1);
            asm volatile("s_waitcnt vmcnt(0)" : "+v"(v) :: "memory");
            const unsigned long long t1 = __builtin_amdgcn_s_memtime();
            asm volatile("s_waitcnt lgkmcnt(0)" ::: "memory");
            best[b] = min(best[b], (unsigned)(t1 - t0));
        }
    }
    return best[1] < best[0] ? 1u : 0u;
}
__host__ __device__ __forceinline__ void role_map(int b, int& xg, int& jb) {
    const int g = (GMAP >> ((b & 7) * 4)) & 7, j = b >> 3;
    if (g >= 3) { xg = 3; jb = j * 5 + g - 3; }
    else if (MIX == 2) { xg = g == 2 ? 2 : (j + g) & 1; jb = g == 2 ? j : (j & ~1) + g; }
    else { xg = MIX ? (j + g) % 3 : g; jb = j; }
}
__device__ __forceinline__ void tiles_w(u32x8 (&w)[16], int i, const char* p, int wid, int lane) {
    const unsigned r = 4 * (lane >> 4) + ((lane & 15) >> 2), c = 4 * (wid & 3) + ((lane >> 1) & 1);
    p += (lane & 1) * 8;
#pragma unroll
    for (int t = 0; t < 2; ++t) w[8 * t + i] = ilv(tr_rdu(p + off_b(r, c + 2 * t)), tr_rdu(p + 8192 + off_b(r, c + 2 * t)), tr_rdu(p + off_b(r + 16, c + 2 * t)), tr_rdu(p + 8192 + off_b(r + 16, c + 2 * t)));
}
__device__ __forceinline__ unsigned img_place(int wid, int l16) { return 32 * ((wid >> 1) & 1) + 16 * ((l16 >> 2) & 1) + 8 * (wid & 1) + 4 * (l16 >> 3) + (l16 & 3); }
#define W16(w) [w0] "v"(w[0]), [w1] "v"(w[1]), [w2] "v"(w[2]), [w3] "v"(w[3]), [w4] "v"(w[4]), [w5] "v"(w[5]), [w6] "v"(w[6]), [w7] "v"(w[7]), \
               [w8] "v"(w[8]), [w9] "v"(w[9]), [w10] "v"(w[10]), [w11] "v"(w[11]), [w12] "v"(w[12]), [w13] "v"(w[13]), [w14] "v"(w[14]), [w15] "v"(w[15])
__device__ __forceinline__ void vox_ws(const bf16* __restrict__ x, const bf16* __restrict__ Wv, const bf16* __restrict__ Wo, int h,
                                       u32x4 rsWu, unsigned* done, const Hello& hi, char* lds, int tid) {
    const int wid = __builtin_amdgcn_readfirstlane(tid >> 6), lane = tid & 63, l16 = lane & 15, g4 = lane >> 4;
    const unsigned lbase = (unsigned)(uintptr_t)lds;
    const u32x4 rsA = mk_rs(x, SEQ * DM * 2);
    const unsigned dvo = (unsigned)(((wid & 3) * 8 + (lane >> 3)) * 1024 + ((lane & 7) ^ (lane >> 3)) * 16);
    auto fetch = [&](int n) {
        if (wid < 4) {
#pragma unroll
            for (int i = 0; i < 8; ++i) dma16<false>(rsA, dvo, n * 32768 + i * 128, lbase + XIMG(n) + (8 * wid + i) * 1024); } };
    fetch(0);
    bf16x8 wf[32];
    if (wid < 4) {
        unsigned long long hv = 0;
        stream_w(Wv + h * DH, HD * 2, [&](int i, int p) { return (unsigned)((64 * i + 16 * p) * (HD * 2)); }, [&](int i, const char* p) { cols32(wf, i, p, wid, lane);
                     if (wid == 0 && i == 4) hv = hi.peek(); }, lds, wid, lane);
        if (wid == 0) hi.wait(hv);
        asm volatile("" ::: "memory");
    } else
        stream_w(Wo + h * DH * DM, DM * 2, [&](int i, int p) { return (unsigned)((64 * (i & 1) + 16 * p) * (DM * 2) + (i >> 1) * 256); }, [&](int i, const char* p) {
                     if (wid == 4 + (i >> 1)) {
#pragma unroll
                         for (int k = 0; k < 16; ++k) wf[4 * (k >> 1) + 2 * (i & 1) + (k & 1)] = cat8(tr_rd(p + (k & 1) * 8192 + tr_addr16(lane, k >> 1, 0)), tr_rd(p + (k & 1) * 8192 + tr_addr16(lane, k >> 1, 1))); } }, lds, wid, lane);
    __syncthreads();
    fetch(1); fetch(2);
    u32x16 w[8];
#pragma unroll
    for (int i = 0; i < 8; ++i) w[i] = cat16(wf[4 * i], wf[4 * i + 1], wf[4 * i + 2], wf[4 * i + 3]);
    PRB(2);
    unsigned n, cnt = 0;
    if (wid < 4) {
        unsigned keep, im, t0, so = 3u << 15, vw = lbase + 8192 + wid * 2048 + lane * 16;
        asm volatile(ASM_VOXA : [n] "=&s"(n), [keep] "=&s"(keep), [im] "=&s"(im), [t0] "=&s"(t0), [so] "+s"(so), [vw] "+v"(vw)
                     : W128(w), [xb] "v"(lbase + 32768 + (l16 >> 3) * 16384 + (l16 & 7) * 128 + (g4 ^ (l16 & 7)) * 16), [lb] "s"(pin_s(lbase + 32768 + wid * 8192)), [ra] "s"(rsA), [dv] "v"(dvo)
                     : "memory", "scc", CL128_159, CL160_191);
    } else {
        unsigned t1, t2, sp = 0, vi = lbase + lane * 16;
        if (VXP) __builtin_amdgcn_s_setprio(1);
        asm volatile(ASM_VOXB : [n] "=&s"(n), [t1] "=&s"(t1), [t2] "=&s"(t2), [cnt] "+s"(cnt), [sp] "+s"(sp), [vi] "+v"(vi)
                     : W128(w), [st] "v"((unsigned)(OFF_VOX + (8 * (wid & 3) * 32 + h) * 1024 + lane * 16)), [rw] "s"(rsWu), [em] "s"(__builtin_amdgcn_ballot_w64(tid == 256)), [dn] "s"(done)
                     : "memory", "scc", CL128_159, CL160_191);
        __builtin_amdgcn_s_setprio(0);
    }
    VMWAIT(0);
    __syncthreads();
    if (tid == 256) for (; cnt < (unsigned)NSEG; ++cnt) cnt_add(done + 32 * cnt);
}
__device__ __forceinline__ u32x2 mix4(f32x4 a, f32x4 b, float g) {
    const float og = rbf(1.f - g);
    float r[4];
#pragma unroll
    for (int j = 0; j < 4; ++j) r[j] = rbf(g * rbf(a[j])) + rbf(og * b[j]);
    return u32x2{cvtpk(r[0], r[1]), cvtpk(r[2], r[3])}; }
__global__ __launch_bounds__(512, 2) void k_chain(const bf16* __restrict__ mem0, const bf16* __restrict__ Wq, const bf16* __restrict__ Wk,
                                                  const bf16* __restrict__ Wv, const bf16* __restrict__ Wo,
                                                  const bf16* __restrict__ x, char* __restrict__ ws,
                                                  const bf16* __restrict__ ckv, const bf16* __restrict__ awq, const bf16* __restrict__ awuk,
                                                  const bf16* __restrict__ awuv, const bf16* __restrict__ awo,
                                                  const bf16* __restrict__ gw, bf16* __restrict__ out) {
    extern __shared__ __attribute__((aligned(16))) char lds[];
    const int tid = threadIdx.x, wid = __builtin_amdgcn_readfirstlane(tid >> 6), lane = tid & 63;
    unsigned* tmo = (unsigned*)(ws + OFF_TMO);
    int xg, jb; role_map(blockIdx.x, xg, jb);
    char* const wsP = ws + ((0u - (unsigned)(uintptr_t)ws) & 16383u);
    unsigned pv = (unsigned)(uintptr_t)wsP & 0x1FC000u;
    const __amdgpu_buffer_rsrc_t rsP = __builtin_amdgcn_make_buffer_rsrc((void*)wsP, 0, (int)SZ_PREG, 0x00020000);
    const u32x4 rsPu = mk_rs(wsP, (unsigned)SZ_PREG);
    const __amdgpu_buffer_rsrc_t rsW = __builtin_amdgcn_make_buffer_rsrc((void*)ws, 0, (int)WS_END, 0x00020000);
    const unsigned lbase = (unsigned)(uintptr_t)lds;
    const u32x4 PZ = {0xFFFFFFFFu, 0xFFFFFFFFu, 0xFFFFFFFFu, 0xFFFFFFFFu};
    auto anchor_off = [&](int s) { return slot_off((unsigned)(s & (NR - 1)) * SLOTS + SL_P + ((s >> 2) & 3), pv) + ANCHOR + (s >> 4) * 768; };
    auto anchor = [&](int s, int tries) {
        unsigned v = 0;
        if (ANCH) {
            const unsigned o = anchor_off(s);
#pragma unroll 1
            for (int i = 0; i < tries; ++i) {
                v = (unsigned)__builtin_amdgcn_readfirstlane(__builtin_amdgcn_raw_buffer_load_b32(rsP, 0, o, AUX_SC1));
                if (v != ~0u) break; } }
        return v; };
    const Hello hi = {(unsigned long long*)(ws + OFF_MAIL + 32768) + blockIdx.x, __builtin_amdgcn_s_memrealtime()};
    if (tid < 16) __hip_atomic_store((unsigned long long*)(ws + OFF_MAIL) + blockIdx.x * 16 + tid, hi.num, __ATOMIC_RELAXED, __HIP_MEMORY_SCOPE_AGENT);
    GST(0);
    PRB(1);
    if (xg == 3) {
        const int w = jb, j = blockIdx.x >> 3, g5 = w - 5 * j;
        int tz = threadIdx.x; asm volatile("" : "+v"(tz));
        int k0 = g5, L0 = -1;
        u32x16 wx[4];
        if (w < 32) vox_ws(x, Wv, Wo, w, mk_rs(ws, (unsigned)WS_END), tmo + W_VO, hi, lds, tz);
        else if (w < 64) {
            const int z = (w >> 4) & 1, panel = w & 15;
            proj_ws<true>(x, (z == 0 ? Wq : Wk) + panel * 256, (bf16*)(ws + OFF_QB + (size_t)z * SZ_ACT) + panel * 8192, 0, 1, tmo + W_PROG + 32 * (w - 32), z == 1 ? attn::C2 : 1.f, awo + (w - 32) * DH * DM, wx, hi, lds, tz);
            int tid = threadIdx.x; asm volatile("" : "+v"(tid));
            const int lane = tid & 63;
#pragma unroll
            for (int i = 0; i < 16; ++i) { const int id = tid + 512 * i; const int r = id >> 6, c = id & 63; const u32x16 v = wx[i >> 2];
                *(u32x4*)(lds + ((r >> 5) * 4 + (c >> 4)) * 8192 + off_b(r & 31, c & 15)) = u32x4{v[(i & 3) * 4], v[(i & 3) * 4 + 1], v[(i & 3) * 4 + 2], v[(i & 3) * 4 + 3]}; }
            __syncthreads();
#pragma unroll
            for (int i = 0; i < 16; ++i) { const char* img = lds + ((i & 3) * 4 + (wid >> 1)) * 8192;
                __builtin_amdgcn_raw_buffer_store_b128(__builtin_bit_cast(u32x4, cat8(tr_rd(img + tr_addr16(lane, 4 * (wid & 1) + (i >> 2), 0)), tr_rd(img + tr_addr16(lane, 4 * (wid & 1) + (i >> 2), 1)))),
                                                       rsW, lane * 16 + (i & 3) * 1024, (unsigned)OFF_WT + ((4 * wid + (i >> 2)) * 128 + 4 * (w - 32)) * 1024, AUX_SC1); }
            VMWAIT(0);
            __syncthreads();
            if (tid == 0) __hip_atomic_fetch_add(tmo + W_CTRA, 0x10000u, __ATOMIC_RELAXED, __HIP_MEMORY_SCOPE_AGENT);
        } else {
            const int i = j == 12 ? 19 : j - 13, e = g5 + i - 18;
            const int pair = i < 18 ? 3 * g5 + (g5 == 4) + i / 6 : 12, z = i < 18 ? (i % 6) >> 1 : e >> 1, half = (i < 18 ? i : e) & 1, n0 = pair * 256;
            if (TCH && w > 64 && w < 129) {
                const int t = w - 65;
                const u32x4 rt = mk_rs(t < 32 ? Wo + t * DH * DM : (t < 48 ? Wq : Wk) + (t & 15) * 256 + 128, t < 32 ? DH * DM * 2 : DM * HD * 2 - 8192 + 256);
                const unsigned vt = t < 32 ? (unsigned)tid * 16u : (unsigned)((tid >> 4) * (HD * 2) + (tid & 15) * 16), it = t < 32 ? 8192u : HD * 64u;
#pragma unroll
                for (int q = 0; q < 16; ++q) dma16<false>(rt, vt, q * it, lbase + wid * 1024); }
            if (XT && w > 128 && w < 129 + 5 * XT) {
#pragma unroll
                for (int q = 0; q < 4; ++q) dma16<false>(mk_rs(x, SEQ * DM * 2), (unsigned)lane * 16u, (unsigned)(((w - 124) / 5) * 32768 + (wid * 4 + q) * 1024), lbase + wid * 1024); }
            if (WARM && w < 128) {
#pragma unroll
                for (int q = 0; q < 4; ++q) dma16<false>(mk_rs(ckv, SEQ * DM * 2), (unsigned)lane * 16u, (unsigned)((w - 64) * 32768 + (wid * 4 + q) * 1024), lbase + wid * 1024); }
            const unsigned ts = (unsigned)__builtin_amdgcn_s_memtime();
            if (w == MASTER) {
#pragma unroll
                for (int q = 0; q < 8; ++q) __builtin_amdgcn_raw_buffer_store_b128(u32x4{}, rsW, tid * 16 + q * 8192, (unsigned)OFF_TMO, AUX_SC1);
                VMWAIT(0);
                __syncthreads();
                if (wid < 4) {
                    unsigned long long was = 0;
#pragma unroll 1
                    for (unsigned i = 0; i < SPIN_CAP; ++i) {
                        const unsigned long long v = __hip_atomic_load((unsigned long long*)(ws + OFF_MAIL) + tid * 16, __ATOMIC_RELAXED, __HIP_MEMORY_SCOPE_AGENT);
                        if (i == 0 || v != was) __hip_atomic_store(hi.rep - blockIdx.x + tid, v, __ATOMIC_RELAXED, __HIP_MEMORY_SCOPE_AGENT);
                        was = v;
                        if (__builtin_amdgcn_ballot_w64(v + 5000u - hi.num > (1u << 24)) == 0) break; } }
            } else if (wid == 0) { until(ts, ADELAY / 2); hi.wait(hi.peek()); }
            if (ADELAY) until(ts, ADELAY);
            VMWAIT(0);
            proj_ws<false>(z == 0 ? x : ckv, (z == 0 ? awq : z == 1 ? awuk : awuv) + n0, (bf16*)(ws + OFF_QA + (size_t)z * SZ_ACT) + n0 * 32, half, 2, nullptr, z == 0 ? attn::C2 : 1.f, nullptr, wx, hi, lds, tz);
            if (tid == 0) { cnt_add(tmo + W_CTRP + 32 * pair); cnt_add(tmo + W_CTRA); }
            if (g5 < 3 && i == 18) { k0 = g5 < 2 ? 3 : 4; L0 = g5 == 0 ? 19 : 20; } else L0 = i;
            GST(1);
            if (wid == 0) wave_wait<8>(tmo + W_CTRP + 32 * ((k0 < 3 ? 6 * k0 + L0 % 6 : 7 * k0 - 3 + L0 % 7) >> 1), 6u, tmo, 0x30001u);
            __syncthreads();
        }
        GST(2);
        const int prev = attn::attn_phase(ws + OFF_QA, tmo + W_AQ, tmo + W_QCNT, tmo + W_LIST, g5, k0, L0, tmo, lds, (volatile LAS int*)(lds + 147456));
        GST(3);
        PRB(12);
        int tq = threadIdx.x; asm volatile("" : "+v"(tq));
        oproj_ws(g5, j & 3, tmo + W_OQ + (g5 * 4 + (j & 3)) * 32, (const bf16*)(ws + OFF_OA), (const bf16*)(ws + OFF_WT), (float*)(ws + OFF_PART), prev, tmo, lds, tq);
        GST(4);
        PRB(11);
        if (w < 2 * SPLIT) {
            const int wq = __builtin_amdgcn_readfirstlane(tq >> 6), lq = tq & 63, row = w * 16 + (lq & 15);
            if (wq == 0) wave_wait<20>(tmo + W_FA + 32 * (w >> 1), w < 2 * FSP ? 20u : 52u, tmo, 0xA0000u + w);
            if (wq == 1 && w < 2 * FSP) wave_wait<20>(tmo + W_PG, (w >> 1) + 5u, tmo, 0xA1000u + w);
            __syncthreads();
            const unsigned ti = (unsigned)(((w >> 1) * 32 + 4 * wq) * 2 + (w & 1));
            f32x4 qa[4][5]; u32x2 bb[4];
#pragma unroll
            for (int c = 0; c < 4; ++c) {
#pragma unroll
                for (int k = 0; k < 5; ++k) qa[c][k] = __builtin_bit_cast(f32x4, __builtin_amdgcn_raw_buffer_load_b128(rsW, (unsigned)OFF_PART + (ti + 2 * c) * 1024 + lq * 16, (unsigned)(k * SEQ * DM * 4), AUX_SC1));
                bb[c] = __builtin_amdgcn_raw_buffer_load_b64(rsW, (unsigned)OFF_BB + (ti + 2 * c) * 512 + lq * 8, 0, AUX_SC1); }
            const float g = __uint_as_float(__builtin_amdgcn_raw_buffer_load_b32(rsW, (unsigned)(OFF_G + row * 4), 0, AUX_SC1));
#pragma unroll
            for (int c = 0; c < 4; ++c) {
                const f32x4 bs = {__uint_as_float(bb[c][0] << 16), __uint_as_float(bb[c][0] & 0xFFFF0000u), __uint_as_float(bb[c][1] << 16), __uint_as_float(bb[c][1] & 0xFFFF0000u)};
                *(u32x2*)(out + (size_t)row * DM + (4 * wq + c) * 16 + 4 * (lq >> 4)) = mix4(((qa[c][0] + qa[c][1]) + (qa[c][2] + qa[c][3])) + qa[c][4], bs, g); }
        }
    } else if (xg == 0) {
        int tid = threadIdx.x; asm volatile("" : "+v"(tid));
        const int lane = tid & 63, l16 = lane & 15, g4 = lane >> 4, sidx = l16 < 8 ? 0x4444 : 0xEEEE;
        const int h = jb;
        const unsigned q2 = l16 >> 2, p2 = lane & 3;
        u32x8 w[16];
        {   unsigned long long hv = 0;
            stream_w((wid < 4 ? Wk : Wq) + h * DH, HD * 2, [&](int i, int p) { return (unsigned)((64 * i + 16 * p) * (HD * 2)); }, [&](int i, const char* p) { tiles_w(w, i, p, wid, lane);
                         if (wid == 0 && i == 4) hv = hi.peek(); }, lds, wid, lane);
            if (wid == 0) hi.wait(hv); }
        __syncthreads();
        constexpr unsigned LM = 0, LQ = 8192, LE = 24576, LK = 32768;
        const unsigned r8 = l16 & 7, pr8 = (r8 & 1) | ((r8 & 2) << 1) | ((r8 & 4) >> 1);
        const unsigned ma = lbase + LM + (4 * (l16 >> 3) + g4) * 128 + pr8 * 16;
        GST(1);
        PRB(9);
        *(u32x4*)(lds + LM + tid * 16) = *(const u32x4*)(mem0 + (tid & 7) * DM + (tid >> 3) * 8);
#pragma unroll
        for (int i = 0; i < 3; ++i) *(u32x4*)(lds + LQ + i * 8192 + tid * 16) = PZ;
        pv |= wave_wait<8>(tmo + W_CAL + (h & 15) * 32, (KPOIS ? NH : NPAGE) << 16, tmo, 0xB0000u) & 0x3FFFu;
        GST(2);
        const unsigned qw = lbase + LQ + (wid < 4 ? 2048 : 0) + img_place(wid, l16) * 16 + (2 * (g4 & 1) + (g4 >> 1)) * 4;
        const unsigned fr = lbase + LQ + (4 * g4 + q2) * 16 + (p2 & 1) * 8;
        const unsigned kxa = lbase + LK + (l16 >> 3) * 1024 + (g4 >> 1) * 2048 + (g4 & 1) * 256 + (8 * ((l16 >> 1) & 1) + 2 * ((l16 >> 2) & 1) + (l16 & 1)) * 16;
        unsigned cap = SPIN_CAP >> 2, cnt;
#define H_OUT [cnt] "=&s"(cnt), [cap] "+s"(cap)
#define H_OPS W16(w), [ix] "v"(sidx), [ma] "v"(ma), [qw] "v"(qw + (s & 1) * 8192), [qo] "v"(qw + (~s & 1) * 8192), [tg] "v"(s + 1), [s] "s"(s)
#define H_CLOB "memory", "vcc", "scc", "s94", "s95", CL8(136, 137, 138, 139, 140, 141, 142, 143), CL8(144, 145, 146, 147, 148, 149, 150, 151), CL8(152, 153, 154, 155, 156, 157, 158, 159), \
                CL8(160, 161, 162, 163, 164, 165, 166, 167), CL8(168, 169, 170, 171, 172, 173, 174, 175)
        if (wid >= 4) __builtin_amdgcn_s_setprio(1);
        if (wid == 7) {
            unsigned fb = 0, ao = anchor_off(0), due = 0, dl = 0;
            int lastf = 0;
            LDS_BARRIER();
#pragma unroll 1
            for (int s = 0; s < NSEG; ++s) {
                ST(0);
                unsigned keep, f2, tm;
                asm volatile(ASM_HP : H_OUT, [keep] "=&s"(keep), [f2] "=&s"(f2), [tm] "=&s"(tm)
                             : H_OPS, [o] "v"((unsigned)lane * 16u), [rs] "s"(rsPu), [fb] "s"(fb), [lm] "s"(lbase + LM), [due] "s"(due), [dp] "s"(pin_s(due + s + dl)),
                               [k0] "s"(pin_s((APEN << 6) - s - dl)), [k1] "s"(pin_s(((APEN - AFM) << 6) - s - dl)), [l1] "s"((APEN + 16u) << 6),
                               [av] "v"(h == 0 ? min(lane, 11) * 64u : 0x7FFFFFFFu), [ao] "s"(ao) : H_CLOB);
                DUR(1, tm >> 6);
                if (ADS && cnt) {
                    if (s >= AS0 && (unsigned)(s - lastf - 2) <= ADW - 2u) dl = min(dl + ((dl == 0 && s < 16 ? ADS0 : ADS) << 6), ADM << 6);
                    lastf = s; }
                else if (ADD && dl && (s - lastf) % ADD == 0) dl -= 64u;
                ST(3);
                SP(12, cnt);
                if (__builtin_expect(cap == 0, 0)) __hip_atomic_store(tmo, 0x10000u + h, __ATOMIC_RELAXED, __HIP_MEMORY_SCOPE_AGENT);
                const unsigned t0 = (unsigned)__builtin_amdgcn_s_memtime();
                if (s < JIT) LDS_BARRIER();
                fb = pin_s(slot_off((unsigned)((s + 1) & (NR - 1)) * SLOTS + SL_MEM, pv));
                ao = pin_s(anchor_off(min(s + 1, NSEG - 1)));
                until(t0, ADT);
                const unsigned aw = anchor(s, 4);
                due = pin_s((aw & ~63u) + (AM << 6));
                until_a(aw, s, AM, t0, MDT);
            }
        } else if (wid == 4) {
            unsigned kb = 0, cn = 1u << 16;
            LDS_BARRIER();
            const unsigned pb = ((h & 7) >> 1) * 1280;
            const unsigned pxo = pb + (h & 1) * 512 + g4 * 128 + r8 * 16 + (l16 >> 3) * 8, pmo = lane & 40 ? 0x7FFFFFFFu : pb + 1024 + (g4 & 1) * 128 + r8 * 16 + (h & 1) * 8;
#pragma unroll 1
            for (int s = 0; s + 1 < NSEG; ++s) {
                const unsigned po = slot_off((unsigned)(s & (NR - 1)) * SLOTS + SL_P + (h >> 3), pv), pq = slot_off((unsigned)((s - 1) & (NR - 1)) * SLOTS + SL_P + (h >> 3), pv);
                ST(0);
                VMWAIT(0);
                asm volatile(ASM_H4 : H_OUT, [cn] "+s"(cn)
                             : H_OPS, [kx] "v"(kxa + kb), [ta] "v"(lbase + LQ + 4096 + (lane & 1) * 128 + (lane & 2) * 256 + (lane & 4) * 512 + (s & 1) * 8192), [fr] "v"(fr + (s & 1) * 8192),
                               [er] "v"(lbase + LE + (l16 >> 3) * 512 + g4 * 128 + r8 * 16), [sr] "v"(lbase + LE + 1024 + (s & 1) * 256 + r8 * 32 + (g4 & 1) * 16),
                               [px] "v"(pxo), [pm] "v"(pmo), [rs] "s"(rsPu), [po] "s"(po), [pq] "s"(pq), [lo] "s"(0x21800000u), [sp] "s"(0x3c000000u)
                             : H_CLOB, CL8(176, 177, 178, 179, 180, 181, 182, 183), CL8(184, 185, 186, 187, 188, 189, 190, 191), CL192_223, "v224", "v225", "v226", "v227");
                ST(5);
                kb = kb == 32768 ? 0 : kb + 16384;
                if (s < JIT) LDS_BARRIER();
            }
            { const int s = NSEG - 1; asm volatile(ASM_HQ : H_OUT : H_OPS : H_CLOB); }
        } else if (wid > 4) {
            const int rt = wid - 5;
            const unsigned kra = LK + (l16 >> 3) * 1024 + g4 * 256 + r8 * 16, qa = LK + 8192 + rt * 1024 + lane * 16;
            const unsigned sw = lbase + LE + 1024 + r8 * 32 + (g4 & 1) * 16 + rt * 8 + (g4 >> 1) * 4;
            const int i = rt * 16 + l16 - (16 * (g4 >> 1) + 4 * (g4 & 1));
            const f32x4 NEG4 = {-__builtin_inff(), -__builtin_inff(), -__builtin_inff(), -__builtin_inff()};
            unsigned kb = 0;
            f32x4 X[2];
            auto own = [&]() {
                X[0] = f32x4{}; X[1] = f32x4{};
#pragma unroll
                for (int ks = 0; ks < 4; ++ks) { const u32x4 qf = *(const u32x4*)(lds + qa + kb + ks * 2048);
                    X[0] = mfma32(*(const u32x4*)(lds + kra + kb + ks * 2048), qf, X[0]); X[1] = mfma32(*(const u32x4*)(lds + kra + kb + ks * 2048 + 128), qf, X[1]); }
#pragma unroll
                for (int r = 0; r < 4; ++r) { if (r > i) X[0][r] = NEG4[0]; if (8 + r > i) X[1][r] = NEG4[0]; }
                asm volatile("" : "+v"(X[0]), "+v"(X[1])); };
            LDS_BARRIER();
            own();
#pragma unroll 1
            for (int s = 0; s < NSEG; ++s) {
                ST(0);
                asm volatile(ASM_H5 : H_OUT
                             : H_OPS, [kx] "v"(kxa + rt * 64 + kb), [fr] "v"(fr + (s & 1) * 8192), [em] "s"(0x00FF00FF00FF00FFull),
                               [ew] "v"(lbase + LE + rt * 512 + g4 * 128 + r8 * 16), [sw] "v"(sw + (s & 1) * 256), [so] "v"(sw + (~s & 1) * 256)
                             : H_CLOB, CL8(176, 177, 178, 179, 180, 181, 182, 183), CL8(184, 185, 186, 187, 188, 189, 190, 191), CL8(208, 209, 210, 211, 212, 213, 214, 215));
                u32x4 qf[4];
#pragma unroll
                for (int ks = 0; ks < 4; ++ks) qf[ks] = *(const u32x4*)(lds + qa + (g4 >> 1) * 1536 + kb + (ks & 1) * 4096 + (ks >> 1) * 512);
                for (int n = 0; n < (1 << 14) && __builtin_amdgcn_ballot_w64(*(volatile LAS int*)(size_t)(lbase + LQ + 6144 + (lane & 1) * 128 + (lane & 2) * 256 + (s & 1) * 8192) != s + 1) != 0; ++n) {}
                asm volatile("" ::: "memory");
                f32x4 S[3] = {f32x4{}, X[0], X[1]};
#pragma unroll
                for (int ks = 0; ks < 4; ++ks) {
                    const char* p = lds + (fr - lbase) + 2048 + (s & 1) * 8192 + ks * 512;
                    const u32x2 a = tr_rdu(p), b = tr_rdu(p + 256);
                    S[0] = mfma32(u32x4{a[0], a[1], b[0], b[1]}, qf[ks], S[0]); }
                kb = kb == 32768 ? 0 : kb + 16384;
                if (g4 < 2) S[0] = NEG4;
                {
                    float mx = fmaxf(fmaxf(S[0][0], S[0][1]), fmaxf(S[0][2], S[0][3]));
#pragma unroll
                    for (int t = 1; t < 3; ++t) mx = fmaxf(mx, fmaxf(fmaxf(S[t][0], S[t][1]), fmaxf(S[t][2], S[t][3])));
                    mx = xg_max(mx);
#pragma unroll
                    for (int t = 0; t < 3; ++t)
#pragma unroll
                        for (int r = 0; r < 4; ++r) S[t][r] = __builtin_amdgcn_exp2f(S[t][r] - mx);
                    const f32x4 s4 = (S[0] + S[1]) + S[2];
                    const float inv = __builtin_amdgcn_rcpf(xg_sum((s4[0] + s4[1]) + (s4[2] + s4[3])));
#pragma unroll
                    for (int t = 0; t < 3; ++t) S[t] *= inv; }
                const unsigned po = (unsigned)(OFF_PR + (size_t)(s * 4 + (h >> 3)) * 16384);
                if (rt || lane < 32)
                    __builtin_amdgcn_raw_buffer_store_b128(u32x4{cvtpk(S[1][0], S[1][1]), cvtpk(S[1][2], S[1][3]), cvtpk(S[2][0], S[2][1]), cvtpk(S[2][2], S[2][3])}, rsW,
                                                           (rt ? (h & 7) * 1024 : 8192 + (h & 7) * 512) + lane * 16, po, AUX_SC1);
                const auto sv = __builtin_amdgcn_permlane16_swap(cvtpk(S[0][0], S[0][1]), cvtpk(S[0][2], S[0][3]), false, false);
                if (g4 >= 2) __builtin_amdgcn_raw_buffer_store_b64(u32x2{sv[0], sv[1]}, rsW, 12288 + rt * 2048 + (h & 7) * 256 + l16 * 16 + (g4 & 1) * 8, po, AUX_SC1);
                ST(5);
                if (s < JIT) LDS_BARRIER();
                own();
                VMWAIT(0);
                if (lane == 0 && s >= NSEG - 2) cnt_add(tmo + W_RDY + s * 32);
            }
        } else {
            const u32x4 rsK = mk_rs(ws + OFF_QB, (unsigned)(2 * SZ_ACT));
            unsigned kn = 0, kb = 0, tf = 0;
            unsigned* const pk = tmo + W_PROG + 32 * (h >> 1) + (lane & 1) * 512;
            auto look = [&]() { const unsigned v = __hip_atomic_load(pk, __ATOMIC_RELAXED, __HIP_MEMORY_SCOPE_AGENT);
                kn = min((unsigned)__builtin_amdgcn_readlane(v, 0), (unsigned)__builtin_amdgcn_readlane(v, 1)); };
            auto fetch = [&](int sg) {
#pragma unroll 1
                for (unsigned spins = 0; kn <= (unsigned)sg; ++spins) {
                    if (spins) __builtin_amdgcn_s_sleep(1);
                    look();
                    if (bail(spins, SPIN_CAP << 1, tmo, 0x51000u + (unsigned)(sg << 8) + h)) break; }
#pragma unroll
                for (int i = 0; i < 16; ++i) dma16<true>(rsK, (unsigned)lane * 16u, (unsigned)((i < 8 ? SZ_ACT : 0) + (sg * 32 + h) * 8192 + (i & 7) * 1024), lbase + LK + kb + i * 1024);
                kb = kb == 32768 ? 0 : kb + 16384; };
            if (wid == 3) { fetch(0); if (LOOK) look(); VMWAIT(0); }
            LDS_BARRIER();
#pragma unroll 1
            for (int s = 0; s < NSEG; ++s) {
                VMWAIT(0);
                if (wid == 3) DUR(0, DURT() - tf);
                ST(0);
                asm volatile(ASM_HK : H_OUT : H_OPS, [c2] "s"(attn::C2) : H_CLOB);
                if (wid == 3) {
                    if (FDT) until((unsigned)__builtin_amdgcn_s_memtime(), FDT);
                    if (h == 1 && lane == 0) __hip_atomic_store(tmo + W_PG, (unsigned)s, __ATOMIC_RELAXED, __HIP_MEMORY_SCOPE_AGENT);
                    if (s < JIT) { fetch(s + 1); if (LOOK) look(); VMWAIT(0); } }
                if (s < JIT) LDS_BARRIER();
                tf = DURT();
                if (wid == 3 && s + 1 >= JIT && s + 2 < NSEG) { fetch(s + 2); if (kn <= (unsigned)s + 3u) look(); }
            }
        }
        DUROUT(0);
        GST(3);
        PRB(5);
    } else if (xg == 1) {
        int tid = threadIdx.x; asm volatile("" : "+v"(tid));
        const int lane = tid & 63, l16 = lane & 15, g4 = lane >> 4, sidx = l16 < 8 ? 0x4444 : 0xEEEE;
        const int h = jb;
        if (h < NPAGE) {
            if (wid == 0) { const unsigned b = calibrate(rsP, h, pv, (unsigned*)(ws + WS_END + 79000 * 8));
                hi.wait(hi.peek());
                if (lane < 16) __hip_atomic_fetch_add(tmo + W_CAL + lane * 32, 0x10000u + (b << h), __ATOMIC_RELAXED, __HIP_MEMORY_SCOPE_AGENT); }
            __syncthreads();
        } else if (KPOIS) {
#pragma unroll 1
            for (unsigned o = (unsigned)(h - NPAGE) * 8192u; o < (unsigned)SZ_RING; o += (NH - NPAGE) * 8192u) __builtin_amdgcn_raw_buffer_store_b128(PZ, rsP, tid * 16, o, AUX_SC1);
            VMWAIT(0);
            if (wid == 0) hi.wait(hi.peek());
            __syncthreads();
            if (tid < 16) __hip_atomic_fetch_add(tmo + W_CAL + lane * 32, 0x10000u, __ATOMIC_RELAXED, __HIP_MEMORY_SCOPE_AGENT);
        }
        GST(1);
        {
            const int row0 = (h * 8 + wid) * 8;
            const bf16x8 gv = *(const bf16x8*)(gw + lane * 8);
            float gl = 0.f;
#pragma unroll
            for (int r = 0; r < 8; ++r) {
                const bf16x8 xv = *(const bf16x8*)(x + (size_t)(row0 + r) * DM + lane * 8);
                float sg = 0.f;
#pragma unroll
                for (int j = 0; j < 8; ++j) sg += rbf(bf2f((unsigned short)xv[j]) * bf2f((unsigned short)gv[j]));
#pragma unroll
                for (int o = 32; o >= 1; o >>= 1) sg += __shfl_xor(sg, o);
                if (lane == r) gl = sg;
            }
            gl = rbf(gl);
            gl = rbf(1.f / (1.f + __expf(-gl)));
            if (lane < 8) __builtin_amdgcn_raw_buffer_store_b32(__float_as_uint(gl), rsW, (unsigned)(OFF_G + (row0 + lane) * 4), 0, AUX_SC1);
        }
        constexpr unsigned LF = 0, LP = 1024, LV = LP + 16384, LA = LV + 16384, LB = LA + 65536;
        static_assert(LAG == 2, "slots");
        pv |= wave_wait<8>(tmo + W_CAL + (h & 15) * 32, (KPOIS ? NH : NPAGE) << 16, tmo, 0xB0002u) & 0x3FFFu;
        volatile LAS unsigned* const lf = (volatile LAS unsigned*)(size_t)(lbase + LF);
        if (tid < 2) lf[tid] = 0u;
        if (wid == 1) wave_wait<4>(tmo + W_VO, 32u, tmo, 0x50000u + h);
        VMWAIT(0);
        __syncthreads();
        GST(2);
        const unsigned rot = ROT ? (MIX == 2 ? h >> 1 : MIX ? h / 3 : h) : 0;
        unsigned lb3 = 0;
        if (wid < 4) {
            if (wid == 0) {
                const unsigned mo = lane & 6 ? 0x7FFFFFFFu : h * 256 + (l16 >> 3) * 128 + (2 * g4 + (l16 & 1)) * 16;
                unsigned cap = SPIN_CAP >> 2, pa = (unsigned)__builtin_amdgcn_s_memrealtime() << 6;
#pragma unroll 1
                for (int s = 0; s < NSEG; ++s) {
                    const unsigned rg = (unsigned)(s & (NR - 1)) * SLOTS, vb = slot_off(rg + SL_VM + h, pv);
                    unsigned pb[4], vg[4], lx[4], vs[4];
#pragma unroll
                    for (int g = 0; g < 4; ++g) { const unsigned k = (g + rot) & 3u;
                        pb[g] = pin_s(slot_off(rg + SL_P + k, pv)); vg[g] = pin_s(vb + k * 2048);
                        lx[g] = pin_s(lbase + (k < 2 ? LA + (s & 3) * 16384 : LB + lb3 - 16384) + k * 8192); vs[g] = pin_s(lbase + LV + (s & 1) * 8192 + k * 2048); }
                    ST(0);
                    unsigned aw, tn, cnt; unsigned long long tp = 0;
                    asm volatile(ASM_CSTEP
                                 : [aw] "=&s"(aw), [tn] "=&s"(tn), [cnt] "=&s"(cnt), [cap] "+s"(cap)
                                 : [l16] "v"((unsigned)lane * 16u), [pm] "v"(g4 * 1280 + l16 * 16), [ix] "v"(sidx), [sel] "v"(lane & 1 ? 0x03020706u : 0x05040100u), [mo] "v"(mo),
                                   [rs] "s"(rsPu), [pb0] "s"(pb[0]), [pb1] "s"(pb[1]), [pb2] "s"(pb[2]), [pb3] "s"(pb[3]), [vb0] "s"(vg[0]), [vb1] "s"(vg[1]), [vb2] "s"(vg[2]), [vb3] "s"(vg[3]),
                                   [lx0] "s"(lx[0]), [lx1] "s"(lx[1]), [lx2] "s"(lx[2]), [lx3] "s"(lx[3]), [vs0] "s"(vs[0]), [vs1] "s"(vs[1]), [vs2] "s"(vs[2]), [vs3] "s"(vs[3]),
                                   [fl] "s"(lbase + LF), [tg] "s"(s + 1), [pa] "s"(pin_s(pa)), [ab] "s"(pin_s(anchor_off(s))), [ax] "s"(AX << 6), [ap] "s"(AP << 6),
                                   [on] "s"(pin_s(NSEG - 1 - s)), [mb] "s"(pin_s(slot_off((unsigned)((s + 1) & (NR - 1)) * SLOTS + SL_MEM, pv)))
                                 : "memory", "vcc", "scc", "s93", "s94", "s95", CL8(8, 9, 10, 11, 12, 13, 14, 15), CL16_63, CL64_127, CL128_159, CL160_191, CL192_223, CL224_251);
                    pa = pin_s((aw & ~63u) + ((AM + AA) << 6));
                    if (__builtin_expect(cap == 0, 0)) __hip_atomic_store(tmo, 0x20000u + h, __ATOMIC_RELAXED, __HIP_MEMORY_SCOPE_AGENT);
                    if (s == 0) PRB(4);
                    if (s == 10) PRB(10);
                    lb3 = lb3 == 32768u ? 0u : lb3 + 16384u;
                }
            } else if (wid == 1) {
                const u32x4 rsX = mk_rs(ws + OFF_VOX, (unsigned)(4 * SZ_ACT));
                auto half = [&](int sg, unsigned hf, unsigned la) { dma16k(rsX, (unsigned)lane * 16u, ((unsigned)sg << 20) + (h * 32 + hf * 16) * 1024, lbase + la); };
                bool early = false;
#pragma unroll 1
                for (int s = 0; s < NSEG; ++s) {
                    const unsigned tf = DURT();
                    if (XDT) until((unsigned)__builtin_amdgcn_s_memtime(), XDT);
                    if (!early) half(s, 0, LA + (s & 3) * 16384);
                    half(s, 1, LB + lb3);
                    unsigned* const fw = tmo + W_VO + 32 * min(s + 1, NSEG - 1);
                    early = EARLY && __builtin_amdgcn_readfirstlane(__hip_atomic_load(fw, __ATOMIC_RELAXED, __HIP_MEMORY_SCOPE_AGENT)) >= 32u && s + 1 < NSEG;
                    VMWAIT(0);
                    lf[0] = s + 1;
                    DUR(3, DURT() - tf);
                    if (early) {
                        for (int n = 0; n < (1 << 20) && __builtin_amdgcn_readfirstlane(lf[1]) != (unsigned)s + 1u; ++n) __builtin_amdgcn_s_sleep(1);
                        half(s + 1, 0, LA + ((s + 1) & 3) * 16384);
                    } else wave_wait<4>(fw, 32u, tmo, 0x50000u + (unsigned)(s << 8) + h);
                    LDS_BARRIER();
                    lb3 = lb3 == 32768u ? 0u : lb3 + 16384u;
                }
            }
            static_assert(SPLIT <= NSEG - 2 && FSP <= SPLIT && FSP + 4 < NSEG, "tail");
            int lane;
            asm volatile("v_mbcnt_lo_u32_b32 %0, -1, 0\n\tv_mbcnt_hi_u32_b32 %0, -1, %0" : "=v"(lane));
            f32x4 qs = f32x4{}; float g = 0.f;
            auto branch_a = [&](int tau) {
                wave_wait<4>(tmo + W_FA + 32 * tau, 20u, tmo, 0x55000u + h);
                f32x4 qa[5];
#pragma unroll
                for (int k = 0; k < 5; ++k) qa[k] = __builtin_bit_cast(f32x4, __builtin_amdgcn_raw_buffer_load_b128(rsW, (unsigned)OFF_PART + ((tau * 32 + h) * 2 + (wid & 1)) * 1024 + lane * 16, (unsigned)(k * SEQ * DM * 4), AUX_SC1));
                g = __uint_as_float(__builtin_amdgcn_raw_buffer_load_b32(rsW, (unsigned)(OFF_G + (tau * SEGL + (wid & 1) * 16 + (lane & 15)) * 4), 0, AUX_SC1));
                qs = ((qa[0] + qa[1]) + (qa[2] + qa[3])) + qa[4]; };
            auto finish = [&](int tau) {
                const char* p = lds + LP + (tau & 1) * 8192 + (wid & 1) * 1024 + lane * 16;
                const f32x4 bs = (*(const f32x4*)p + *(const f32x4*)(p + 2048)) + (*(const f32x4*)(p + 4096) + *(const f32x4*)(p + 6144));
                if (tau < SPLIT) __builtin_amdgcn_raw_buffer_store_b64(u32x2{cvtpk(bs[0], bs[1]), cvtpk(bs[2], bs[3])}, rsW, (unsigned)OFF_BB + ((tau * 32 + h) * 2 + (wid & 1)) * 512 + lane * 8, 0, AUX_SC1);
                else *(u32x2*)(out + (size_t)(tau * SEGL + (wid & 1) * 16 + (lane & 15)) * DM + h * 16 + 4 * (lane >> 4)) = mix4(qs, f32x4{rbf(bs[0]), rbf(bs[1]), rbf(bs[2]), rbf(bs[3])}, g); };
            if (wid < 2) {
                branch_a(NSEG - 1);
                LDS_BARRIER(); LDS_BARRIER();
                finish(NSEG - 1);
            } else {
#pragma unroll 1
                for (int s = 0; s < NSEG + LAG; ++s) {
                    const int tau = s - LAG - 1;
                    const unsigned tf = DURT();
                    if (HDT) until((unsigned)__builtin_amdgcn_s_memtime(), HDT);
                    if (s > 0 && s + NR <= NSEG) {
                        const unsigned rg = (unsigned)((s - 1) & (NR - 1)) * SLOTS;
                        const unsigned o = slot_off(rg + SL_VM + h, pv) + (wid & 1) * 4096;
#pragma unroll
                        for (int i = 0; i < 4; ++i) __builtin_amdgcn_raw_buffer_store_b128(PZ, rsP, lane * 16 + i * 1024, o, AUX_SC1);
                        if (wid == 3 && s > 1 && lane < 16) __builtin_amdgcn_raw_buffer_store_b128(PZ, rsP, h * 256 + lane * 16, slot_off(rg + SL_MEM, pv), AUX_SC1);
                    }
                    if (tau >= 0) finish(tau);
                    if (wid == 2 && lane == 0 && tau > FSP && tau <= SPLIT) cnt_add(tmo + W_FA + 32 * (tau - 1));
                    if (tau + 1 >= SPLIT && tau + 2 < NSEG) branch_a(tau + 1);
                    if (tau < SPLIT) VMWAIT(0);
                    if (wid == 3 && s < NSEG) DUR(0, DURT() - tf);
                    LDS_BARRIER();
                }
            }
        } else {
            const unsigned po = (unsigned)(OFF_PR + (wid - 4) * 16384 + lane * 16);
            auto fr = [&](int i) { return i < 8 ? (i + rot) & 7u : i < 12 ? (i + rot) & 3u : (i + rot) & 1u; };
            LDS_BARRIER(); LDS_BARRIER();
#pragma unroll 1
            for (int sg = 0; sg < NSEG; ++sg) {
                const int s = sg + LAG;
                const unsigned tf = DURT();
                if (s >= NSEG) wave_wait<2>(tmo + W_RDY + 32 * sg, 64u, tmo, 0x53000u + (unsigned)(s << 8) + h);
                else if (PDL) until((unsigned)__builtin_amdgcn_s_memtime(), PDL);
                u32x4 pf[16];
#pragma unroll
                for (int i = 0; i < 16; ++i) pf[i] = __builtin_amdgcn_raw_buffer_load_b128(rsW, po, (unsigned)(sg * 65536) + ((i < 8 ? 0 : i < 12 ? 8 : i & 14) + fr(i)) * 1024, AUX_SC1);
                __builtin_amdgcn_sched_barrier(0);
                const char* gp = lds + (wid < 6 ? LA + (sg & 3) * 16384 : LB + lb3) + (wid & 1) * 8192 + lane * 16, * gv = lds + LV + (sg & 1) * 8192 + (wid - 4) * 2048 + lane * 16;
                f32x4 a0 = f32x4{}, a1 = f32x4{};
#pragma unroll
                for (int i = 0; i < 8; ++i) a1 = mfma32(*(const u32x4*)(gp + fr(i) * 1024), pf[i], a1);
#pragma unroll
                for (int i = 8; i < 12; ++i) a0 = mfma32(*(const u32x4*)(gp + fr(i) * 2048 + (g4 >> 1) * 512), pf[i], a0);
#pragma unroll
                for (int i = 12; i < 14; ++i) { const u32x4 vf = *(const u32x4*)(gv + fr(i) * 1024); a0 = mfma32(vf, pf[i], a0); a1 = mfma32(vf, pf[i + 2], a1); }
                char* p = lds + LP + (sg & 1) * 8192 + (wid - 4) * 2048 + lane * 16;
                *(f32x4*)p = a0; *(f32x4*)(p + 1024) = a1;
                lb3 = lb3 == 32768u ? 0u : lb3 + 16384u;
                if (wid == 7 && s < NSEG) DUR(1, DURT() - tf);
                ST(5);
                LDS_BARRIER();
            }
        }
        DUROUT(1);
        GST(3);
        PRB(7);
    } else {
        int tid = threadIdx.x; asm volatile("" : "+v"(tid));
        const int lane = tid & 63, l16 = lane & 15, g4 = lane >> 4, sidx = l16 < 8 ? 0x4444 : 0xEEEE;
        const int h = jb;
        const unsigned q2 = l16 >> 2, p2 = lane & 3;
        constexpr unsigned LM = 0, LI = 8192;
        u32x8 w[16];
        if (wid >= 4) stream_w(Wv + h * DH, HD * 2, [&](int i, int p) { return (unsigned)((64 * i + 16 * p) * (HD * 2)); }, [&](int i, const char* p) { tiles_w(w, i, p, wid, lane); }, lds, wid, lane);
        else {
            unsigned long long hv = 0;
            stream_w(Wo + h * DH * DM, DM * 2, [&](int i, int p) { return (unsigned)((64 * (p >> 1) + 32 * (p & 1) + 16 * (i & 1)) * (DM * 2) + (i >> 1) * 256); }, [&](int i, const char* p) {
                         if (wid == (i >> 1)) {
#pragma unroll
                             for (int k = 0; k < 8; ++k) { const char* img = p + (k & 1) * 8192 + 8 * (p2 & 1);
                                 const unsigned r0 = 4 * g4 + q2, c0 = 4 * (k >> 1) + (p2 >> 1);
                                 w[4 * (k >> 1) + 2 * (i & 1) + (k & 1)] = ilv(tr_rdu(img + off_b(r0, c0)), tr_rdu(img + off_b(r0, c0 + 2)), tr_rdu(img + off_b(r0 + 16, c0)), tr_rdu(img + off_b(r0 + 16, c0 + 2))); } }
                         if (wid == 0 && i == 4) hv = hi.peek(); }, lds, wid, lane);
            if (wid == 0) hi.wait(hv);
            asm volatile("" ::: "memory"); }
        __syncthreads();
        const unsigned ma = lbase + (4 * (l16 >> 3) + g4) * 128 + (l16 & 7) * 16;
        *(u32x4*)(lds + LM + tid * 16) = *(const u32x4*)(mem0 + (tid & 7) * DM + (tid >> 3) * 8);
        GST(1);
        pv |= wave_wait<8>(tmo + W_CAL + (h & 15) * 32, (KPOIS ? NH : NPAGE) << 16, tmo, 0xB0001u) & 0x3FFFu;
        GST(2);
        unsigned fb = 0, cap = SPIN_CAP >> 2;
        const unsigned vw = lbase + LI + img_place(wid, l16) * 16 + (2 * (g4 & 1) + (g4 >> 1)) * 4, fr = lbase + LI + (4 * g4 + q2) * 16 + (p2 & 1) * 8;
        if (wid >= 4) __builtin_amdgcn_s_setprio(1);
        const u32x4 rsWu = mk_rs(ws, (unsigned)WS_END);
        auto prefetch = [&](int s) {
                const unsigned pc = (unsigned)(h / 3 * 8 + 2 * wid), la = lbase + 16384 + wid * 1024;
                if (s + 1 >= LAG && pc < 64u) {
                    dma16<true>(rsWu, (unsigned)lane * 16u, (unsigned)OFF_PR + (unsigned)(s + 1 - LAG) * 65536u + pc * 1024u, la);
                    dma16<true>(rsWu, (unsigned)lane * 16u, (unsigned)OFF_PR + (unsigned)(s + 1 - LAG) * 65536u + pc * 1024u + 1024u, la); }
            };
#pragma unroll 1
        for (int s = 0; s < NSEG; ++s) {
            const unsigned rg = (unsigned)(s & (NR - 1)) * SLOTS;
            ST(0);
            unsigned cnt;
#define V_OPS W16(w), [ix] "v"(sidx), [ma] "v"(ma), [s] "s"(s)
            if (wid < 4) {
                const unsigned vb = 8 * wid + 2 * (g4 & 1) + (g4 >> 1), vo = h * 256 + l16 * 16;
                asm volatile(ASM_VO : H_OUT : V_OPS, [fr] "v"(fr), [rs] "s"(rsPu), [so0] "v"(slot_off(rg + SL_VM + vb, pv) + vo), [so1] "v"(slot_off(rg + SL_VM + vb + 4, pv) + vo)
                             : H_CLOB, CL8(176, 177, 178, 179, 180, 181, 182, 183), CL8(184, 185, 186, 187, 188, 189, 190, 191), CL192_223);
                ST(4);
                if (PF & 1) prefetch(s);
            } else if (wid < 7) asm volatile(ASM_VV : H_OUT : V_OPS, [qw] "v"(vw) : H_CLOB);
            else {
                unsigned keep, f2;
                asm volatile(ASM_VP : H_OUT, [keep] "=&s"(keep), [f2] "=&s"(f2)
                             : V_OPS, [qw] "v"(vw), [o] "v"((unsigned)lane * 16u), [rs] "s"(rsPu), [fb] "s"(fb), [lm] "s"(lbase + LM) : H_CLOB);
                SP(12, cnt);
                if (__builtin_expect(cap == 0, 0)) __hip_atomic_store(tmo, 0x90000u + h, __ATOMIC_RELAXED, __HIP_MEMORY_SCOPE_AGENT);
                const unsigned t0 = (unsigned)__builtin_amdgcn_s_memtime();
                fb = pin_s(slot_off((unsigned)((s + 1) & (NR - 1)) * SLOTS + SL_MEM, pv));
                until(t0, VAD);
                until_a(anchor(s, 4), s, AV, t0, VPD);
            }
        }
        VMWAIT(0);
        GST(3);
    }
    GST(5);
}

extern "C" void kernel_launch(void* const* d_in, const int* in_sizes, int n_in,
                              void* d_out, int out_size, void* d_ws, size_t ws_size,
                              hipStream_t stream) {
    (void)in_sizes; (void)n_in; (void)out_size; (void)ws_size;
    const bf16* x = (const bf16*)d_in[0];
    const bf16* ckv = (const bf16*)d_in[1];
    const bf16* a_wq = (const bf16*)d_in[2];
    const bf16* a_wuk = (const bf16*)d_in[3];
    const bf16* a_wuv = (const bf16*)d_in[4];
    const bf16* a_wo = (const bf16*)d_in[5];
    const bf16* mem0 = (const bf16*)d_in[7];
    const bf16* bWq = (const bf16*)d_in[8];
    const bf16* bWk = (const bf16*)d_in[9];
    const bf16* bWv = (const bf16*)d_in[10];
    const bf16* bWo = (const bf16*)d_in[11];
    const bf16* gw = (const bf16*)d_in[13];
    char* ws = (char*)d_ws;
    bf16* out = (bf16*)d_out;

    static bool attr = false;
    if (!attr) {
        attr = true;
        (void)hipFuncSetAttribute((const void*)k_chain, hipFuncAttributeMaxDynamicSharedMemorySize, CHAIN_LDS);
    }
    if (!KPOIS) (void)hipMemsetAsync(ws, 0xFF, SZ_POISON, stream);
    k_chain<<<256, 512, CHAIN_LDS, stream>>>(mem0, bWq, bWk, bWv, bWo, x, ws, ckv, a_wq, a_wuk, a_wuv, a_wo, gw, out);
}
```
